```python
import torch

def _softmax(x, axis=-1):
    return torch.softmax(x, dim=axis)
def _gelu(x):
    return torch.nn.functional.gelu(x, approximate="tanh")
def _silu(x):
    return torch.nn.functional.silu(x)
def _sigmoid(x):
    return torch.sigmoid(x)
def _layernorm(x, axis=-1, eps=1e-5):
    m = x.mean(dim=axis, keepdim=True)
    v = x.var(dim=axis, keepdim=True, unbiased=False)
    return (x - m) / torch.sqrt(v + eps)
def _scatter_add(tgt, idx, src, axis=0):
    return tgt.clone().index_add_(axis, idx, src)
def _segment_sum(src, seg, n):
    out = torch.zeros((n,) + src.shape[1:], dtype=src.dtype)
    return out.index_add_(0, seg, src)
def _pad(x, pw):
    flat = tuple(v for pair in reversed(pw) for v in pair)
    return torch.nn.functional.pad(x, flat)

kernel_name = 'sequential_preLN__sliding_block_cached__glu_gated_glu__decode'

def setup_inputs():

    _w = (lambda a: (lambda t: t.to(torch.bfloat16) if t.is_floating_point() else t)(torch.as_tensor(a)))
    def _A_setup():
        B, Sq, D = 4, 1, 512
        Hq, Hkv, Sk, Dh = 32, 8, 8192, 128
        assert Hq % Hkv == 0, (Hq, Hkv)
        return {
            "x": _w((lambda *s: torch.randn(*s, dtype=torch.bfloat16) * 0.05)(B, Sq, D)),
            "k_cache": _w((lambda *s: torch.randn(*s, dtype=torch.bfloat16) * 3.2)(B, Hkv, Sk, Dh)),
            "v_cache": _w((lambda *s: torch.randn(*s, dtype=torch.bfloat16) * 0.4)(B, Hkv, Sk, Dh)),
            "wq": _w((lambda *s: torch.randn(*s, dtype=torch.bfloat16) * 3.2)(D, Hq * Dh) * (D ** -0.5)),
            "wo": _w((lambda *s: torch.randn(*s, dtype=torch.bfloat16) * 0.4)(Hq * Dh, D) * ((Hq * Dh) ** -0.5)),
        }
    _A_in = _A_setup()

    def _B_setup():
        B, S, D, F = 4, 1, 512, 1792
        return {
            "x": _w((lambda *s: torch.randn(*s, dtype=torch.bfloat16) * 0.05)(B, S, D)),
            "w_gate": _w((lambda *s: torch.randn(*s, dtype=torch.bfloat16) * 0.1)(D, F)),
            "w_up": _w((lambda *s: torch.randn(*s, dtype=torch.bfloat16) * 0.05)(D, F)),
            "w_down": _w((lambda *s: torch.randn(*s, dtype=torch.bfloat16) * 0.05)(F, D)),
        }
    _B_in = _B_setup()

    return {
        "A_x": _A_in["x"],
        "A_k_cache": _A_in["k_cache"],
        "A_v_cache": _A_in["v_cache"],
        "A_wq": _A_in["wq"],
        "A_wo": _A_in["wo"],
        "B_x": _B_in["x"],
        "B_w_gate": _B_in["w_gate"],
        "B_w_up": _B_in["w_up"],
        "B_w_down": _B_in["w_down"],
    }

def reference(A_x, A_k_cache, A_v_cache, A_wq, A_wo, B_x, B_w_gate, B_w_up, B_w_down):
    def _A_ref(x, k_cache, v_cache, wq, wo):
        B, Sq, D = x.shape
        Hq, Dh = 32, 128
        Hkv, Sk = k_cache.shape[1], k_cache.shape[2]
        W = min(4096, max(256, Sk // 4))
        rep = Hq // Hkv
        q = (lambda x, *d: x.permute(*d))((x @ wq).reshape(B, Sq, Hq, Dh), 0, 2, 1, 3)
        k = (lambda x, n, axis: torch.repeat_interleave(x, n, dim=axis))(k_cache[:, :, Sk - W:, :], rep, axis=1)
        v = (lambda x, n, axis: torch.repeat_interleave(x, n, dim=axis))(v_cache[:, :, Sk - W:, :], rep, axis=1)
        scores = torch.einsum("bhqd,bhkd->bhqk", q, k) * (Dh ** -0.5)
        p = _softmax(scores, axis=-1)
        o = torch.einsum("bhqk,bhkd->bhqd", p, v)
        return (lambda x, *d: x.permute(*d))(o, 0, 2, 1, 3).reshape(B, Sq, Hq * Dh) @ wo

    def _B_ref(x, w_gate, w_up, w_down):
        g = torch.einsum("bsd,df->bsf", x, w_gate)
        u = torch.einsum("bsd,df->bsf", x, w_up)
        h = _sigmoid(g) * u
        return torch.einsum("bsf,fd->bsd", h, w_down)

    x = A_x
    h = x + _A_ref(_layernorm(x, axis=-1), A_k_cache, A_v_cache, A_wq, A_wo)
    h2 = h + _B_ref(_layernorm(h, axis=-1), B_w_gate, B_w_up, B_w_down)
    return h2
```

```cpp
#include <hip/hip_runtime.h>
#include <hip/hip_bf16.h>
#include <stdint.h>

typedef unsigned short u16;
typedef unsigned v4u __attribute__((ext_vector_type(4)));

constexpr int B = 4, D = 512, HQ = 32, HKV = 8, SK = 8192, DH = 128, W = 2048, F = 1792;
constexpr int NSPLIT = 8, KPS = W / NSPLIT;

__device__ __forceinline__ float bf2f(u16 b) { return __builtin_bit_cast(float, (unsigned)b << 16); }
__device__ __forceinline__ float rbf(float f) {
  __hip_bfloat16 h = __float2bfloat16(f); return __bfloat162float(h);
}
__device__ __forceinline__ u16 f2bf(float f) { __hip_bfloat16 h = __float2bfloat16(f); return __builtin_bit_cast(u16, h); }

#define MIMIC 1
__device__ __forceinline__ float R(float f) { return MIMIC ? rbf(f) : f; }

__device__ __forceinline__ void ln_row(const float* v, float* o) {
  float s = 0;
  for (int i = 0; i < 8; ++i) s += v[i];
  for (int m = 32; m >= 1; m >>= 1) s += __shfl_xor(s, m);
  float mean = s * (1.0f / D);
  float q = 0;
  for (int i = 0; i < 8; ++i) { float d = v[i] - mean; q += d * d; }
  for (int m = 32; m >= 1; m >>= 1) q += __shfl_xor(q, m);
  float var = R(q * (1.0f / D));
  float mb = R(mean);
  float den = R(sqrtf(R(var + 1e-5f)));
  for (int i = 0; i < 8; ++i) o[i] = R(R(v[i] - mb) / den);
}

__global__ void k_q(const u16* x, const u16* wq, float* q) {
  __shared__ float xn[B][D];
  __shared__ float red[4][B][64];
  int tid = threadIdx.x, lane = tid & 63, w = tid >> 6;
  { float v[8], o[8];
    for (int i = 0; i < 8; ++i) v[i] = bf2f(x[w * D + lane * 8 + i]);
    ln_row(v, o);
    for (int i = 0; i < 8; ++i) xn[w][lane * 8 + i] = o[i]; }
  __syncthreads();
  int col = blockIdx.x * 64 + lane;
  float acc[B] = {0, 0, 0, 0};
  for (int k = w * 128; k < w * 128 + 128; ++k) {
    float wv = bf2f(wq[(size_t)k * (HQ * DH) + col]);
    for (int b = 0; b < B; ++b) acc[b] += xn[b][k] * wv;
  }
  for (int b = 0; b < B; ++b) red[w][b][lane] = acc[b];
  __syncthreads();
  float s = red[0][w][lane] + red[1][w][lane] + red[2][w][lane] + red[3][w][lane];
  q[w * (HQ * DH) + col] = R(s);
}

__global__ void k_attn(const float* q, const u16* kc, const u16* vc, float* pm, float* pl, float* po) {
  __shared__ float qs[4][DH];
  __shared__ float ps[4][KPS];
  __shared__ float rm[4][4], rl[4][4];
  int s = blockIdx.x, kvh = blockIdx.y, b = blockIdx.z, tid = threadIdx.x, lane = tid & 63, w = tid >> 6;
  for (int i = tid; i < 4 * DH; i += 256) qs[i / DH][i % DH] = q[b * (HQ * DH) + kvh * 4 * DH + i];
  __syncthreads();
  size_t base = ((size_t)(b * HKV + kvh) * SK + (SK - W) + s * KPS) * DH;
  const u16* kr = kc + base + (size_t)tid * DH;
  float sc[4] = {0, 0, 0, 0};
  for (int d = 0; d < DH; ++d) { float kv = bf2f(kr[d]); for (int g = 0; g < 4; ++g) sc[g] += qs[g][d] * kv; }
  float mx[4];
  for (int g = 0; g < 4; ++g) {
    sc[g] = R(R(sc[g]) * 0.08838834764831845f);
    float m = sc[g];
    for (int o = 32; o >= 1; o >>= 1) m = fmaxf(m, __shfl_xor(m, o));
    if (lane == 0) rm[w][g] = m;
  }
  __syncthreads();
  for (int g = 0; g < 4; ++g) {
    mx[g] = fmaxf(fmaxf(rm[0][g], rm[1][g]), fmaxf(rm[2][g], rm[3][g]));
    float p = __expf(sc[g] - mx[g]);
    ps[g][tid] = p;
    float l = p;
    for (int o = 32; o >= 1; o >>= 1) l += __shfl_xor(l, o);
    if (lane == 0) rl[w][g] = l;
  }
  __syncthreads();
  int g = w;
  float a0 = 0, a1 = 0;
  for (int k = 0; k < KPS; ++k) {
    unsigned vv = *(const unsigned*)(vc + base + (size_t)k * DH + 2 * lane);
    float p = ps[g][k];
    a0 += p * bf2f(vv & 0xffff); a1 += p * bf2f(vv >> 16);
  }
  int row = (b * HQ + kvh * 4 + g) * NSPLIT + s;
  po[(size_t)row * DH + 2 * lane] = a0; po[(size_t)row * DH + 2 * lane + 1] = a1;
  if (lane == 0) { pm[row] = mx[g]; pl[row] = rl[0][g] + rl[1][g] + rl[2][g] + rl[3][g]; }
}

__global__ void k_comb(const float* pm, const float* pl, const float* po, float* o) {
  int row = blockIdx.x; int d = threadIdx.x;
  float M = -INFINITY;
  for (int s = 0; s < NSPLIT; ++s) M = fmaxf(M, pm[row * NSPLIT + s]);
  float L = 0, a = 0;
  for (int s = 0; s < NSPLIT; ++s) { float wt = __expf(pm[row * NSPLIT + s] - M); L += wt * pl[row * NSPLIT + s]; a += wt * po[(size_t)(row * NSPLIT + s) * DH + d]; }
  o[row * DH + d] = R(a / L);
}

__global__ void k_wo(const float* o, const u16* wo, const u16* x, float* h) {
  __shared__ float red[16][B][64];
  int tid = threadIdx.x, lane = tid & 63, w = tid >> 6;
  int col = blockIdx.x * 64 + lane;
  float acc[B] = {0, 0, 0, 0};
  for (int k = w * 256; k < w * 256 + 256; ++k) {
    float wv = bf2f(wo[(size_t)k * D + col]);
    for (int b = 0; b < B; ++b) acc[b] += o[b * 4096 + k] * wv;
  }
  for (int b = 0; b < B; ++b) red[w][b][lane] = acc[b];
  __syncthreads();
  if (w < B) { float s = 0; for (int i = 0; i < 16; ++i) s += red[i][w][lane];
    h[w * D + col] = R(bf2f(x[w * D + col]) + R(s)); }
}

__global__ void k_gu(const float* h, const u16* wg, const u16* wu, float* hh) {
  __shared__ float xn[B][D];
  __shared__ float red[4][2][B][64];
  int tid = threadIdx.x, lane = tid & 63, w = tid >> 6;
  { float v[8], o[8];
    for (int i = 0; i < 8; ++i) v[i] = h[w * D + lane * 8 + i];
    ln_row(v, o);
    for (int i = 0; i < 8; ++i) xn[w][lane * 8 + i] = o[i]; }
  __syncthreads();
  int col = blockIdx.x * 64 + lane;
  float ag[B] = {0, 0, 0, 0}, au[B] = {0, 0, 0, 0};
  for (int k = w * 128; k < w * 128 + 128; ++k) {
    float g = bf2f(wg[(size_t)k * F + col]), u = bf2f(wu[(size_t)k * F + col]);
    for (int b = 0; b < B; ++b) { ag[b] += xn[b][k] * g; au[b] += xn[b][k] * u; }
  }
  for (int b = 0; b < B; ++b) { red[w][0][b][lane] = ag[b]; red[w][1][b][lane] = au[b]; }
  __syncthreads();
  float g = R(red[0][0][w][lane] + red[1][0][w][lane] + red[2][0][w][lane] + red[3][0][w][lane]);
  float u = R(red[0][1][w][lane] + red[1][1][w][lane] + red[2][1][w][lane] + red[3][1][w][lane]);
  float sg = R(1.0f / (1.0f + __expf(-g)));
  hh[w * F + col] = R(sg * u);
}

__global__ void k_dn(const float* hh, const u16* wd, const float* h, u16* out) {
  __shared__ float red[16][B][64];
  int tid = threadIdx.x, lane = tid & 63, w = tid >> 6;
  int col = blockIdx.x * 64 + lane;
  float acc[B] = {0, 0, 0, 0};
  for (int k = w * 112; k < w * 112 + 112; ++k) {
    float wv = bf2f(wd[(size_t)k * D + col]);
    for (int b = 0; b < B; ++b) acc[b] += hh[b * F + k] * wv;
  }
  for (int b = 0; b < B; ++b) red[w][b][lane] = acc[b];
  __syncthreads();
  if (w < B) { float s = 0; for (int i = 0; i < 16; ++i) s += red[i][w][lane];
    out[w * D + col] = f2bf(h[w * D + col] + R(s)); }
}

extern "C" void kernel_launch(void* const* d_in, const int* in_sizes, int n_in, void* d_out, int out_size, void* d_ws, size_t ws_size, hipStream_t stream) {
  const u16* x = (const u16*)d_in[0]; const u16* kc = (const u16*)d_in[1]; const u16* vc = (const u16*)d_in[2];
  const u16* wq = (const u16*)d_in[3]; const u16* wo = (const u16*)d_in[4];
  const u16* wg = (const u16*)d_in[6]; const u16* wu = (const u16*)d_in[7]; const u16* wd = (const u16*)d_in[8];
  float* ws = (float*)d_ws;
  float* q = ws; float* pm = q + B * 4096; float* pl = pm + B * HQ * NSPLIT; float* po = pl + B * HQ * NSPLIT;
  float* o = po + B * HQ * NSPLIT * DH; float* h = o + B * 4096; float* hh = h + B * D;
  k_q<<<64, 256, 0, stream>>>(x, wq, q);
  k_attn<<<dim3(NSPLIT, HKV, B), 256, 0, stream>>>(q, kc, vc, pm, pl, po);
  k_comb<<<B * HQ, 128, 0, stream>>>(pm, pl, po, o);
  k_wo<<<8, 1024, 0, stream>>>(o, wo, x, h);
  k_gu<<<28, 256, 0, stream>>>(h, wg, wu, hh);
  k_dn<<<8, 1024, 0, stream>>>(hh, wd, h, (u16*)d_out);
}
```

```cpp
#include <hip/hip_runtime.h>
#include <hip/hip_bf16.h>
#include <stdint.h>

typedef unsigned short u16;
typedef unsigned v4u __attribute__((ext_vector_type(4)));

constexpr int B = 4, D = 512, HQ = 32, HKV = 8, SK = 8192, DH = 128, W = 2048, F = 1792;
constexpr int NSPLIT = 8, KPS = W / NSPLIT;

__device__ __forceinline__ float bf2f(u16 b) { return __builtin_bit_cast(float, (unsigned)b << 16); }
__device__ __forceinline__ float rbf(float f) {
  __hip_bfloat16 h = __float2bfloat16(f); return __bfloat162float(h);
}
__device__ __forceinline__ u16 f2bf(float f) { __hip_bfloat16 h = __float2bfloat16(f); return __builtin_bit_cast(u16, h); }

#define MIMIC 1
__device__ __forceinline__ float R(float f) { return MIMIC ? rbf(f) : f; }

__device__ __forceinline__ void ln_row(const float* v, float* o) {
  float s = 0;
  for (int i = 0; i < 8; ++i) s += v[i];
  for (int m = 32; m >= 1; m >>= 1) s += __shfl_xor(s, m);
  float mean = s * (1.0f / D);
  float q = 0;
  for (int i = 0; i < 8; ++i) { float d = v[i] - mean; q += d * d; }
  for (int m = 32; m >= 1; m >>= 1) q += __shfl_xor(q, m);
  float var = R(q * (1.0f / D));
  float mb = R(mean);
  float den = R(sqrtf(R(var + 1e-5f)));
  for (int i = 0; i < 8; ++i) o[i] = R(R(v[i] - mb) / den);
}

__global__ void k_q(const u16* x, const u16* wq, float* q) {
  __shared__ float xn[B][D];
  __shared__ float red[4][B][64];
  int tid = threadIdx.x, lane = tid & 63, w = tid >> 6;
  { float v[8], o[8];
    for (int i = 0; i < 8; ++i) v[i] = bf2f(x[w * D + lane * 8 + i]);
    ln_row(v, o);
    for (int i = 0; i < 8; ++i) xn[w][lane * 8 + i] = o[i]; }
  __syncthreads();
  int col = blockIdx.x * 64 + lane;
  float acc[B] = {0, 0, 0, 0};
  for (int k = w * 128; k < w * 128 + 128; ++k) {
    float wv = bf2f(wq[(size_t)k * (HQ * DH) + col]);
    for (int b = 0; b < B; ++b) acc[b] += xn[b][k] * wv;
  }
  for (int b = 0; b < B; ++b) red[w][b][lane] = acc[b];
  __syncthreads();
  float s = red[0][w][lane] + red[1][w][lane] + red[2][w][lane] + red[3][w][lane];
  q[w * (HQ * DH) + col] = R(s);
}

#define KVNT 1
typedef float f32x4 __attribute__((ext_vector_type(4)));
typedef short bf16x8 __attribute__((ext_vector_type(8)));
__device__ __forceinline__ v4u ldkv(const void* p) { if (KVNT) return __builtin_nontemporal_load((const v4u*)p); else return *(const v4u*)p; }
__device__ __forceinline__ f32x4 mfma16(v4u a, v4u b, f32x4 c) { return __builtin_amdgcn_mfma_f32_16x16x32_bf16(__builtin_bit_cast(bf16x8, a), __builtin_bit_cast(bf16x8, b), c, 0, 0, 0); }
template <int CTRL> __device__ __forceinline__ float dpp(float x) { return __builtin_bit_cast(float, __builtin_amdgcn_mov_dpp(__builtin_bit_cast(int, x), CTRL, 0xf, 0xf, true)); }
__device__ __forceinline__ float row16_max(float x) { x = fmaxf(x, dpp<0x128>(x)); x = fmaxf(x, dpp<0x124>(x)); x = fmaxf(x, dpp<0x122>(x)); x = fmaxf(x, dpp<0x121>(x)); return x; }
__device__ __forceinline__ float row16_sum(float x) { x += dpp<0x128>(x); x += dpp<0x124>(x); x += dpp<0x122>(x); x += dpp<0x121>(x); return x; }
__device__ __forceinline__ float x16_max(float x) { auto s = __builtin_amdgcn_permlane16_swap(__float_as_uint(x), __float_as_uint(x), false, false); return fmaxf(__uint_as_float(s[0]), __uint_as_float(s[1])); }
__device__ __forceinline__ float x16_sum(float x) { auto s = __builtin_amdgcn_permlane16_swap(__float_as_uint(x), __float_as_uint(x), false, false); return __uint_as_float(s[0]) + __uint_as_float(s[1]); }

__global__ void __launch_bounds__(512) k_attn(const float* q, const u16* kc, const u16* vc, float* pm, float* pl, float* po) {
  __shared__ __attribute__((aligned(16))) u16 qs[4 * DH];
  __shared__ __attribute__((aligned(16))) u16 ps[8][4 * 32];
  __shared__ __attribute__((aligned(16))) float rm[8][4], rl[8][4];
  __shared__ __attribute__((aligned(16))) float mrg[8][512];
  const int c = blockIdx.x, kvh = c & 7, b = c >> 6, s = (c >> 3) & 7;
  const int tid = threadIdx.x, lane = tid & 63, w = tid >> 6, n = lane & 15, g = lane >> 4;
  const int wr = (w + c * 5) & 7;
  size_t base = ((size_t)(b * HKV + kvh) * SK + (SK - W) + s * KPS + wr * 32) * DH;
  float qv = q[b * (HQ * DH) + kvh * 4 * DH + tid];
  v4u rk[8], rv[8];
#pragma unroll
  for (int t = 0; t < 2; ++t)
#pragma unroll
    for (int ks = 0; ks < 4; ++ks) rk[t * 4 + ks] = ldkv(kc + base + (size_t)(16 * t + n) * DH + 32 * ks + 8 * g);
#pragma unroll
  for (int i = 0; i < 8; ++i) rv[i] = ldkv(vc + base + (size_t)(4 * i + g) * DH + 8 * n);
  __builtin_amdgcn_sched_barrier(0);
  qs[tid] = f2bf(qv);
  __syncthreads();
  v4u qa[4];
#pragma unroll
  for (int ks = 0; ks < 4; ++ks) qa[ks] = *(const v4u*)(qs + (n & 3) * DH + 32 * ks + 8 * g);
  f32x4 d0 = {0, 0, 0, 0}, d1 = {0, 0, 0, 0};
#pragma unroll
  for (int ks = 0; ks < 4; ++ks) { d0 = mfma16(qa[ks], rk[ks], d0); d1 = mfma16(qa[ks], rk[4 + ks], d1); }
  float sc[4], mx[4];
#pragma unroll
  for (int h = 0; h < 4; ++h) {
    sc[h] = (g & 1) ? d1[h] : d0[h];
    sc[h] = R(R(sc[h]) * 0.08838834764831845f);
    mx[h] = x16_max(row16_max(sc[h]));
  }
  if (lane == 0) *(f32x4*)rm[w] = f32x4{mx[0], mx[1], mx[2], mx[3]};
  __syncthreads();
  f32x4 M = *(const f32x4*)rm[0];
#pragma unroll
  for (int i = 1; i < 8; ++i) { f32x4 t = *(const f32x4*)rm[i]; M[0] = fmaxf(M[0], t[0]); M[1] = fmaxf(M[1], t[1]); M[2] = fmaxf(M[2], t[2]); M[3] = fmaxf(M[3], t[3]); }
  float ls[4];
  const int key = lane & 31, pos = (key & 3) * 8 + (key >> 2);
#pragma unroll
  for (int h = 0; h < 4; ++h) {
    float p = __expf(sc[h] - M[h]);
    if (lane < 32) ps[w][h * 32 + pos] = f2bf(p);
    ls[h] = x16_sum(row16_sum(p));
  }
  if (lane == 0) *(f32x4*)rl[w] = f32x4{ls[0], ls[1], ls[2], ls[3]};
  __builtin_amdgcn_wave_barrier();
  asm volatile("s_waitcnt lgkmcnt(0)" ::: "memory");
  v4u pa = *(const v4u*)(&ps[w][(n & 3) * 32 + g * 8]);
  f32x4 acc[8];
#pragma unroll
  for (int jj = 0; jj < 8; ++jj) {
    v4u bb;
#pragma unroll
    for (int d = 0; d < 4; ++d) bb[d] = __builtin_amdgcn_perm(rv[2 * d + 1][jj >> 1], rv[2 * d][jj >> 1], (jj & 1) ? 0x07060302u : 0x05040100u);
    acc[jj] = mfma16(pa, bb, f32x4{0, 0, 0, 0});
  }
  {
    f32x4 a0 = g == 0 ? acc[0] : g == 1 ? acc[2] : g == 2 ? acc[4] : acc[6];
    f32x4 a1 = g == 0 ? acc[1] : g == 1 ? acc[3] : g == 2 ? acc[5] : acc[7];
    *(f32x4*)&mrg[w][(2 * g) * 64 + n * 4] = a0;
    *(f32x4*)&mrg[w][(2 * g + 1) * 64 + n * 4] = a1;
  }
  __syncthreads();
  float o = 0;
#pragma unroll
  for (int i = 0; i < 8; ++i) o += mrg[i][tid];
  const int jj = tid >> 6, cc = (tid >> 2) & 15, r = tid & 3;
  int row = (b * HQ + kvh * 4 + r) * NSPLIT + s;
  po[(size_t)row * DH + 8 * cc + jj] = o;
  if (tid < 4) { float L = 0; for (int i = 0; i < 8; ++i) L += rl[i][tid]; pm[row] = M[tid]; pl[row] = L; }
}

__global__ void k_comb(const float* pm, const float* pl, const float* po, float* o) {
  int row = blockIdx.x; int d = threadIdx.x;
  float M = -INFINITY;
  for (int s = 0; s < NSPLIT; ++s) M = fmaxf(M, pm[row * NSPLIT + s]);
  float L = 0, a = 0;
  for (int s = 0; s < NSPLIT; ++s) { float wt = __expf(pm[row * NSPLIT + s] - M); L += wt * pl[row * NSPLIT + s]; a += wt * po[(size_t)(row * NSPLIT + s) * DH + d]; }
  o[row * DH + d] = R(a / L);
}

__global__ void k_wo(const float* o, const u16* wo, const u16* x, float* h) {
  __shared__ float red[16][B][64];
  int tid = threadIdx.x, lane = tid & 63, w = tid >> 6;
  int col = blockIdx.x * 64 + lane;
  float acc[B] = {0, 0, 0, 0};
  for (int k = w * 256; k < w * 256 + 256; ++k) {
    float wv = bf2f(wo[(size_t)k * D + col]);
    for (int b = 0; b < B; ++b) acc[b] += o[b * 4096 + k] * wv;
  }
  for (int b = 0; b < B; ++b) red[w][b][lane] = acc[b];
  __syncthreads();
  if (w < B) { float s = 0; for (int i = 0; i < 16; ++i) s += red[i][w][lane];
    h[w * D + col] = R(bf2f(x[w * D + col]) + R(s)); }
}

__global__ void k_gu(const float* h, const u16* wg, const u16* wu, float* hh) {
  __shared__ float xn[B][D];
  __shared__ float red[4][2][B][64];
  int tid = threadIdx.x, lane = tid & 63, w = tid >> 6;
  { float v[8], o[8];
    for (int i = 0; i < 8; ++i) v[i] = h[w * D + lane * 8 + i];
    ln_row(v, o);
    for (int i = 0; i < 8; ++i) xn[w][lane * 8 + i] = o[i]; }
  __syncthreads();
  int col = blockIdx.x * 64 + lane;
  float ag[B] = {0, 0, 0, 0}, au[B] = {0, 0, 0, 0};
  for (int k = w * 128; k < w * 128 + 128; ++k) {
    float g = bf2f(wg[(size_t)k * F + col]), u = bf2f(wu[(size_t)k * F + col]);
    for (int b = 0; b < B; ++b) { ag[b] += xn[b][k] * g; au[b] += xn[b][k] * u; }
  }
  for (int b = 0; b < B; ++b) { red[w][0][b][lane] = ag[b]; red[w][1][b][lane] = au[b]; }
  __syncthreads();
  float g = R(red[0][0][w][lane] + red[1][0][w][lane] + red[2][0][w][lane] + red[3][0][w][lane]);
  float u = R(red[0][1][w][lane] + red[1][1][w][lane] + red[2][1][w][lane] + red[3][1][w][lane]);
  float sg = R(1.0f / (1.0f + __expf(-g)));
  hh[w * F + col] = R(sg * u);
}

__global__ void k_dn(const float* hh, const u16* wd, const float* h, u16* out) {
  __shared__ float red[16][B][64];
  int tid = threadIdx.x, lane = tid & 63, w = tid >> 6;
  int col = blockIdx.x * 64 + lane;
  float acc[B] = {0, 0, 0, 0};
  for (int k = w * 112; k < w * 112 + 112; ++k) {
    float wv = bf2f(wd[(size_t)k * D + col]);
    for (int b = 0; b < B; ++b) acc[b] += hh[b * F + k] * wv;
  }
  for (int b = 0; b < B; ++b) red[w][b][lane] = acc[b];
  __syncthreads();
  if (w < B) { float s = 0; for (int i = 0; i < 16; ++i) s += red[i][w][lane];
    out[w * D + col] = f2bf(h[w * D + col] + R(s)); }
}

extern "C" void kernel_launch(void* const* d_in, const int* in_sizes, int n_in, void* d_out, int out_size, void* d_ws, size_t ws_size, hipStream_t stream) {
  const u16* x = (const u16*)d_in[0]; const u16* kc = (const u16*)d_in[1]; const u16* vc = (const u16*)d_in[2];
  const u16* wq = (const u16*)d_in[3]; const u16* wo = (const u16*)d_in[4];
  const u16* wg = (const u16*)d_in[6]; const u16* wu = (const u16*)d_in[7]; const u16* wd = (const u16*)d_in[8];
  float* ws = (float*)d_ws;
  float* q = ws; float* pm = q + B * 4096; float* pl = pm + B * HQ * NSPLIT; float* po = pl + B * HQ * NSPLIT;
  float* o = po + B * HQ * NSPLIT * DH; float* h = o + B * 4096; float* hh = h + B * D;
  k_q<<<64, 256, 0, stream>>>(x, wq, q);
  k_attn<<<256, 512, 0, stream>>>(q, kc, vc, pm, pl, po);
  k_comb<<<B * HQ, 128, 0, stream>>>(pm, pl, po, o);
  k_wo<<<8, 1024, 0, stream>>>(o, wo, x, h);
  k_gu<<<28, 256, 0, stream>>>(h, wg, wu, hh);
  k_dn<<<8, 1024, 0, stream>>>(hh, wd, h, (u16*)d_out);
}
```

```cpp
#include <hip/hip_runtime.h>
#include <hip/hip_bf16.h>
#include <stdint.h>

namespace {

typedef unsigned short u16;
typedef unsigned long long u64;
typedef unsigned v4u __attribute__((ext_vector_type(4)));
typedef unsigned v2u __attribute__((ext_vector_type(2)));
typedef float f32x4 __attribute__((ext_vector_type(4)));
typedef float f32x2 __attribute__((ext_vector_type(2)));
typedef short bf16x8 __attribute__((ext_vector_type(8)));
typedef short s16x4 __attribute__((ext_vector_type(4)));
typedef __bf16 bf2 __attribute__((ext_vector_type(2)));
#define LAS __attribute__((address_space(3)))
#define GAS __attribute__((address_space(1)))

constexpr int DM = 512, HKV = 8, SK = 8192, DH = 128, WIN = 2048, FF = 1792, NQ = 4096;
constexpr int NTHR = 576;

constexpr int WS_EPOCH = 0;
constexpr int WS_RDY = 4096;
constexpr int WS_QG = 8192;
constexpr int WS_PA = WS_QG + 512 * 1024;
constexpr int WS_HP = WS_PA + 512 * 1024;
constexpr int WS_H = WS_HP + 256 * 1024;
constexpr int WS_OP = WS_H + 4096;
constexpr int WS_END = WS_OP + 56 * 8192;
constexpr int OOB = 0x40000000;

constexpr int L_K = 0;
constexpr int L_V = 65536;
constexpr int L_WQ = 131072;
constexpr int L_HN = L_WQ;
constexpr int L_Q = L_WQ + 16384;
constexpr int L_XN = L_Q + 1280;
constexpr int L_O = L_XN;
constexpr int L_QP = L_XN + 1280;
constexpr int L_P = L_QP + 1024;
constexpr int L_RM = L_P + 2304;
constexpr int L_RL = L_RM + 128;
constexpr int L_HH = L_RL + 128;
constexpr int L_ZERO = L_HH + 256;
constexpr int L_CNT = L_ZERO + 16;
constexpr int L_BYTES = L_CNT + 16;

constexpr unsigned SENT = 0xFFFFFFFFu;
constexpr int SPIN_CAP = 1 << 16;
constexpr int AUX_SC1 = 16;
constexpr float LOG2E = 1.4426950408889634f;

#define KV_NT 1
#define WQ_NT 1
#define W2_NT 1
#define GU_NT 0

__device__ __forceinline__ float bflo(unsigned x) { return __builtin_bit_cast(float, x << 16); }
__device__ __forceinline__ float bfhi(unsigned x) { return __builtin_bit_cast(float, x & 0xffff0000u); }
__device__ __forceinline__ unsigned pk2(float lo, float hi) {
  bf2 v = {(__bf16)lo, (__bf16)hi};
  return __builtin_bit_cast(unsigned, v);
}
__device__ __forceinline__ float rb(float x) { return (float)(__bf16)x; }
__device__ __forceinline__ unsigned nosent(unsigned x) { return x < 0xFFFFFFFEu ? x : 0xFFFFFFFEu; }
__device__ __forceinline__ v4u nosent4(f32x4 v) {
  return v4u{nosent(__float_as_uint(v[0])), nosent(__float_as_uint(v[1])), nosent(__float_as_uint(v[2])), nosent(__float_as_uint(v[3]))};
}
__device__ __forceinline__ bool fresh(v4u v) { return max(max(v[0], v[1]), max(v[2], v[3])) != SENT; }
__device__ __forceinline__ float vmax(float a, float b) { float r; asm("v_max_f32 %0, %1, %2" : "=v"(r) : "v"(a), "v"(b)); return r; }

__device__ __forceinline__ f32x4 mfma32(v4u a, v4u b, f32x4 c) {
  return __builtin_amdgcn_mfma_f32_16x16x32_bf16(__builtin_bit_cast(bf16x8, a), __builtin_bit_cast(bf16x8, b), c, 0, 0, 0);
}
template <int CTRL> __device__ __forceinline__ float dpp(float x) {
  return __builtin_bit_cast(float, __builtin_amdgcn_mov_dpp(__builtin_bit_cast(int, x), CTRL, 0xf, 0xf, true));
}
template <int CTRL> __device__ __forceinline__ unsigned dppu(unsigned x) { return (unsigned)__builtin_amdgcn_mov_dpp((int)x, CTRL, 0xf, 0xf, true); }
constexpr int ROR8 = 0x128, ROR4 = 0x124, ROR2 = 0x122, ROR1 = 0x121, SHL1 = 0x101, SHL2 = 0x102, SHL4 = 0x104;
__device__ __forceinline__ float row_max(float x) { x = fmaxf(x, dpp<ROR8>(x)); x = fmaxf(x, dpp<ROR4>(x)); x = fmaxf(x, dpp<ROR2>(x)); return fmaxf(x, dpp<ROR1>(x)); }
__device__ __forceinline__ float row_sum(float x) { x += dpp<ROR8>(x); x += dpp<ROR4>(x); x += dpp<ROR2>(x); return x + dpp<ROR1>(x); }
__device__ __forceinline__ float x16_max(float x) { auto s = __builtin_amdgcn_permlane16_swap(__float_as_uint(x), __float_as_uint(x), false, false); return fmaxf(__uint_as_float(s[0]), __uint_as_float(s[1])); }
__device__ __forceinline__ float x16_sum(float x) { auto s = __builtin_amdgcn_permlane16_swap(__float_as_uint(x), __float_as_uint(x), false, false); return __uint_as_float(s[0]) + __uint_as_float(s[1]); }
__device__ __forceinline__ float x32_sum(float x) { auto s = __builtin_amdgcn_permlane32_swap(__float_as_uint(x), __float_as_uint(x), false, false); return __uint_as_float(s[0]) + __uint_as_float(s[1]); }
__device__ __forceinline__ float wave_sum(float x) { return x32_sum(x16_sum(row_sum(x))); }
__device__ __forceinline__ f32x4 sel2(bool c, f32x4 a, f32x4 b) {
  return f32x4{c ? b[0] : a[0], c ? b[1] : a[1], c ? b[2] : a[2], c ? b[3] : a[3]};
}
__device__ __forceinline__ f32x4 sel4(int g, f32x4 a, f32x4 b, f32x4 c, f32x4 d) { return sel2(g & 2, sel2(g & 1, a, b), sel2(g & 1, c, d)); }

#define PIN(x) asm volatile("" : "+v"(x))
#define SCHED() __builtin_amdgcn_sched_barrier(0)
#define LDS_FENCE() asm volatile("s_waitcnt lgkmcnt(0)" ::: "memory")
#define BARRIER() do { asm volatile("s_waitcnt lgkmcnt(0)\n\ts_barrier" ::: "memory"); } while (0)
#define VMWAIT(N) asm volatile("s_waitcnt vmcnt(" #N ")" ::: "memory")
#define STAMP(i) do {} while (0)
#define STAMP_OUT() do {} while (0)

__device__ __forceinline__ v4u make_srd(const void* p, unsigned bytes) {
  const u64 a = (u64)p;
  return v4u{(unsigned)a, (unsigned)(a >> 32) & 0xffffu, bytes, 0x00020000u};
}
template <int NT> __device__ __forceinline__ void dma_buf(unsigned lds, int voff, v4u srd, int soff) {
  if (NT) asm volatile("s_mov_b32 m0, %0\n\ts_nop 0\n\tbuffer_load_dwordx4 %1, %2, %3 offen nt lds" :: "s"(lds), "v"(voff), "s"(srd), "s"(soff) : "m0", "memory");
  else asm volatile("s_mov_b32 m0, %0\n\ts_nop 0\n\tbuffer_load_dwordx4 %1, %2, %3 offen lds" :: "s"(lds), "v"(voff), "s"(srd), "s"(soff) : "m0", "memory");
}
template <int NT> __device__ __forceinline__ void dma_glb(unsigned lds, const void* p) {
  if (NT) asm volatile("s_mov_b32 m0, %0\n\ts_nop 0\n\tglobal_load_lds_dwordx4 %1, off nt" :: "s"(lds), "v"(p) : "m0", "memory");
  else asm volatile("s_mov_b32 m0, %0\n\ts_nop 0\n\tglobal_load_lds_dwordx4 %1, off" :: "s"(lds), "v"(p) : "m0", "memory");
}
__device__ __forceinline__ void ld16_asm(v4u& d, int voff, v4u srd, int soff) {
  asm volatile("buffer_load_dwordx4 %0, %1, %2, %3 offen" : "=v"(d) : "v"(voff), "s"(srd), "s"(soff) : "memory");
}
__device__ __forceinline__ void ld16_sc1_asm(v4u& d, int voff, v4u srd) {
  asm volatile("buffer_load_dwordx4 %0, %1, %2, 0 offen sc1" : "=v"(d) : "v"(voff), "s"(srd) : "memory");
}
__device__ __forceinline__ void ld8_sc1_asm(v2u& d, v4u srd) {
  asm volatile("buffer_load_dwordx2 %0, off, %1, 0 sc1" : "=v"(d) : "s"(srd) : "memory");
}
__device__ __forceinline__ void st16_sc1_asm(v4u v, int voff, v4u srd) {
  asm volatile("buffer_store_dwordx4 %0, %1, %2, 0 offen sc1\n\ts_nop 1" :: "v"(v), "v"(voff), "s"(srd) : "memory");
}

__device__ __forceinline__ v4u ld_sc1(__amdgpu_buffer_rsrc_t rs, int off) { return __builtin_amdgcn_raw_buffer_load_b128(rs, off, 0, AUX_SC1); }
__device__ __forceinline__ void st_sc1(__amdgpu_buffer_rsrc_t rs, int off, v4u v) { __builtin_amdgcn_raw_buffer_store_b128(v, rs, off, 0, AUX_SC1); }
__device__ __forceinline__ v2u tr_rd(LAS unsigned char* L, int off) {
  return __builtin_bit_cast(v2u, __builtin_amdgcn_ds_read_tr16_b64_v4i16((LAS s16x4*)(L + off)));
}
__device__ __forceinline__ v4u tr_frag(LAS unsigned char* L, int off0, int off1) {
  const v2u a = tr_rd(L, off0), b = tr_rd(L, off1);
  return v4u{a[0], a[1], b[0], b[1]};
}

template <int N> __device__ __forceinline__ void poll_fresh(__amdgpu_buffer_rsrc_t rs, const int (&off)[N], v4u (&v)[N]) {
  for (int it = 0; it < SPIN_CAP; ++it) {
    asm volatile("" ::: "memory");
    bool ok = true;
#pragma unroll
    for (int i = 0; i < N; ++i) v[i] = ld_sc1(rs, off[i]);
#pragma unroll
    for (int i = 0; i < N; ++i) ok &= fresh(v[i]);
    if (__all(ok)) break;
  }
}
__device__ __forceinline__ void wait_ready(__amdgpu_buffer_rsrc_t rs, int lane, unsigned tlo, unsigned thi) {
  for (int it = 0; it < SPIN_CAP; ++it) {
    asm volatile("" ::: "memory");
    const v4u r0 = ld_sc1(rs, WS_RDY + lane * 16), r1 = ld_sc1(rs, WS_RDY + 1024 + lane * 16);
    const bool ok = (r0[0] == tlo) & (r0[1] == thi) & (r0[2] == tlo) & (r0[3] == thi) & (r1[0] == tlo) & (r1[1] == thi) & (r1[2] == tlo) & (r1[3] == thi);
    if (__all(ok)) break;
  }
}

__global__ void __launch_bounds__(NTHR, 3)
fused_block(const u16* __restrict__ x, const u16* __restrict__ kc, const u16* __restrict__ vc, const u16* __restrict__ wq,
            const u16* __restrict__ wo, const u16* __restrict__ wg, const u16* __restrict__ wu, const u16* __restrict__ wd,
            u16* __restrict__ out, unsigned char* __restrict__ ws) {
  __shared__ __attribute__((aligned(16))) unsigned char smem_[L_BYTES];
  LAS unsigned char* L = (LAS unsigned char*)smem_;
  const unsigned lbase = (unsigned)(size_t)L;
  const int c = blockIdx.x, kvh = c & 7, i32 = c >> 3;
  const int lane = threadIdx.x & 63, wv = __builtin_amdgcn_readfirstlane(threadIdx.x >> 6);
  const int w = wv == 0 ? 8 : wv - 1;
  const int n = lane & 15, g = lane >> 4;
  const v4u srd_ws = make_srd(ws, WS_END);
  STAMP(0);

  if (wv == 0) {
    const int rq = i32 >> 3, cq = i32 & 7;
    const v4u srd_x = make_srd(x, 4 * DM * 2);
    const v4u srd_wq = make_srd(wq + (size_t)rq * 128 * NQ + kvh * 512 + cq * 64, 128 * NQ * 2);
    v2u ep;
    v4u xr[4];
    ld8_sc1_asm(ep, srd_ws);
#pragma unroll
    for (int i = 0; i < 4; ++i) ld16_asm(xr[i], g * 1024 + n * 16, srd_x, ((rq + i) & 3) * 256);
    {
      const int row = lane >> 3, v0 = row * (NQ * 2) + (((lane & 7) ^ (2 * ((row >> 1) & 1))) * 16);
#pragma unroll
      for (int i = 0; i < 16; ++i) dma_buf<WQ_NT>(lbase + L_WQ + i * 1024, (i & 1) ? (v0 ^ 64) : v0, srd_wq, i * 8 * NQ * 2);
    }
    if (lane == 0) { *(LAS v4u*)(L + L_ZERO) = v4u{0u, 0u, 0u, 0u}; *(LAS unsigned*)(L + L_CNT) = 0u; }
    BARRIER();
    asm volatile("s_waitcnt vmcnt(16)" : "+v"(ep), "+v"(xr[0]), "+v"(xr[1]), "+v"(xr[2]), "+v"(xr[3]) :: "memory");
    {
      float v[32], s = 0.f;
#pragma unroll
      for (int i = 0; i < 4; ++i)
#pragma unroll
        for (int k = 0; k < 4; ++k) { v[8 * i + 2 * k] = bflo(xr[i][k]); v[8 * i + 2 * k + 1] = bfhi(xr[i][k]); }
#pragma unroll
      for (int k = 0; k < 32; k += 4) s += (v[k] + v[k + 1]) + (v[k + 2] + v[k + 3]);
      const float mean = row_sum(s) * (1.0f / DM);
      float q0 = 0.f, q1 = 0.f;
#pragma unroll
      for (int k = 0; k < 32; k += 2) { const float d0 = v[k] - mean, d1 = v[k + 1] - mean; q0 = fmaf(d0, d0, q0); q1 = fmaf(d1, d1, q1); }
      const float q = row_sum(q0 + q1);
      const float mb = rb(mean);
      const float inv = __builtin_amdgcn_rcpf(rb(__builtin_amdgcn_sqrtf(rb(rb(q * (1.0f / DM)) + 1e-5f))));
      v4u o;
#pragma unroll
      for (int k = 0; k < 4; ++k) o[k] = pk2(rb(v[2 * k] - mb) * inv, rb(v[2 * k + 1] - mb) * inv);
      *(LAS v4u*)(L + L_XN + g * 320 + n * 16) = o;
    }
    STAMP(1);
    asm volatile("s_waitcnt vmcnt(0) lgkmcnt(0)" ::: "memory");
    STAMP(4);
    f32x4 acc[4];
    {
      v4u xa[4];
#pragma unroll
      for (int ks = 0; ks < 4; ++ks) xa[ks] = *(LAS v4u*)(L + L_XN + (n & 3) * 320 + ks * 64 + g * 16);
      const int q4 = n >> 2, p = n & 3, y = ((q4 >> 1) & 1) | ((g & 1) << 1);
      const int rbase = L_WQ + (8 * g + q4) * 128 + 8 * (p & 1);
#pragma unroll
      for (int t = 0; t < 4; ++t) {
        const int o = rbase + (((2 * t + (p >> 1)) ^ (2 * y)) * 16);
        acc[t] = f32x4{0.f, 0.f, 0.f, 0.f};
#pragma unroll
        for (int ks = 0; ks < 4; ++ks) acc[t] = mfma32(xa[ks], tr_frag(L, o + ks * 4096, o + ks * 4096 + 512), acc[t]);
      }
    }
    *(LAS f32x4*)(L + L_QP + lane * 16) = sel4(g, acc[0], acc[1], acc[2], acc[3]);
    LDS_FENCE();
    const f32x4 q0 = *(LAS f32x4*)(L + L_QP + (lane & 31) * 32), q1 = *(LAS f32x4*)(L + L_QP + (lane & 31) * 32 + 16);
    const u64 tag = (((u64)ep[1] << 32) | ep[0]) + 0x9E3779B97F4A7C15ull;
    const unsigned tlo = (unsigned)tag, thi = (unsigned)(tag >> 32);
    STAMP(5);
    for (int it = 0; it < (SPIN_CAP << 4); ++it) {
      if (__builtin_amdgcn_readfirstlane(*(volatile LAS unsigned*)(L + L_CNT)) >= 8u) break;
    }
    STAMP(2);
    {
      const bool up = lane >= 32;
      const int o = WS_QG + ((kvh * 4 + (up ? 1 : 0)) * 4 + rq) * 4096 + (32 * cq + (lane & 31)) * 16;
      st16_sc1_asm(v4u{__float_as_uint(up ? q0[1] : q0[0]), __float_as_uint(up ? q1[1] : q1[0]), tlo, thi}, o, srd_ws);
      st16_sc1_asm(v4u{__float_as_uint(up ? q0[3] : q0[2]), __float_as_uint(up ? q1[3] : q1[2]), tlo, thi}, o + 2 * 4 * 4096, srd_ws);
      if (lane == 0) __hip_atomic_store((GAS u64*)(ws + WS_RDY + c * 8), tag, __ATOMIC_RELAXED, __HIP_MEMORY_SCOPE_AGENT);
    }
    STAMP(3);
    STAMP_OUT();
    return;
  }

  const bool is_wo = i32 < 16, is_ffn = i32 >= 16 && i32 < 23, is_red = i32 >= 24;
  const int jf = kvh * 7 + (i32 - 16);
  const int rho = (i32 - 24) * 8 + kvh;
  const __amdgpu_buffer_rsrc_t rs = __builtin_amdgcn_make_buffer_rsrc(ws, 0, WS_END, 0x00020000);
  const int b = i32 >> 3, s = i32 & 7;
  const size_t kvbase = ((size_t)(b * HKV + kvh) * SK + (SK - WIN) + s * 256) * DH;
  const v4u srd_k = make_srd(kc + kvbase, 65536), srd_v = make_srd(vc + kvbase, 65536);
  const int rot = w + 37 * c;
  const int kv0 = g * 256 + ((n ^ g) * 16);
  const int vv0 = g * 256 + ((n ^ ((g << 2) | (w & 3))) * 16);
  const unsigned ldst = lbase + w * 1024;
  const int hp_off = WS_HP + (kvh * 4 + (i32 >> 2)) * 8192 + (i32 & 3) * 2048 + w * 256;
  const int qoff = WS_QG + ((kvh * 4 + b) * 4 + (lane >> 5)) * 4096 + (32 * w + (lane & 31)) * 16;
  asm volatile("" :: "s"(wo), "s"(wg), "s"(wu), "s"(wd), "s"(out), "s"(x));
  v2u ep;
  v4u qg[2];
  {
    const v4u sent = {SENT, SENT, SENT, SENT};
    const int o1 = is_wo ? (lane < 16 ? hp_off + lane * 16 : OOB) : is_ffn ? WS_OP + jf * 8192 + w * 1024 + lane * 16
                 : (is_red && w == 0 && lane < 4) ? WS_H + rho * 64 + lane * 16 : OOB;
    const int o0 = lane < 16 ? WS_PA + c * 2048 + w * 256 + lane * 16 : OOB;
    BARRIER();
    ld8_sc1_asm(ep, srd_ws);
    st16_sc1_asm(sent, o0, srd_ws);
    st16_sc1_asm(sent, o1, srd_ws);
  }
#define KLOAD(i) dma_buf<KV_NT>(ldst + L_K + (i) * 8192, kv0 ^ (((i) & 3) << 6), srd_k, ((8 * (i) + rot) & 63) << 10)
#define VLOAD(i) dma_buf<KV_NT>(ldst + L_V + (i) * 8192, vv0, srd_v, ((8 * (i) + rot) & 63) << 10)
  KLOAD(0); KLOAD(1); KLOAD(2); KLOAD(3);
  VMWAIT(3);
  STAMP(12);
  if (lane == 0) __hip_atomic_fetch_add((LAS unsigned*)(L + L_CNT), 1u, __ATOMIC_RELAXED, __HIP_MEMORY_SCOPE_WORKGROUP);
  KLOAD(4); VMWAIT(3); KLOAD(5); VMWAIT(3); KLOAD(6); VMWAIT(3); KLOAD(7);
  STAMP(1);
  VMWAIT(3); VLOAD(0); VMWAIT(3); VLOAD(1);
  ld16_sc1_asm(qg[0], qoff, srd_ws);
  ld16_sc1_asm(qg[1], qoff + 8192, srd_ws);
  VMWAIT(4); VLOAD(2); VMWAIT(4); VLOAD(3); VMWAIT(4); VLOAD(4);
  asm volatile("s_waitcnt vmcnt(3)" : "+v"(ep), "+v"(qg[0]), "+v"(qg[1]) :: "memory");
  STAMP(13);
  const u64 tag = (((u64)ep[1] << 32) | ep[0]) + 0x9E3779B97F4A7C15ull;
  const unsigned tlo = (unsigned)tag, thi = (unsigned)(tag >> 32);
  for (int it = 0; it < SPIN_CAP; ++it) {
    const bool ok = (qg[0][2] == tlo) & (qg[0][3] == thi) & (qg[1][2] == tlo) & (qg[1][3] == thi);
    if (__all(ok)) break;
    asm volatile("buffer_load_dwordx4 %0, %2, %3, 0 offen sc1\n\tbuffer_load_dwordx4 %1, %4, %3, 0 offen sc1\n\ts_waitcnt vmcnt(0)"
                 : "=&v"(qg[0]), "=&v"(qg[1]) : "v"(qoff), "s"(srd_ws), "v"(qoff + 8192) : "memory");
  }
  VLOAD(5); VLOAD(6); VLOAD(7);
  STAMP(2);
  {
    const float a = x32_sum(__uint_as_float(qg[0][0]) + __uint_as_float(qg[1][0]));
    const float bq = x32_sum(__uint_as_float(qg[0][1]) + __uint_as_float(qg[1][1]));
    if (lane < 32) *(LAS unsigned*)(L + L_Q + (w >> 1) * 320 + (w & 1) * 128 + lane * 4) = pk2(a, bq);
  }
  BARRIER();
  STAMP(3);
  float sc[2];
  {
    const int qb = L_Q + (2 * (n >> 3) + (n & 1)) * 320 + g * 16;
    const int kb = L_K + (n >> 2) * 8192 + w * 1024 + (n & 3) * 256 + ((g ^ n) * 16);
    f32x4 d0 = {0.f, 0.f, 0.f, 0.f}, d1 = {0.f, 0.f, 0.f, 0.f};
#pragma unroll
    for (int ks = 0; ks < 4; ++ks) {
      const v4u qa = *(LAS v4u*)(L + qb + ks * 64);
      d0 = mfma32(qa, *(LAS v4u*)(L + (kb ^ (ks << 6))), d0);
      d1 = mfma32(qa, *(LAS v4u*)(L + (kb ^ (ks << 6)) + 32768), d1);
    }
    f32x2 mx;
#pragma unroll
    for (int j = 0; j < 2; ++j) {
      sc[j] = rb(rb((g & 1) ? d1[j] : d0[j]) * 0.08838834764831845f);
      mx[j] = x16_max(row_max(sc[j]));
    }
    if ((lane & 31) == 0) *(LAS f32x2*)(L + L_RM + (g >> 1) * 64 + w * 8) = mx;
  }
  BARRIER();
  STAMP(14);
  const u16* gup = ((lane >> 2) & 1 ? wu : wg) + (size_t)(64 * w + (lane >> 3)) * FF + 32 * jf + 8 * ((lane & 3) ^ (g & 1));
  {
    const u16* p = is_wo ? wo + (size_t)((kvh * 4 + (i32 >> 2)) * 128 + 4 * w) * DM + (i32 & 3) * 128 + g * (DM - 128) + (vv0 >> 1) : is_ffn ? gup : x;
    const int st = is_wo ? 32 * DM : is_ffn ? 8 * FF : 0;
    const unsigned d0 = lbase + L_K + (is_wo ? w * 1024 : w * 8192), ds = is_wo ? 8192 : 1024;
    const int alt = is_ffn ? (((lane & 2) ? -16 : 16)) : 0;
#pragma unroll
    for (int i = 0; i < 4; ++i) {
      if (GU_NT == W2_NT) dma_glb<W2_NT>(d0 + i * ds, p + i * st + ((i & 1) ? alt : 0));
      else if (is_ffn) dma_glb<GU_NT>(d0 + i * ds, p + i * st + ((i & 1) ? alt : 0));
      else dma_glb<W2_NT>(d0 + i * ds, p + i * st + ((i & 1) ? alt : 0));
    }
  }
  STAMP(15);
  float mh;
  {
    f32x4 t[4];
#pragma unroll
    for (int i = 0; i < 4; ++i) t[i] = *(LAS f32x4*)(L + L_RM + (g >> 1) * 64 + i * 16);
    const float M0 = fmaxf(fmaxf(fmaxf(t[0][0], t[0][2]), fmaxf(t[1][0], t[1][2])), fmaxf(fmaxf(t[2][0], t[2][2]), fmaxf(t[3][0], t[3][2])));
    const float M1 = fmaxf(fmaxf(fmaxf(t[0][1], t[0][3]), fmaxf(t[1][1], t[1][3])), fmaxf(fmaxf(t[2][1], t[2][3]), fmaxf(t[3][1], t[3][3])));
    const float p0 = __builtin_amdgcn_exp2f((sc[0] - M0) * LOG2E), p1 = __builtin_amdgcn_exp2f((sc[1] - M1) * LOG2E);
    const int po = L_P + (g >> 1) * 1152 + ((4 * (g & 1) + (n >> 2)) * 32 + 4 * w + (n & 3)) * 2;
    const unsigned pp = pk2(p0, p1);
    *(LAS u16*)(L + po) = (u16)pp;
    *(LAS u16*)(L + po + 576) = (u16)(pp >> 16);
    const f32x2 ls = {x16_sum(row_sum(p0)), x16_sum(row_sum(p1))};
    if ((lane & 31) == 0) *(LAS f32x2*)(L + L_RL + (g >> 1) * 64 + w * 8) = ls;
    mh = (g & 1) ? M1 : M0;
  }
  STAMP(4);
  const int tq = n >> 2, tp = n & 3;
  const int tr0 = (8 * g + tq) * 256 + (((2 * w + (tp >> 1)) ^ ((tq << 2) | ((2 * g) & 3))) * 16) + 8 * (tp & 1);
  const int tr1 = (8 * g + 4 + tq) * 256 + (((2 * w + (tp >> 1)) ^ ((tq << 2) | ((2 * g + 1) & 3))) * 16) + 8 * (tp & 1);
  VMWAIT(6);
  BARRIER();
  {
    v4u pa[8];
#pragma unroll
    for (int i = 0; i < 8; ++i) pa[i] = *(LAS v4u*)(L + L_P + (n >> 2) * 576 + i * 64 + g * 16);
    f32x4 t[4];
#pragma unroll
    for (int i = 0; i < 4; ++i) t[i] = *(LAS f32x4*)(L + L_RL + (g >> 1) * 64 + i * 16);
    f32x4 acc = {0.f, 0.f, 0.f, 0.f};
#pragma unroll
    for (int i = 0; i < 6; ++i) acc = mfma32(pa[i], tr_frag(L, L_V + tr0 + i * 8192, L_V + tr1 + i * 8192), acc);
    const f32x4 ts = (t[0] + t[1]) + (t[2] + t[3]);
    unsigned ml[2] = {nosent(__float_as_uint(mh)), nosent(__float_as_uint((g & 1) ? ts[1] + ts[3] : ts[0] + ts[2]))};
    int pao = (n & 3) == 0 ? WS_PA + c * 2048 + w * 256 + g * 64 + (n >> 2) * 16 : OOB;
    PIN(ml[0]); PIN(ml[1]); PIN(pao); PIN(acc); PIN(pa[6]); PIN(pa[7]);
    VMWAIT(4);
    STAMP(5);
    BARRIER();
#pragma unroll
    for (int i = 6; i < 8; ++i) acc = mfma32(pa[i], tr_frag(L, L_V + tr0 + i * 8192, L_V + tr1 + i * 8192), acc);
    const unsigned d0 = nosent(pk2(acc[0], dpp<SHL1>(acc[0])));
    st_sc1(rs, pao, v4u{d0, dppu<SHL2>(d0), ml[0], ml[1]});
  }
  STAMP(6);

  if (is_wo) {
    const int hh = i32 >> 2;
    int off[2];
#pragma unroll
    for (int i = 0; i < 2; ++i) off[i] = WS_PA + (kvh + 8 * (8 * (n & 3) + 2 * (n >> 2) + i)) * 2048 + w * 256 + hh * 64 + g * 16;
    VMWAIT(1);
    BARRIER();
    v4u bf[4];
#pragma unroll
    for (int i = 0; i < 4; ++i) { bf[i] = tr_frag(L, L_K + tr0 + i * 8192, L_K + tr1 + i * 8192); PIN(bf[i]); }
    STAMP(7);
    v4u pv[2];
    poll_fresh<2>(rs, off, pv);
    STAMP(8);
    const float m0 = __uint_as_float(pv[0][2]), m1 = __uint_as_float(pv[1][2]);
    float mm = vmax(m0, m1);
    mm = vmax(mm, dpp<ROR4>(mm)); mm = vmax(mm, dpp<ROR8>(mm));
    const float e0 = __builtin_amdgcn_exp2f((m0 - mm) * LOG2E), e1 = __builtin_amdgcn_exp2f((m1 - mm) * LOG2E);
    float t[5];
    t[0] = e0 * bflo(pv[0][0]) + e1 * bflo(pv[1][0]);
    t[1] = e0 * bfhi(pv[0][0]) + e1 * bfhi(pv[1][0]);
    t[2] = e0 * bflo(pv[0][1]) + e1 * bflo(pv[1][1]);
    t[3] = e0 * bfhi(pv[0][1]) + e1 * bfhi(pv[1][1]);
    t[4] = e0 * __uint_as_float(pv[0][3]) + e1 * __uint_as_float(pv[1][3]);
#pragma unroll
    for (int i = 0; i < 5; ++i) { t[i] += dpp<ROR4>(t[i]); t[i] += dpp<ROR8>(t[i]); }
    const float inv = __builtin_amdgcn_rcpf(t[4]);
    if (n < 4) *(LAS v2u*)(L + L_O + n * 320 + w * 32 + g * 8) = v2u{pk2(t[0] * inv, t[1] * inv), pk2(t[2] * inv, t[3] * inv)};
    BARRIER();
    f32x4 acc = {0.f, 0.f, 0.f, 0.f};
#pragma unroll
    for (int i = 0; i < 4; ++i) acc = mfma32(*(LAS v4u*)(L + L_O + (n & 3) * 320 + i * 64 + g * 16), bf[i], acc);
    st_sc1(rs, lane < 16 ? hp_off + lane * 16 : OOB, nosent4(acc));
    STAMP(9);
  } else if (is_ffn) {
    {
      const int alt = (lane & 2) ? -16 : 16;
#pragma unroll
      for (int i = 4; i < 8; ++i) dma_glb<GU_NT>(lbase + L_K + w * 8192 + i * 1024, gup + i * 8 * FF + ((i & 1) ? alt : 0));
    }
    BARRIER();
    {
      const v4u srd_d = make_srd(wd + (size_t)(32 * jf + 4 * w) * DM, 4 * DM * 2);
#pragma unroll
      for (int i = 0; i < 4; ++i) dma_buf<W2_NT>(ldst + L_V + i * 8192, vv0 + g * (DM * 2 - 256), srd_d, i * 256);
    }
    if (w < 4) wait_ready(rs, lane, tlo, thi);
    VMWAIT(0);
    BARRIER();
    v4u bf[16], bd[4];
    {
      const int q2 = L_V + (w >> 1) * 8192, x0 = ((8 * (w & 1)) ^ (2 * w)) << 4;
#pragma unroll
      for (int i = 0; i < 4; ++i) { bd[i] = tr_frag(L, q2 + (tr0 ^ x0 ^ (i << 5)), q2 + (tr1 ^ x0 ^ (i << 5))); PIN(bd[i]); }
    }
    int ao[4];
#pragma unroll
    for (int i = 0; i < 4; ++i) { ao[i] = (n >> 2) == i ? L_HH + (n & 3) * 64 + g * 16 : L_ZERO; PIN(ao[i]); }
    if (w < 4) {
      const int z = ((tq >> 1) & 1) | ((g & 1) << 1);
      const int o = L_K + (8 * g + tq) * 128 + ((4 * (tp >> 1) + (w ^ z)) * 16) + 8 * (tp & 1);
#pragma unroll
      for (int i = 0; i < 16; ++i) { bf[i] = tr_frag(L, o + i * 4096, o + i * 4096 + 512); PIN(bf[i]); }
      STAMP(7);
      const int off[1] = {WS_H + lane * 64 + w * 16};
      v4u hv[1];
      poll_fresh<1>(rs, off, hv);
      STAMP(8);
      float v[8];
#pragma unroll
      for (int k = 0; k < 4; ++k) { v[2 * k] = bflo(hv[0][k]); v[2 * k + 1] = bfhi(hv[0][k]); }
      const float mean = wave_sum(((v[0] + v[1]) + (v[2] + v[3])) + ((v[4] + v[5]) + (v[6] + v[7]))) * (1.0f / DM);
      float q0 = 0.f, q1 = 0.f;
#pragma unroll
      for (int k = 0; k < 8; k += 2) { const float d0 = v[k] - mean, d1 = v[k + 1] - mean; q0 = fmaf(d0, d0, q0); q1 = fmaf(d1, d1, q1); }
      const float q = wave_sum(q0 + q1);
      const float mb = rb(mean);
      const float inv = __builtin_amdgcn_rcpf(rb(__builtin_amdgcn_sqrtf(rb(rb(q * (1.0f / DM)) + 1e-5f))));
      v4u o4;
#pragma unroll
      for (int k = 0; k < 4; ++k) o4[k] = pk2(rb(v[2 * k] - mb) * inv, rb(v[2 * k + 1] - mb) * inv);
      *(LAS v4u*)(L + L_HN + w * 1088 + lane * 16) = o4;
    }
    BARRIER();
    if (w < 4) {
      f32x4 acc = {0.f, 0.f, 0.f, 0.f};
#pragma unroll
      for (int i = 0; i < 16; ++i) acc = mfma32(*(LAS v4u*)(L + L_HN + (n >> 2) * 1088 + i * 64 + g * 16), bf[i], acc);
      const float a = rb(acc[0]), u = dpp<ROR8>(a);
      const float sg = rb(__builtin_amdgcn_rcpf(1.0f + __builtin_amdgcn_exp2f(a * -LOG2E)));
      if (n < 8) *(LAS u16*)(L + L_HH + g * 64 + (8 * w + n) * 2) = (u16)pk2(sg * u, 0.f);
    }
    BARRIER();
    {
      f32x4 acc = {0.f, 0.f, 0.f, 0.f};
#pragma unroll
      for (int i = 0; i < 4; ++i) acc = mfma32(*(LAS v4u*)(L + ao[i]), bd[i], acc);
      st_sc1(rs, WS_OP + jf * 8192 + w * 1024 + lane * 16, nosent4(acc));
    }
    STAMP(9);
  } else if (is_red && w == 0) {
    const int r2 = lane >> 3, k = lane & 7, bb = r2 & 3;
    const float xv = bflo(x[bb * DM + 8 * rho + k]);
    int off1[4], off2[7];
#pragma unroll
    for (int i = 0; i < 4; ++i) off1[i] = WS_HP + (8 * i + r2) * 8192 + (8 * rho + k) * 16;
#pragma unroll
    for (int i = 0; i < 7; ++i) off2[i] = WS_OP + (8 * i + r2) * 8192 + (8 * rho + k) * 16;
    wait_ready(rs, lane, tlo, thi);
    STAMP(7);
    float hval;
    {
      v4u pv[4];
      poll_fresh<4>(rs, off1, pv);
      STAMP(8);
      f32x4 a = (__builtin_bit_cast(f32x4, pv[0]) + __builtin_bit_cast(f32x4, pv[1])) + (__builtin_bit_cast(f32x4, pv[2]) + __builtin_bit_cast(f32x4, pv[3]));
#pragma unroll
      for (int i = 0; i < 4; ++i) { a[i] += dpp<ROR8>(a[i]); a[i] = x32_sum(x16_sum(a[i])); }
      const float av = (bb & 2) ? ((bb & 1) ? a[3] : a[2]) : ((bb & 1) ? a[1] : a[0]);
      hval = rb(xv + rb(av));
      const unsigned dw = nosent(pk2(hval, dpp<SHL1>(hval)));
      const unsigned dw2 = dppu<SHL2>(dw);
      const unsigned dw4 = dppu<SHL4>(dw), dw6 = dppu<SHL4>(dw2);
      st_sc1(rs, (k == 0 && lane < 32) ? WS_H + rho * 64 + bb * 16 : OOB, v4u{dw, dw2, dw4, dw6});
    }
    STAMP(9);
    {
      v4u pv[7];
      poll_fresh<7>(rs, off2, pv);
      STAMP(10);
      f32x4 a = ((__builtin_bit_cast(f32x4, pv[0]) + __builtin_bit_cast(f32x4, pv[1])) + (__builtin_bit_cast(f32x4, pv[2]) + __builtin_bit_cast(f32x4, pv[3]))) +
                ((__builtin_bit_cast(f32x4, pv[4]) + __builtin_bit_cast(f32x4, pv[5])) + __builtin_bit_cast(f32x4, pv[6]));
#pragma unroll
      for (int i = 0; i < 4; ++i) { a[i] += dpp<ROR8>(a[i]); a[i] = x32_sum(x16_sum(a[i])); }
      const float av = (bb & 2) ? ((bb & 1) ? a[3] : a[2]) : ((bb & 1) ? a[1] : a[0]);
      const float ov = hval + rb(av);
      const unsigned dw = pk2(ov, dpp<SHL1>(ov));
      const unsigned dw2 = dppu<SHL2>(dw);
      const unsigned dw4 = dppu<SHL4>(dw), dw6 = dppu<SHL4>(dw2);
      if (k == 0 && lane < 32) *(GAS v4u*)(out + bb * DM + 8 * rho) = v4u{dw, dw2, dw4, dw6};
      if (c == 192 && lane == 0) __hip_atomic_store((GAS u64*)(ws + WS_EPOCH), tag, __ATOMIC_RELAXED, __HIP_MEMORY_SCOPE_AGENT);
    }
    STAMP(11);
  }
  STAMP_OUT();
}

}

extern "C" void kernel_launch(void* const* d_in, const int* in_sizes, int n_in, void* d_out, int out_size, void* d_ws, size_t ws_size,
                              hipStream_t stream) {
  (void)in_sizes; (void)n_in; (void)out_size; (void)ws_size;
  fused_block<<<256, NTHR, 0, stream>>>((const u16*)d_in[0], (const u16*)d_in[1], (const u16*)d_in[2], (const u16*)d_in[3], (const u16*)d_in[4],
                                        (const u16*)d_in[6], (const u16*)d_in[7], (const u16*)d_in[8], (u16*)d_out, (unsigned char*)d_ws);
}
```

```cpp
#define FIXDUR 4000
#define FIXALL 0
#include <hip/hip_runtime.h>
#include <hip/hip_bf16.h>
#include <stdint.h>

namespace {

typedef unsigned short u16;
typedef unsigned long long u64;
typedef unsigned v4u __attribute__((ext_vector_type(4)));
typedef unsigned v2u __attribute__((ext_vector_type(2)));
typedef float f32x4 __attribute__((ext_vector_type(4)));
typedef float f32x2 __attribute__((ext_vector_type(2)));
typedef short bf16x8 __attribute__((ext_vector_type(8)));
typedef short s16x4 __attribute__((ext_vector_type(4)));
typedef __bf16 bf2 __attribute__((ext_vector_type(2)));
#define LAS __attribute__((address_space(3)))
#define GAS __attribute__((address_space(1)))

constexpr int DM = 512, HKV = 8, SK = 8192, DH = 128, WIN = 2048, FF = 1792, NQ = 4096;
constexpr int NTHR = 576;

constexpr int WS_EPOCH = 0;
constexpr int WS_RDY = 4096;
constexpr int WS_QG = 8192;
constexpr int WS_PA = WS_QG + 512 * 1024;
constexpr int WS_HP = WS_PA + 512 * 1024;
constexpr int WS_H = WS_HP + 256 * 1024;
constexpr int WS_OP = WS_H + 4096;
constexpr int WS_END = WS_OP + 56 * 8192;
constexpr int OOB = 0x40000000;

constexpr int L_K = 0;
constexpr int L_V = 65536;
constexpr int L_WQ = 131072;
constexpr int L_HN = L_WQ;
constexpr int L_Q = L_WQ + 16384;
constexpr int L_XN = L_Q + 1280;
constexpr int L_O = L_XN;
constexpr int L_QP = L_XN + 1280;
constexpr int L_P = L_QP + 1024;
constexpr int L_RM = L_P + 2304;
constexpr int L_RL = L_RM + 128;
constexpr int L_HH = L_RL + 128;
constexpr int L_ZERO = L_HH + 256;
constexpr int L_CNT = L_ZERO + 16;
constexpr int L_BYTES = L_CNT + 16;

constexpr unsigned SENT = 0xFFFFFFFFu;
constexpr int SPIN_CAP = 1 << 16;
constexpr int AUX_SC1 = 16;
constexpr float LOG2E = 1.4426950408889634f;

#define KV_NT 1
#define WQ_NT 1
#define W2_NT 1
#define GU_NT 0

__device__ __forceinline__ float bflo(unsigned x) { return __builtin_bit_cast(float, x << 16); }
__device__ __forceinline__ float bfhi(unsigned x) { return __builtin_bit_cast(float, x & 0xffff0000u); }
__device__ __forceinline__ unsigned pk2(float lo, float hi) {
  bf2 v = {(__bf16)lo, (__bf16)hi};
  return __builtin_bit_cast(unsigned, v);
}
__device__ __forceinline__ float rb(float x) { return (float)(__bf16)x; }
__device__ __forceinline__ unsigned nosent(unsigned x) { return x < 0xFFFFFFFEu ? x : 0xFFFFFFFEu; }
__device__ __forceinline__ v4u nosent4(f32x4 v) {
  return v4u{nosent(__float_as_uint(v[0])), nosent(__float_as_uint(v[1])), nosent(__float_as_uint(v[2])), nosent(__float_as_uint(v[3]))};
}
__device__ __forceinline__ bool fresh(v4u v) { return max(max(v[0], v[1]), max(v[2], v[3])) != SENT; }
__device__ __forceinline__ float vmax(float a, float b) { float r; asm("v_max_f32 %0, %1, %2" : "=v"(r) : "v"(a), "v"(b)); return r; }

__device__ __forceinline__ f32x4 mfma32(v4u a, v4u b, f32x4 c) {
  return __builtin_amdgcn_mfma_f32_16x16x32_bf16(__builtin_bit_cast(bf16x8, a), __builtin_bit_cast(bf16x8, b), c, 0, 0, 0);
}
template <int CTRL> __device__ __forceinline__ float dpp(float x) {
  return __builtin_bit_cast(float, __builtin_amdgcn_mov_dpp(__builtin_bit_cast(int, x), CTRL, 0xf, 0xf, true));
}
template <int CTRL> __device__ __forceinline__ unsigned dppu(unsigned x) { return (unsigned)__builtin_amdgcn_mov_dpp((int)x, CTRL, 0xf, 0xf, true); }
constexpr int ROR8 = 0x128, ROR4 = 0x124, ROR2 = 0x122, ROR1 = 0x121, SHL1 = 0x101, SHL2 = 0x102, SHL4 = 0x104;
__device__ __forceinline__ float row_max(float x) { x = fmaxf(x, dpp<ROR8>(x)); x = fmaxf(x, dpp<ROR4>(x)); x = fmaxf(x, dpp<ROR2>(x)); return fmaxf(x, dpp<ROR1>(x)); }
__device__ __forceinline__ float row_sum(float x) { x += dpp<ROR8>(x); x += dpp<ROR4>(x); x += dpp<ROR2>(x); return x + dpp<ROR1>(x); }
__device__ __forceinline__ float x16_max(float x) { auto s = __builtin_amdgcn_permlane16_swap(__float_as_uint(x), __float_as_uint(x), false, false); return fmaxf(__uint_as_float(s[0]), __uint_as_float(s[1])); }
__device__ __forceinline__ float x16_sum(float x) { auto s = __builtin_amdgcn_permlane16_swap(__float_as_uint(x), __float_as_uint(x), false, false); return __uint_as_float(s[0]) + __uint_as_float(s[1]); }
__device__ __forceinline__ float x32_sum(float x) { auto s = __builtin_amdgcn_permlane32_swap(__float_as_uint(x), __float_as_uint(x), false, false); return __uint_as_float(s[0]) + __uint_as_float(s[1]); }
__device__ __forceinline__ float wave_sum(float x) { return x32_sum(x16_sum(row_sum(x))); }
__device__ __forceinline__ f32x4 sel2(bool c, f32x4 a, f32x4 b) {
  return f32x4{c ? b[0] : a[0], c ? b[1] : a[1], c ? b[2] : a[2], c ? b[3] : a[3]};
}
__device__ __forceinline__ f32x4 sel4(int g, f32x4 a, f32x4 b, f32x4 c, f32x4 d) { return sel2(g & 2, sel2(g & 1, a, b), sel2(g & 1, c, d)); }

#define PIN(x) asm volatile("" : "+v"(x))
#define SCHED() __builtin_amdgcn_sched_barrier(0)
#define LDS_FENCE() asm volatile("s_waitcnt lgkmcnt(0)" ::: "memory")
#define BARRIER() do { asm volatile("s_waitcnt lgkmcnt(0)\n\ts_barrier" ::: "memory"); } while (0)
#define VMWAIT(N) asm volatile("s_waitcnt vmcnt(" #N ")" ::: "memory")
#define STAMP(i) do { if ((i) == 0) tstart = __builtin_amdgcn_s_memrealtime(); } while (0)
#define STAMP_OUT() do { if (FIXALL || (c == 192 && w == 0)) while (__builtin_amdgcn_s_memrealtime() < tstart + FIXDUR) __builtin_amdgcn_s_sleep(1); } while (0)

__device__ __forceinline__ v4u make_srd(const void* p, unsigned bytes) {
  const u64 a = (u64)p;
  return v4u{(unsigned)a, (unsigned)(a >> 32) & 0xffffu, bytes, 0x00020000u};
}
template <int NT> __device__ __forceinline__ void dma_buf(unsigned lds, int voff, v4u srd, int soff) {
  if (NT) asm volatile("s_mov_b32 m0, %0\n\ts_nop 0\n\tbuffer_load_dwordx4 %1, %2, %3 offen nt lds" :: "s"(lds), "v"(voff), "s"(srd), "s"(soff) : "m0", "memory");
  else asm volatile("s_mov_b32 m0, %0\n\ts_nop 0\n\tbuffer_load_dwordx4 %1, %2, %3 offen lds" :: "s"(lds), "v"(voff), "s"(srd), "s"(soff) : "m0", "memory");
}
template <int NT> __device__ __forceinline__ void dma_glb(unsigned lds, const void* p) {
  if (NT) asm volatile("s_mov_b32 m0, %0\n\ts_nop 0\n\tglobal_load_lds_dwordx4 %1, off nt" :: "s"(lds), "v"(p) : "m0", "memory");
  else asm volatile("s_mov_b32 m0, %0\n\ts_nop 0\n\tglobal_load_lds_dwordx4 %1, off" :: "s"(lds), "v"(p) : "m0", "memory");
}
__device__ __forceinline__ void ld16_asm(v4u& d, int voff, v4u srd, int soff) {
  asm volatile("buffer_load_dwordx4 %0, %1, %2, %3 offen" : "=v"(d) : "v"(voff), "s"(srd), "s"(soff) : "memory");
}
__device__ __forceinline__ void ld16_sc1_asm(v4u& d, int voff, v4u srd) {
  asm volatile("buffer_load_dwordx4 %0, %1, %2, 0 offen sc1" : "=v"(d) : "v"(voff), "s"(srd) : "memory");
}
__device__ __forceinline__ void ld8_sc1_asm(v2u& d, v4u srd) {
  asm volatile("buffer_load_dwordx2 %0, off, %1, 0 sc1" : "=v"(d) : "s"(srd) : "memory");
}
__device__ __forceinline__ void st16_sc1_asm(v4u v, int voff, v4u srd) {
  asm volatile("buffer_store_dwordx4 %0, %1, %2, 0 offen sc1\n\ts_nop 1" :: "v"(v), "v"(voff), "s"(srd) : "memory");
}

__device__ __forceinline__ v4u ld_sc1(__amdgpu_buffer_rsrc_t rs, int off) { return __builtin_amdgcn_raw_buffer_load_b128(rs, off, 0, AUX_SC1); }
__device__ __forceinline__ void st_sc1(__amdgpu_buffer_rsrc_t rs, int off, v4u v) { __builtin_amdgcn_raw_buffer_store_b128(v, rs, off, 0, AUX_SC1); }
__device__ __forceinline__ v2u tr_rd(LAS unsigned char* L, int off) {
  return __builtin_bit_cast(v2u, __builtin_amdgcn_ds_read_tr16_b64_v4i16((LAS s16x4*)(L + off)));
}
__device__ __forceinline__ v4u tr_frag(LAS unsigned char* L, int off0, int off1) {
  const v2u a = tr_rd(L, off0), b = tr_rd(L, off1);
  return v4u{a[0], a[1], b[0], b[1]};
}

template <int N> __device__ __forceinline__ void poll_fresh(__amdgpu_buffer_rsrc_t rs, const int (&off)[N], v4u (&v)[N]) {
  for (int it = 0; it < SPIN_CAP; ++it) {
    asm volatile("" ::: "memory");
    bool ok = true;
#pragma unroll
    for (int i = 0; i < N; ++i) v[i] = ld_sc1(rs, off[i]);
#pragma unroll
    for (int i = 0; i < N; ++i) ok &= fresh(v[i]);
    if (__all(ok)) break;
  }
}
__device__ __forceinline__ void wait_ready(__amdgpu_buffer_rsrc_t rs, int lane, unsigned tlo, unsigned thi) {
  for (int it = 0; it < SPIN_CAP; ++it) {
    asm volatile("" ::: "memory");
    const v4u r0 = ld_sc1(rs, WS_RDY + lane * 16), r1 = ld_sc1(rs, WS_RDY + 1024 + lane * 16);
    const bool ok = (r0[0] == tlo) & (r0[1] == thi) & (r0[2] == tlo) & (r0[3] == thi) & (r1[0] == tlo) & (r1[1] == thi) & (r1[2] == tlo) & (r1[3] == thi);
    if (__all(ok)) break;
  }
}

__global__ void __launch_bounds__(NTHR, 3)
fused_block(const u16* __restrict__ x, const u16* __restrict__ kc, const u16* __restrict__ vc, const u16* __restrict__ wq,
            const u16* __restrict__ wo, const u16* __restrict__ wg, const u16* __restrict__ wu, const u16* __restrict__ wd,
            u16* __restrict__ out, unsigned char* __restrict__ ws) {
  __shared__ __attribute__((aligned(16))) unsigned char smem_[L_BYTES];
  LAS unsigned char* L = (LAS unsigned char*)smem_;
  const unsigned lbase = (unsigned)(size_t)L;
  const int c = blockIdx.x, kvh = c & 7, i32 = c >> 3;
  const int lane = threadIdx.x & 63, wv = __builtin_amdgcn_readfirstlane(threadIdx.x >> 6);
  const int w = wv == 0 ? 8 : wv - 1;
  const int n = lane & 15, g = lane >> 4;
  const v4u srd_ws = make_srd(ws, WS_END);
  u64 tstart;
  STAMP(0);

  if (wv == 0) {
    const int rq = i32 >> 3, cq = i32 & 7;
    const v4u srd_x = make_srd(x, 4 * DM * 2);
    const v4u srd_wq = make_srd(wq + (size_t)rq * 128 * NQ + kvh * 512 + cq * 64, 128 * NQ * 2);
    v2u ep;
    v4u xr[4];
    ld8_sc1_asm(ep, srd_ws);
#pragma unroll
    for (int i = 0; i < 4; ++i) ld16_asm(xr[i], g * 1024 + n * 16, srd_x, ((rq + i) & 3) * 256);
    {
      const int row = lane >> 3, v0 = row * (NQ * 2) + (((lane & 7) ^ (2 * ((row >> 1) & 1))) * 16);
#pragma unroll
      for (int i = 0; i < 16; ++i) dma_buf<WQ_NT>(lbase + L_WQ + i * 1024, (i & 1) ? (v0 ^ 64) : v0, srd_wq, i * 8 * NQ * 2);
    }
    if (lane == 0) { *(LAS v4u*)(L + L_ZERO) = v4u{0u, 0u, 0u, 0u}; *(LAS unsigned*)(L + L_CNT) = 0u; }
    BARRIER();
    asm volatile("s_waitcnt vmcnt(16)" : "+v"(ep), "+v"(xr[0]), "+v"(xr[1]), "+v"(xr[2]), "+v"(xr[3]) :: "memory");
    {
      float v[32], s = 0.f;
#pragma unroll
      for (int i = 0; i < 4; ++i)
#pragma unroll
        for (int k = 0; k < 4; ++k) { v[8 * i + 2 * k] = bflo(xr[i][k]); v[8 * i + 2 * k + 1] = bfhi(xr[i][k]); }
#pragma unroll
      for (int k = 0; k < 32; k += 4) s += (v[k] + v[k + 1]) + (v[k + 2] + v[k + 3]);
      const float mean = row_sum(s) * (1.0f / DM);
      float q0 = 0.f, q1 = 0.f;
#pragma unroll
      for (int k = 0; k < 32; k += 2) { const float d0 = v[k] - mean, d1 = v[k + 1] - mean; q0 = fmaf(d0, d0, q0); q1 = fmaf(d1, d1, q1); }
      const float q = row_sum(q0 + q1);
      const float mb = rb(mean);
      const float inv = __builtin_amdgcn_rcpf(rb(__builtin_amdgcn_sqrtf(rb(rb(q * (1.0f / DM)) + 1e-5f))));
      v4u o;
#pragma unroll
      for (int k = 0; k < 4; ++k) o[k] = pk2(rb(v[2 * k] - mb) * inv, rb(v[2 * k + 1] - mb) * inv);
      *(LAS v4u*)(L + L_XN + g * 320 + n * 16) = o;
    }
    STAMP(1);
    asm volatile("s_waitcnt vmcnt(0) lgkmcnt(0)" ::: "memory");
    STAMP(4);
    f32x4 acc[4];
    {
      v4u xa[4];
#pragma unroll
      for (int ks = 0; ks < 4; ++ks) xa[ks] = *(LAS v4u*)(L + L_XN + (n & 3) * 320 + ks * 64 + g * 16);
      const int q4 = n >> 2, p = n & 3, y = ((q4 >> 1) & 1) | ((g & 1) << 1);
      const int rbase = L_WQ + (8 * g + q4) * 128 + 8 * (p & 1);
#pragma unroll
      for (int t = 0; t < 4; ++t) {
        const int o = rbase + (((2 * t + (p >> 1)) ^ (2 * y)) * 16);
        acc[t] = f32x4{0.f, 0.f, 0.f, 0.f};
#pragma unroll
        for (int ks = 0; ks < 4; ++ks) acc[t] = mfma32(xa[ks], tr_frag(L, o + ks * 4096, o + ks * 4096 + 512), acc[t]);
      }
    }
    *(LAS f32x4*)(L + L_QP + lane * 16) = sel4(g, acc[0], acc[1], acc[2], acc[3]);
    LDS_FENCE();
    const f32x4 q0 = *(LAS f32x4*)(L + L_QP + (lane & 31) * 32), q1 = *(LAS f32x4*)(L + L_QP + (lane & 31) * 32 + 16);
    const u64 tag = (((u64)ep[1] << 32) | ep[0]) + 0x9E3779B97F4A7C15ull;
    const unsigned tlo = (unsigned)tag, thi = (unsigned)(tag >> 32);
    STAMP(5);
    for (int it = 0; it < (SPIN_CAP << 4); ++it) {
      if (__builtin_amdgcn_readfirstlane(*(volatile LAS unsigned*)(L + L_CNT)) >= 8u) break;
    }
    STAMP(2);
    {
      const bool up = lane >= 32;
      const int o = WS_QG + ((kvh * 4 + (up ? 1 : 0)) * 4 + rq) * 4096 + (32 * cq + (lane & 31)) * 16;
      st16_sc1_asm(v4u{__float_as_uint(up ? q0[1] : q0[0]), __float_as_uint(up ? q1[1] : q1[0]), tlo, thi}, o, srd_ws);
      st16_sc1_asm(v4u{__float_as_uint(up ? q0[3] : q0[2]), __float_as_uint(up ? q1[3] : q1[2]), tlo, thi}, o + 2 * 4 * 4096, srd_ws);
      if (lane == 0) __hip_atomic_store((GAS u64*)(ws + WS_RDY + c * 8), tag, __ATOMIC_RELAXED, __HIP_MEMORY_SCOPE_AGENT);
    }
    return;
  }

  const bool is_wo = i32 < 16, is_ffn = i32 >= 16 && i32 < 23, is_red = i32 >= 24;
  const int jf = kvh * 7 + (i32 - 16);
  const int rho = (i32 - 24) * 8 + kvh;
  const __amdgpu_buffer_rsrc_t rs = __builtin_amdgcn_make_buffer_rsrc(ws, 0, WS_END, 0x00020000);
  const int b = i32 >> 3, s = i32 & 7;
  const size_t kvbase = ((size_t)(b * HKV + kvh) * SK + (SK - WIN) + s * 256) * DH;
  const v4u srd_k = make_srd(kc + kvbase, 65536), srd_v = make_srd(vc + kvbase, 65536);
  const int rot = w + 37 * c;
  const int kv0 = g * 256 + ((n ^ g) * 16);
  const int vv0 = g * 256 + ((n ^ ((g << 2) | (w & 3))) * 16);
  const unsigned ldst = lbase + w * 1024;
  const int hp_off = WS_HP + (kvh * 4 + (i32 >> 2)) * 8192 + (i32 & 3) * 2048 + w * 256;
  const int qoff = WS_QG + ((kvh * 4 + b) * 4 + (lane >> 5)) * 4096 + (32 * w + (lane & 31)) * 16;
  asm volatile("" :: "s"(wo), "s"(wg), "s"(wu), "s"(wd), "s"(out), "s"(x));
  v2u ep;
  v4u qg[2];
  {
    const v4u sent = {SENT, SENT, SENT, SENT};
    const int o1 = is_wo ? (lane < 16 ? hp_off + lane * 16 : OOB) : is_ffn ? WS_OP + jf * 8192 + w * 1024 + lane * 16
                 : (is_red && w == 0 && lane < 4) ? WS_H + rho * 64 + lane * 16 : OOB;
    const int o0 = lane < 16 ? WS_PA + c * 2048 + w * 256 + lane * 16 : OOB;
    BARRIER();
    ld8_sc1_asm(ep, srd_ws);
    st16_sc1_asm(sent, o0, srd_ws);
    st16_sc1_asm(sent, o1, srd_ws);
  }
#define KLOAD(i) dma_buf<KV_NT>(ldst + L_K + (i) * 8192, kv0 ^ (((i) & 3) << 6), srd_k, ((8 * (i) + rot) & 63) << 10)
#define VLOAD(i) dma_buf<KV_NT>(ldst + L_V + (i) * 8192, vv0, srd_v, ((8 * (i) + rot) & 63) << 10)
  KLOAD(0); KLOAD(1); KLOAD(2); KLOAD(3);
  VMWAIT(3);
  STAMP(12);
  if (lane == 0) __hip_atomic_fetch_add((LAS unsigned*)(L + L_CNT), 1u, __ATOMIC_RELAXED, __HIP_MEMORY_SCOPE_WORKGROUP);
  KLOAD(4); VMWAIT(3); KLOAD(5); VMWAIT(3); KLOAD(6); VMWAIT(3); KLOAD(7);
  STAMP(1);
  VMWAIT(3); VLOAD(0); VMWAIT(3); VLOAD(1);
  ld16_sc1_asm(qg[0], qoff, srd_ws);
  ld16_sc1_asm(qg[1], qoff + 8192, srd_ws);
  VMWAIT(4); VLOAD(2); VMWAIT(4); VLOAD(3); VMWAIT(4); VLOAD(4);
  asm volatile("s_waitcnt vmcnt(3)" : "+v"(ep), "+v"(qg[0]), "+v"(qg[1]) :: "memory");
  STAMP(13);
  const u64 tag = (((u64)ep[1] << 32) | ep[0]) + 0x9E3779B97F4A7C15ull;
  const unsigned tlo = (unsigned)tag, thi = (unsigned)(tag >> 32);
  for (int it = 0; it < SPIN_CAP; ++it) {
    const bool ok = (qg[0][2] == tlo) & (qg[0][3] == thi) & (qg[1][2] == tlo) & (qg[1][3] == thi);
    if (__all(ok)) break;
    asm volatile("buffer_load_dwordx4 %0, %2, %3, 0 offen sc1\n\tbuffer_load_dwordx4 %1, %4, %3, 0 offen sc1\n\ts_waitcnt vmcnt(0)"
                 : "=&v"(qg[0]), "=&v"(qg[1]) : "v"(qoff), "s"(srd_ws), "v"(qoff + 8192) : "memory");
  }
  VLOAD(5); VLOAD(6); VLOAD(7);
  STAMP(2);
  {
    const float a = x32_sum(__uint_as_float(qg[0][0]) + __uint_as_float(qg[1][0]));
    const float bq = x32_sum(__uint_as_float(qg[0][1]) + __uint_as_float(qg[1][1]));
    if (lane < 32) *(LAS unsigned*)(L + L_Q + (w >> 1) * 320 + (w & 1) * 128 + lane * 4) = pk2(a, bq);
  }
  BARRIER();
  STAMP(3);
  float sc[2];
  {
    const int qb = L_Q + (2 * (n >> 3) + (n & 1)) * 320 + g * 16;
    const int kb = L_K + (n >> 2) * 8192 + w * 1024 + (n & 3) * 256 + ((g ^ n) * 16);
    f32x4 d0 = {0.f, 0.f, 0.f, 0.f}, d1 = {0.f, 0.f, 0.f, 0.f};
#pragma unroll
    for (int ks = 0; ks < 4; ++ks) {
      const v4u qa = *(LAS v4u*)(L + qb + ks * 64);
      d0 = mfma32(qa, *(LAS v4u*)(L + (kb ^ (ks << 6))), d0);
      d1 = mfma32(qa, *(LAS v4u*)(L + (kb ^ (ks << 6)) + 32768), d1);
    }
    f32x2 mx;
#pragma unroll
    for (int j = 0; j < 2; ++j) {
      sc[j] = rb(rb((g & 1) ? d1[j] : d0[j]) * 0.08838834764831845f);
      mx[j] = x16_max(row_max(sc[j]));
    }
    if ((lane & 31) == 0) *(LAS f32x2*)(L + L_RM + (g >> 1) * 64 + w * 8) = mx;
  }
  BARRIER();
  STAMP(14);
  const u16* gup = ((lane >> 2) & 1 ? wu : wg) + (size_t)(64 * w + (lane >> 3)) * FF + 32 * jf + 8 * ((lane & 3) ^ (g & 1));
  {
    const u16* p = is_wo ? wo + (size_t)((kvh * 4 + (i32 >> 2)) * 128 + 4 * w) * DM + (i32 & 3) * 128 + g * (DM - 128) + (vv0 >> 1) : is_ffn ? gup : x;
    const int st = is_wo ? 32 * DM : is_ffn ? 8 * FF : 0;
    const unsigned d0 = lbase + L_K + (is_wo ? w * 1024 : w * 8192), ds = is_wo ? 8192 : 1024;
    const int alt = is_ffn ? (((lane & 2) ? -16 : 16)) : 0;
#pragma unroll
    for (int i = 0; i < 4; ++i) {
      if (GU_NT == W2_NT) dma_glb<W2_NT>(d0 + i * ds, p + i * st + ((i & 1) ? alt : 0));
      else if (is_ffn) dma_glb<GU_NT>(d0 + i * ds, p + i * st + ((i & 1) ? alt : 0));
      else dma_glb<W2_NT>(d0 + i * ds, p + i * st + ((i & 1) ? alt : 0));
    }
  }
  STAMP(15);
  float mh;
  {
    f32x4 t[4];
#pragma unroll
    for (int i = 0; i < 4; ++i) t[i] = *(LAS f32x4*)(L + L_RM + (g >> 1) * 64 + i * 16);
    const float M0 = fmaxf(fmaxf(fmaxf(t[0][0], t[0][2]), fmaxf(t[1][0], t[1][2])), fmaxf(fmaxf(t[2][0], t[2][2]), fmaxf(t[3][0], t[3][2])));
    const float M1 = fmaxf(fmaxf(fmaxf(t[0][1], t[0][3]), fmaxf(t[1][1], t[1][3])), fmaxf(fmaxf(t[2][1], t[2][3]), fmaxf(t[3][1], t[3][3])));
    const float p0 = __builtin_amdgcn_exp2f((sc[0] - M0) * LOG2E), p1 = __builtin_amdgcn_exp2f((sc[1] - M1) * LOG2E);
    const int po = L_P + (g >> 1) * 1152 + ((4 * (g & 1) + (n >> 2)) * 32 + 4 * w + (n & 3)) * 2;
    const unsigned pp = pk2(p0, p1);
    *(LAS u16*)(L + po) = (u16)pp;
    *(LAS u16*)(L + po + 576) = (u16)(pp >> 16);
    const f32x2 ls = {x16_sum(row_sum(p0)), x16_sum(row_sum(p1))};
    if ((lane & 31) == 0) *(LAS f32x2*)(L + L_RL + (g >> 1) * 64 + w * 8) = ls;
    mh = (g & 1) ? M1 : M0;
  }
  STAMP(4);
  const int tq = n >> 2, tp = n & 3;
  const int tr0 = (8 * g + tq) * 256 + (((2 * w + (tp >> 1)) ^ ((tq << 2) | ((2 * g) & 3))) * 16) + 8 * (tp & 1);
  const int tr1 = (8 * g + 4 + tq) * 256 + (((2 * w + (tp >> 1)) ^ ((tq << 2) | ((2 * g + 1) & 3))) * 16) + 8 * (tp & 1);
  VMWAIT(6);
  BARRIER();
  {
    v4u pa[8];
#pragma unroll
    for (int i = 0; i < 8; ++i) pa[i] = *(LAS v4u*)(L + L_P + (n >> 2) * 576 + i * 64 + g * 16);
    f32x4 t[4];
#pragma unroll
    for (int i = 0; i < 4; ++i) t[i] = *(LAS f32x4*)(L + L_RL + (g >> 1) * 64 + i * 16);
    f32x4 acc = {0.f, 0.f, 0.f, 0.f};
#pragma unroll
    for (int i = 0; i < 6; ++i) acc = mfma32(pa[i], tr_frag(L, L_V + tr0 + i * 8192, L_V + tr1 + i * 8192), acc);
    const f32x4 ts = (t[0] + t[1]) + (t[2] + t[3]);
    unsigned ml[2] = {nosent(__float_as_uint(mh)), nosent(__float_as_uint((g & 1) ? ts[1] + ts[3] : ts[0] + ts[2]))};
    int pao = (n & 3) == 0 ? WS_PA + c * 2048 + w * 256 + g * 64 + (n >> 2) * 16 : OOB;
    PIN(ml[0]); PIN(ml[1]); PIN(pao); PIN(acc); PIN(pa[6]); PIN(pa[7]);
    VMWAIT(4);
    STAMP(5);
    BARRIER();
#pragma unroll
    for (int i = 6; i < 8; ++i) acc = mfma32(pa[i], tr_frag(L, L_V + tr0 + i * 8192, L_V + tr1 + i * 8192), acc);
    const unsigned d0 = nosent(pk2(acc[0], dpp<SHL1>(acc[0])));
    st_sc1(rs, pao, v4u{d0, dppu<SHL2>(d0), ml[0], ml[1]});
  }
  STAMP(6);

  if (is_wo) {
    const int hh = i32 >> 2;
    int off[2];
#pragma unroll
    for (int i = 0; i < 2; ++i) off[i] = WS_PA + (kvh + 8 * (8 * (n & 3) + 2 * (n >> 2) + i)) * 2048 + w * 256 + hh * 64 + g * 16;
    VMWAIT(1);
    BARRIER();
    v4u bf[4];
#pragma unroll
    for (int i = 0; i < 4; ++i) { bf[i] = tr_frag(L, L_K + tr0 + i * 8192, L_K + tr1 + i * 8192); PIN(bf[i]); }
    STAMP(7);
    v4u pv[2];
    poll_fresh<2>(rs, off, pv);
    STAMP(8);
    const float m0 = __uint_as_float(pv[0][2]), m1 = __uint_as_float(pv[1][2]);
    float mm = vmax(m0, m1);
    mm = vmax(mm, dpp<ROR4>(mm)); mm = vmax(mm, dpp<ROR8>(mm));
    const float e0 = __builtin_amdgcn_exp2f((m0 - mm) * LOG2E), e1 = __builtin_amdgcn_exp2f((m1 - mm) * LOG2E);
    float t[5];
    t[0] = e0 * bflo(pv[0][0]) + e1 * bflo(pv[1][0]);
    t[1] = e0 * bfhi(pv[0][0]) + e1 * bfhi(pv[1][0]);
    t[2] = e0 * bflo(pv[0][1]) + e1 * bflo(pv[1][1]);
    t[3] = e0 * bfhi(pv[0][1]) + e1 * bfhi(pv[1][1]);
    t[4] = e0 * __uint_as_float(pv[0][3]) + e1 * __uint_as_float(pv[1][3]);
#pragma unroll
    for (int i = 0; i < 5; ++i) { t[i] += dpp<ROR4>(t[i]); t[i] += dpp<ROR8>(t[i]); }
    const float inv = __builtin_amdgcn_rcpf(t[4]);
    if (n < 4) *(LAS v2u*)(L + L_O + n * 320 + w * 32 + g * 8) = v2u{pk2(t[0] * inv, t[1] * inv), pk2(t[2] * inv, t[3] * inv)};
    BARRIER();
    f32x4 acc = {0.f, 0.f, 0.f, 0.f};
#pragma unroll
    for (int i = 0; i < 4; ++i) acc = mfma32(*(LAS v4u*)(L + L_O + (n & 3) * 320 + i * 64 + g * 16), bf[i], acc);
    st_sc1(rs, lane < 16 ? hp_off + lane * 16 : OOB, nosent4(acc));
    STAMP(9);
  } else if (is_ffn) {
    {
      const int alt = (lane & 2) ? -16 : 16;
#pragma unroll
      for (int i = 4; i < 8; ++i) dma_glb<GU_NT>(lbase + L_K + w * 8192 + i * 1024, gup + i * 8 * FF + ((i & 1) ? alt : 0));
    }
    BARRIER();
    {
      const v4u srd_d = make_srd(wd + (size_t)(32 * jf + 4 * w) * DM, 4 * DM * 2);
#pragma unroll
      for (int i = 0; i < 4; ++i) dma_buf<W2_NT>(ldst + L_V + i * 8192, vv0 + g * (DM * 2 - 256), srd_d, i * 256);
    }
    if (w < 4) wait_ready(rs, lane, tlo, thi);
    VMWAIT(0);
    BARRIER();
    v4u bf[16], bd[4];
    {
      const int q2 = L_V + (w >> 1) * 8192, x0 = ((8 * (w & 1)) ^ (2 * w)) << 4;
#pragma unroll
      for (int i = 0; i < 4; ++i) { bd[i] = tr_frag(L, q2 + (tr0 ^ x0 ^ (i << 5)), q2 + (tr1 ^ x0 ^ (i << 5))); PIN(bd[i]); }
    }
    int ao[4];
#pragma unroll
    for (int i = 0; i < 4; ++i) { ao[i] = (n >> 2) == i ? L_HH + (n & 3) * 64 + g * 16 : L_ZERO; PIN(ao[i]); }
    if (w < 4) {
      const int z = ((tq >> 1) & 1) | ((g & 1) << 1);
      const int o = L_K + (8 * g + tq) * 128 + ((4 * (tp >> 1) + (w ^ z)) * 16) + 8 * (tp & 1);
#pragma unroll
      for (int i = 0; i < 16; ++i) { bf[i] = tr_frag(L, o + i * 4096, o + i * 4096 + 512); PIN(bf[i]); }
      STAMP(7);
      const int off[1] = {WS_H + lane * 64 + w * 16};
      v4u hv[1];
      poll_fresh<1>(rs, off, hv);
      STAMP(8);
      float v[8];
#pragma unroll
      for (int k = 0; k < 4; ++k) { v[2 * k] = bflo(hv[0][k]); v[2 * k + 1] = bfhi(hv[0][k]); }
      const float mean = wave_sum(((v[0] + v[1]) + (v[2] + v[3])) + ((v[4] + v[5]) + (v[6] + v[7]))) * (1.0f / DM);
      float q0 = 0.f, q1 = 0.f;
#pragma unroll
      for (int k = 0; k < 8; k += 2) { const float d0 = v[k] - mean, d1 = v[k + 1] - mean; q0 = fmaf(d0, d0, q0); q1 = fmaf(d1, d1, q1); }
      const float q = wave_sum(q0 + q1);
      const float mb = rb(mean);
      const float inv = __builtin_amdgcn_rcpf(rb(__builtin_amdgcn_sqrtf(rb(rb(q * (1.0f / DM)) + 1e-5f))));
      v4u o4;
#pragma unroll
      for (int k = 0; k < 4; ++k) o4[k] = pk2(rb(v[2 * k] - mb) * inv, rb(v[2 * k + 1] - mb) * inv);
      *(LAS v4u*)(L + L_HN + w * 1088 + lane * 16) = o4;
    }
    BARRIER();
    if (w < 4) {
      f32x4 acc = {0.f, 0.f, 0.f, 0.f};
#pragma unroll
      for (int i = 0; i < 16; ++i) acc = mfma32(*(LAS v4u*)(L + L_HN + (n >> 2) * 1088 + i * 64 + g * 16), bf[i], acc);
      const float a = rb(acc[0]), u = dpp<ROR8>(a);
      const float sg = rb(__builtin_amdgcn_rcpf(1.0f + __builtin_amdgcn_exp2f(a * -LOG2E)));
      if (n < 8) *(LAS u16*)(L + L_HH + g * 64 + (8 * w + n) * 2) = (u16)pk2(sg * u, 0.f);
    }
    BARRIER();
    {
      f32x4 acc = {0.f, 0.f, 0.f, 0.f};
#pragma unroll
      for (int i = 0; i < 4; ++i) acc = mfma32(*(LAS v4u*)(L + ao[i]), bd[i], acc);
      st_sc1(rs, WS_OP + jf * 8192 + w * 1024 + lane * 16, nosent4(acc));
    }
    STAMP(9);
  } else if (is_red && w == 0) {
    const int r2 = lane >> 3, k = lane & 7, bb = r2 & 3;
    const float xv = bflo(x[bb * DM + 8 * rho + k]);
    int off1[4], off2[7];
#pragma unroll
    for (int i = 0; i < 4; ++i) off1[i] = WS_HP + (8 * i + r2) * 8192 + (8 * rho + k) * 16;
#pragma unroll
    for (int i = 0; i < 7; ++i) off2[i] = WS_OP + (8 * i + r2) * 8192 + (8 * rho + k) * 16;
    wait_ready(rs, lane, tlo, thi);
    STAMP(7);
    float hval;
    {
      v4u pv[4];
      poll_fresh<4>(rs, off1, pv);
      STAMP(8);
      f32x4 a = (__builtin_bit_cast(f32x4, pv[0]) + __builtin_bit_cast(f32x4, pv[1])) + (__builtin_bit_cast(f32x4, pv[2]) + __builtin_bit_cast(f32x4, pv[3]));
#pragma unroll
      for (int i = 0; i < 4; ++i) { a[i] += dpp<ROR8>(a[i]); a[i] = x32_sum(x16_sum(a[i])); }
      const float av = (bb & 2) ? ((bb & 1) ? a[3] : a[2]) : ((bb & 1) ? a[1] : a[0]);
      hval = rb(xv + rb(av));
      const unsigned dw = nosent(pk2(hval, dpp<SHL1>(hval)));
      const unsigned dw2 = dppu<SHL2>(dw);
      const unsigned dw4 = dppu<SHL4>(dw), dw6 = dppu<SHL4>(dw2);
      st_sc1(rs, (k == 0 && lane < 32) ? WS_H + rho * 64 + bb * 16 : OOB, v4u{dw, dw2, dw4, dw6});
    }
    STAMP(9);
    {
      v4u pv[7];
      poll_fresh<7>(rs, off2, pv);
      STAMP(10);
      f32x4 a = ((__builtin_bit_cast(f32x4, pv[0]) + __builtin_bit_cast(f32x4, pv[1])) + (__builtin_bit_cast(f32x4, pv[2]) + __builtin_bit_cast(f32x4, pv[3]))) +
                ((__builtin_bit_cast(f32x4, pv[4]) + __builtin_bit_cast(f32x4, pv[5])) + __builtin_bit_cast(f32x4, pv[6]));
#pragma unroll
      for (int i = 0; i < 4; ++i) { a[i] += dpp<ROR8>(a[i]); a[i] = x32_sum(x16_sum(a[i])); }
      const float av = (bb & 2) ? ((bb & 1) ? a[3] : a[2]) : ((bb & 1) ? a[1] : a[0]);
      const float ov = hval + rb(av);
      const unsigned dw = pk2(ov, dpp<SHL1>(ov));
      const unsigned dw2 = dppu<SHL2>(dw);
      const unsigned dw4 = dppu<SHL4>(dw), dw6 = dppu<SHL4>(dw2);
      if (k == 0 && lane < 32) *(GAS v4u*)(out + bb * DM + 8 * rho) = v4u{dw, dw2, dw4, dw6};
      if (c == 192 && lane == 0) __hip_atomic_store((GAS u64*)(ws + WS_EPOCH), tag, __ATOMIC_RELAXED, __HIP_MEMORY_SCOPE_AGENT);
    }
    STAMP(11);
  }
  STAMP_OUT();
}

}

extern "C" void kernel_launch(void* const* d_in, const int* in_sizes, int n_in, void* d_out, int out_size, void* d_ws, size_t ws_size,
                              hipStream_t stream) {
  (void)in_sizes; (void)n_in; (void)out_size; (void)ws_size;
  fused_block<<<256, NTHR, 0, stream>>>((const u16*)d_in[0], (const u16*)d_in[1], (const u16*)d_in[2], (const u16*)d_in[3], (const u16*)d_in[4],
                                        (const u16*)d_in[6], (const u16*)d_in[7], (const u16*)d_in[8], (u16*)d_out, (unsigned char*)d_ws);
}
```

```cpp
#include <hip/hip_runtime.h>
#include <hip/hip_bf16.h>
#include <stdint.h>

namespace {

typedef unsigned short u16;
typedef unsigned long long u64;
typedef unsigned v4u __attribute__((ext_vector_type(4)));
typedef unsigned v2u __attribute__((ext_vector_type(2)));
typedef float f32x4 __attribute__((ext_vector_type(4)));
typedef float f32x2 __attribute__((ext_vector_type(2)));
typedef short bf16x8 __attribute__((ext_vector_type(8)));
typedef short s16x4 __attribute__((ext_vector_type(4)));
typedef __bf16 bf2 __attribute__((ext_vector_type(2)));
#define LAS __attribute__((address_space(3)))
#define GAS __attribute__((address_space(1)))

constexpr int DM = 512, HKV = 8, SK = 8192, DH = 128, WIN = 2048, FF = 1792, NQ = 4096;
constexpr int NTHR = 576;

constexpr int WS_EPOCH = 0;
constexpr int WS_RDY = 4096;
constexpr int WS_QG = 8192;
constexpr int WS_PA = WS_QG + 512 * 1024;
constexpr int WS_HP = WS_PA + 512 * 1024;
constexpr int WS_H = WS_HP + 256 * 1024;
constexpr int WS_OP = WS_H + 4096;
constexpr int WS_END = WS_OP + 56 * 8192;
constexpr int OOB = 0x40000000;

constexpr int L_K = 0;
constexpr int L_V = 65536;
constexpr int L_WQ = 131072;
constexpr int L_HN = L_WQ;
constexpr int L_Q = L_WQ + 16384;
constexpr int L_XN = L_Q + 1280;
constexpr int L_O = L_XN;
constexpr int L_QP = L_XN + 1280;
constexpr int L_P = L_QP + 1024;
constexpr int L_RM = L_P + 2304;
constexpr int L_RL = L_RM + 128;
constexpr int L_HH = L_RL + 128;
constexpr int L_ZERO = L_HH + 256;
constexpr int L_CNT = L_ZERO + 16;
constexpr int L_BYTES = L_CNT + 16;

constexpr unsigned SENT = 0xFFFFFFFFu;
constexpr int SPIN_CAP = 1 << 16;
constexpr int AUX_SC1 = 16;
constexpr float LOG2E = 1.4426950408889634f;

#define KV_NT 1
#define WQ_NT 1
#define W2_NT 1
#define GU_NT 0

__device__ __forceinline__ float bflo(unsigned x) { return __builtin_bit_cast(float, x << 16); }
__device__ __forceinline__ float bfhi(unsigned x) { return __builtin_bit_cast(float, x & 0xffff0000u); }
__device__ __forceinline__ unsigned pk2(float lo, float hi) {
  bf2 v = {(__bf16)lo, (__bf16)hi};
  return __builtin_bit_cast(unsigned, v);
}
__device__ __forceinline__ float rb(float x) { return (float)(__bf16)x; }
__device__ __forceinline__ unsigned nosent(unsigned x) { return x < 0xFFFFFFFEu ? x : 0xFFFFFFFEu; }
__device__ __forceinline__ v4u nosent4(f32x4 v) {
  return v4u{nosent(__float_as_uint(v[0])), nosent(__float_as_uint(v[1])), nosent(__float_as_uint(v[2])), nosent(__float_as_uint(v[3]))};
}
__device__ __forceinline__ bool fresh(v4u v) { return max(max(v[0], v[1]), max(v[2], v[3])) != SENT; }
__device__ __forceinline__ float vmax(float a, float b) { float r; asm("v_max_f32 %0, %1, %2" : "=v"(r) : "v"(a), "v"(b)); return r; }

__device__ __forceinline__ f32x4 mfma32(v4u a, v4u b, f32x4 c) {
  return __builtin_amdgcn_mfma_f32_16x16x32_bf16(__builtin_bit_cast(bf16x8, a), __builtin_bit_cast(bf16x8, b), c, 0, 0, 0);
}
template <int CTRL> __device__ __forceinline__ float dpp(float x) {
  return __builtin_bit_cast(float, __builtin_amdgcn_mov_dpp(__builtin_bit_cast(int, x), CTRL, 0xf, 0xf, true));
}
template <int CTRL> __device__ __forceinline__ unsigned dppu(unsigned x) { return (unsigned)__builtin_amdgcn_mov_dpp((int)x, CTRL, 0xf, 0xf, true); }
constexpr int ROR8 = 0x128, ROR4 = 0x124, ROR2 = 0x122, ROR1 = 0x121, SHL1 = 0x101, SHL2 = 0x102, SHL4 = 0x104;
__device__ __forceinline__ float row_max(float x) { x = fmaxf(x, dpp<ROR8>(x)); x = fmaxf(x, dpp<ROR4>(x)); x = fmaxf(x, dpp<ROR2>(x)); return fmaxf(x, dpp<ROR1>(x)); }
__device__ __forceinline__ float row_sum(float x) { x += dpp<ROR8>(x); x += dpp<ROR4>(x); x += dpp<ROR2>(x); return x + dpp<ROR1>(x); }
__device__ __forceinline__ float x16_max(float x) { auto s = __builtin_amdgcn_permlane16_swap(__float_as_uint(x), __float_as_uint(x), false, false); return fmaxf(__uint_as_float(s[0]), __uint_as_float(s[1])); }
__device__ __forceinline__ float x16_sum(float x) { auto s = __builtin_amdgcn_permlane16_swap(__float_as_uint(x), __float_as_uint(x), false, false); return __uint_as_float(s[0]) + __uint_as_float(s[1]); }
__device__ __forceinline__ float x32_sum(float x) { auto s = __builtin_amdgcn_permlane32_swap(__float_as_uint(x), __float_as_uint(x), false, false); return __uint_as_float(s[0]) + __uint_as_float(s[1]); }
__device__ __forceinline__ float wave_sum(float x) { return x32_sum(x16_sum(row_sum(x))); }
__device__ __forceinline__ f32x4 sel2(bool c, f32x4 a, f32x4 b) {
  return f32x4{c ? b[0] : a[0], c ? b[1] : a[1], c ? b[2] : a[2], c ? b[3] : a[3]};
}
__device__ __forceinline__ f32x4 sel4(int g, f32x4 a, f32x4 b, f32x4 c, f32x4 d) { return sel2(g & 2, sel2(g & 1, a, b), sel2(g & 1, c, d)); }

#define PIN(x) asm volatile("" : "+v"(x))
#define SCHED() __builtin_amdgcn_sched_barrier(0)
#define LDS_FENCE() asm volatile("s_waitcnt lgkmcnt(0)" ::: "memory")
#define BARRIER() do { asm volatile("s_waitcnt lgkmcnt(0)\n\ts_barrier" ::: "memory"); } while (0)
#define VMWAIT(N) asm volatile("s_waitcnt vmcnt(" #N ")" ::: "memory")
#define STAMP(i) do {} while (0)
#define STAMP_OUT() do {} while (0)

__device__ __forceinline__ v4u make_srd(const void* p, unsigned bytes) {
  const u64 a = (u64)p;
  return v4u{(unsigned)a, (unsigned)(a >> 32) & 0xffffu, bytes, 0x00020000u};
}
template <int NT> __device__ __forceinline__ void dma_buf(unsigned lds, int voff, v4u srd, int soff) {
  if (NT) asm volatile("s_mov_b32 m0, %0\n\ts_nop 0\n\tbuffer_load_dwordx4 %1, %2, %3 offen nt lds" :: "s"(lds), "v"(voff), "s"(srd), "s"(soff) : "m0", "memory");
  else asm volatile("s_mov_b32 m0, %0\n\ts_nop 0\n\tbuffer_load_dwordx4 %1, %2, %3 offen lds" :: "s"(lds), "v"(voff), "s"(srd), "s"(soff) : "m0", "memory");
}
template <int NT> __device__ __forceinline__ void dma_glb(unsigned lds, const void* p) {
  if (NT) asm volatile("s_mov_b32 m0, %0\n\ts_nop 0\n\tglobal_load_lds_dwordx4 %1, off nt" :: "s"(lds), "v"(p) : "m0", "memory");
  else asm volatile("s_mov_b32 m0, %0\n\ts_nop 0\n\tglobal_load_lds_dwordx4 %1, off" :: "s"(lds), "v"(p) : "m0", "memory");
}
__device__ __forceinline__ void ld16_asm(v4u& d, int voff, v4u srd, int soff) {
  asm volatile("buffer_load_dwordx4 %0, %1, %2, %3 offen" : "=v"(d) : "v"(voff), "s"(srd), "s"(soff) : "memory");
}
__device__ __forceinline__ void ld16_sc1_asm(v4u& d, int voff, v4u srd) {
  asm volatile("buffer_load_dwordx4 %0, %1, %2, 0 offen sc1" : "=v"(d) : "v"(voff), "s"(srd) : "memory");
}
__device__ __forceinline__ void ld8_sc1_asm(v2u& d, v4u srd) {
  asm volatile("buffer_load_dwordx2 %0, off, %1, 0 sc1" : "=v"(d) : "s"(srd) : "memory");
}
__device__ __forceinline__ void st16_sc1_asm(v4u v, int voff, v4u srd) {
  asm volatile("buffer_store_dwordx4 %0, %1, %2, 0 offen sc1\n\ts_nop 1" :: "v"(v), "v"(voff), "s"(srd) : "memory");
}

__device__ __forceinline__ v4u ld_sc1(__amdgpu_buffer_rsrc_t rs, int off) { return __builtin_amdgcn_raw_buffer_load_b128(rs, off, 0, AUX_SC1); }
__device__ __forceinline__ void st_sc1(__amdgpu_buffer_rsrc_t rs, int off, v4u v) { __builtin_amdgcn_raw_buffer_store_b128(v, rs, off, 0, AUX_SC1); }
__device__ __forceinline__ v2u tr_rd(LAS unsigned char* L, int off) {
  return __builtin_bit_cast(v2u, __builtin_amdgcn_ds_read_tr16_b64_v4i16((LAS s16x4*)(L + off)));
}
__device__ __forceinline__ v4u tr_frag(LAS unsigned char* L, int off0, int off1) {
  const v2u a = tr_rd(L, off0), b = tr_rd(L, off1);
  return v4u{a[0], a[1], b[0], b[1]};
}

template <int N> __device__ __forceinline__ void poll_fresh(__amdgpu_buffer_rsrc_t rs, const int (&off)[N], v4u (&v)[N]) {
  for (int it = 0; it < SPIN_CAP; ++it) {
    asm volatile("" ::: "memory");
    bool ok = true;
#pragma unroll
    for (int i = 0; i < N; ++i) v[i] = ld_sc1(rs, off[i]);
#pragma unroll
    for (int i = 0; i < N; ++i) ok &= fresh(v[i]);
    if (__all(ok)) break;
  }
}
__device__ __forceinline__ void wait_ready(__amdgpu_buffer_rsrc_t rs, int lane, unsigned tlo, unsigned thi) {
  for (int it = 0; it < SPIN_CAP; ++it) {
    asm volatile("" ::: "memory");
    const v4u r0 = ld_sc1(rs, WS_RDY + lane * 16), r1 = ld_sc1(rs, WS_RDY + 1024 + lane * 16);
    const bool ok = (r0[0] == tlo) & (r0[1] == thi) & (r0[2] == tlo) & (r0[3] == thi) & (r1[0] == tlo) & (r1[1] == thi) & (r1[2] == tlo) & (r1[3] == thi);
    if (__all(ok)) break;
  }
}

__global__ void __launch_bounds__(NTHR, 3)
fused_block(const u16* __restrict__ x, const u16* __restrict__ kc, const u16* __restrict__ vc, const u16* __restrict__ wq,
            const u16* __restrict__ wo, const u16* __restrict__ wg, const u16* __restrict__ wu, const u16* __restrict__ wd,
            u16* __restrict__ out, unsigned char* __restrict__ ws) {
  __shared__ __attribute__((aligned(16))) unsigned char smem_[L_BYTES];
  LAS unsigned char* L = (LAS unsigned char*)smem_;
  const unsigned lbase = (unsigned)(size_t)L;
  const int c = blockIdx.x, kvh = c & 7, i32 = c >> 3;
  const int lane = threadIdx.x & 63, wv = __builtin_amdgcn_readfirstlane(threadIdx.x >> 6);
  const int w = wv == 0 ? 8 : wv - 1;
  const int n = lane & 15, g = lane >> 4;
  const v4u srd_ws = make_srd(ws, WS_END);
  STAMP(0);

  if (wv == 0) {
    const int rq = i32 >> 3, cq = i32 & 7;
    const v4u srd_x = make_srd(x, 4 * DM * 2);
    const v4u srd_wq = make_srd(wq + (size_t)rq * 128 * NQ + kvh * 512 + cq * 64, 128 * NQ * 2);
    v2u ep;
    v4u xr[4];
    ld8_sc1_asm(ep, srd_ws);
#pragma unroll
    for (int i = 0; i < 4; ++i) ld16_asm(xr[i], g * 1024 + n * 16, srd_x, ((rq + i) & 3) * 256);
    {
      const int row = lane >> 3, v0 = row * (NQ * 2) + (((lane & 7) ^ (2 * ((row >> 1) & 1))) * 16);
#pragma unroll
      for (int i = 0; i < 16; ++i) dma_buf<WQ_NT>(lbase + L_WQ + i * 1024, (i & 1) ? (v0 ^ 64) : v0, srd_wq, i * 8 * NQ * 2);
    }
    if (lane == 0) { *(LAS v4u*)(L + L_ZERO) = v4u{0u, 0u, 0u, 0u}; *(LAS unsigned*)(L + L_CNT) = 0u; }
    BARRIER();
    asm volatile("s_waitcnt vmcnt(16)" : "+v"(ep), "+v"(xr[0]), "+v"(xr[1]), "+v"(xr[2]), "+v"(xr[3]) :: "memory");
    {
      float v[32], s = 0.f;
#pragma unroll
      for (int i = 0; i < 4; ++i)
#pragma unroll
        for (int k = 0; k < 4; ++k) { v[8 * i + 2 * k] = bflo(xr[i][k]); v[8 * i + 2 * k + 1] = bfhi(xr[i][k]); }
#pragma unroll
      for (int k = 0; k < 32; k += 4) s += (v[k] + v[k + 1]) + (v[k + 2] + v[k + 3]);
      const float mean = row_sum(s) * (1.0f / DM);
      float q0 = 0.f, q1 = 0.f;
#pragma unroll
      for (int k = 0; k < 32; k += 2) { const float d0 = v[k] - mean, d1 = v[k + 1] - mean; q0 = fmaf(d0, d0, q0); q1 = fmaf(d1, d1, q1); }
      const float q = row_sum(q0 + q1);
      const float mb = rb(mean);
      const float inv = __builtin_amdgcn_rcpf(rb(__builtin_amdgcn_sqrtf(rb(rb(q * (1.0f / DM)) + 1e-5f))));
      v4u o;
#pragma unroll
      for (int k = 0; k < 4; ++k) o[k] = pk2(rb(v[2 * k] - mb) * inv, rb(v[2 * k + 1] - mb) * inv);
      *(LAS v4u*)(L + L_XN + g * 320 + n * 16) = o;
    }
    STAMP(1);
    asm volatile("s_waitcnt vmcnt(0) lgkmcnt(0)" ::: "memory");
    STAMP(4);
    f32x4 acc[4];
    {
      v4u xa[4];
#pragma unroll
      for (int ks = 0; ks < 4; ++ks) xa[ks] = *(LAS v4u*)(L + L_XN + (n & 3) * 320 + ks * 64 + g * 16);
      const int q4 = n >> 2, p = n & 3, y = ((q4 >> 1) & 1) | ((g & 1) << 1);
      const int rbase = L_WQ + (8 * g + q4) * 128 + 8 * (p & 1);
#pragma unroll
      for (int t = 0; t < 4; ++t) {
        const int o = rbase + (((2 * t + (p >> 1)) ^ (2 * y)) * 16);
        acc[t] = f32x4{0.f, 0.f, 0.f, 0.f};
#pragma unroll
        for (int ks = 0; ks < 4; ++ks) acc[t] = mfma32(xa[ks], tr_frag(L, o + ks * 4096, o + ks * 4096 + 512), acc[t]);
      }
    }
    *(LAS f32x4*)(L + L_QP + lane * 16) = sel4(g, acc[0], acc[1], acc[2], acc[3]);
    LDS_FENCE();
    const f32x4 q0 = *(LAS f32x4*)(L + L_QP + (lane & 31) * 32), q1 = *(LAS f32x4*)(L + L_QP + (lane & 31) * 32 + 16);
    const u64 tag = (((u64)ep[1] << 32) | ep[0]) + 0x9E3779B97F4A7C15ull;
    const unsigned tlo = (unsigned)tag, thi = (unsigned)(tag >> 32);
    STAMP(5);
    for (int it = 0; it < (SPIN_CAP << 4); ++it) {
      if (__builtin_amdgcn_readfirstlane(*(volatile LAS unsigned*)(L + L_CNT)) >= 8u) break;
    }
    STAMP(2);
    {
      const bool up = lane >= 32;
      const int o = WS_QG + ((kvh * 4 + (up ? 1 : 0)) * 4 + rq) * 4096 + (32 * cq + (lane & 31)) * 16;
      st16_sc1_asm(v4u{__float_as_uint(up ? q0[1] : q0[0]), __float_as_uint(up ? q1[1] : q1[0]), tlo, thi}, o, srd_ws);
      st16_sc1_asm(v4u{__float_as_uint(up ? q0[3] : q0[2]), __float_as_uint(up ? q1[3] : q1[2]), tlo, thi}, o + 2 * 4 * 4096, srd_ws);
      if (lane == 0) __hip_atomic_store((GAS u64*)(ws + WS_RDY + c * 8), tag, __ATOMIC_RELAXED, __HIP_MEMORY_SCOPE_AGENT);
    }
    STAMP(3);
    STAMP_OUT();
    return;
  }

  const bool is_wo = i32 < 16, is_ffn = i32 >= 16 && i32 < 23, is_red = i32 >= 24;
  const int jf = kvh * 7 + (i32 - 16);
  const int rho = (i32 - 24) * 8 + kvh;
  const __amdgpu_buffer_rsrc_t rs = __builtin_amdgcn_make_buffer_rsrc(ws, 0, WS_END, 0x00020000);
  const int b = i32 >> 3, s = i32 & 7;
  const size_t kvbase = ((size_t)(b * HKV + kvh) * SK + (SK - WIN) + s * 256) * DH;
  const v4u srd_k = make_srd(kc + kvbase, 65536), srd_v = make_srd(vc + kvbase, 65536);
  const int rot = w + 37 * c;
  const int kv0 = g * 256 + ((n ^ g) * 16);
  const int vv0 = g * 256 + ((n ^ ((g << 2) | (w & 3))) * 16);
  const unsigned ldst = lbase + w * 1024;
  const int hp_off = WS_HP + (kvh * 4 + (i32 >> 2)) * 8192 + (i32 & 3) * 2048 + w * 256;
  const int qoff = WS_QG + ((kvh * 4 + b) * 4 + (lane >> 5)) * 4096 + (32 * w + (lane & 31)) * 16;
  asm volatile("" :: "s"(wo), "s"(wg), "s"(wu), "s"(wd), "s"(out), "s"(x));
  v2u ep;
  v4u qg[2];
  {
    const v4u sent = {SENT, SENT, SENT, SENT};
    const int o1 = is_wo ? (lane < 16 ? hp_off + lane * 16 : OOB) : is_ffn ? WS_OP + jf * 8192 + w * 1024 + lane * 16
                 : (is_red && w == 0 && lane < 4) ? WS_H + rho * 64 + lane * 16 : OOB;
    const int o0 = lane < 16 ? WS_PA + c * 2048 + w * 256 + lane * 16 : OOB;
    BARRIER();
    ld8_sc1_asm(ep, srd_ws);
    st16_sc1_asm(sent, o0, srd_ws);
    st16_sc1_asm(sent, o1, srd_ws);
  }
#define KLOAD(i) dma_buf<KV_NT>(ldst + L_K + (i) * 8192, kv0 ^ (((i) & 3) << 6), srd_k, ((8 * (i) + rot) & 63) << 10)
#define VLOAD(i) dma_buf<KV_NT>(ldst + L_V + (i) * 8192, vv0, srd_v, ((8 * (i) + rot) & 63) << 10)
  KLOAD(0); KLOAD(1); KLOAD(2); KLOAD(3);
  VMWAIT(3);
  STAMP(12);
  if (lane == 0) __hip_atomic_fetch_add((LAS unsigned*)(L + L_CNT), 1u, __ATOMIC_RELAXED, __HIP_MEMORY_SCOPE_WORKGROUP);
  KLOAD(4); VMWAIT(3); KLOAD(5); VMWAIT(3); KLOAD(6); VMWAIT(3); KLOAD(7);
  STAMP(1);
  VMWAIT(3); VLOAD(0); VMWAIT(3); VLOAD(1);
  ld16_sc1_asm(qg[0], qoff, srd_ws);
  ld16_sc1_asm(qg[1], qoff + 8192, srd_ws);
  VMWAIT(4); VLOAD(2); VMWAIT(4); VLOAD(3); VMWAIT(4); VLOAD(4);
  asm volatile("s_waitcnt vmcnt(3)" : "+v"(ep), "+v"(qg[0]), "+v"(qg[1]) :: "memory");
  STAMP(13);
  const u64 tag = (((u64)ep[1] << 32) | ep[0]) + 0x9E3779B97F4A7C15ull;
  const unsigned tlo = (unsigned)tag, thi = (unsigned)(tag >> 32);
  for (int it = 0; it < SPIN_CAP; ++it) {
    const bool ok = (qg[0][2] == tlo) & (qg[0][3] == thi) & (qg[1][2] == tlo) & (qg[1][3] == thi);
    if (__all(ok)) break;
    asm volatile("buffer_load_dwordx4 %0, %2, %3, 0 offen sc1\n\tbuffer_load_dwordx4 %1, %4, %3, 0 offen sc1\n\ts_waitcnt vmcnt(0)"
                 : "=&v"(qg[0]), "=&v"(qg[1]) : "v"(qoff), "s"(srd_ws), "v"(qoff + 8192) : "memory");
  }
  VLOAD(5); VLOAD(6); VLOAD(7);
  STAMP(2);
  {
    const float a = x32_sum(__uint_as_float(qg[0][0]) + __uint_as_float(qg[1][0]));
    const float bq = x32_sum(__uint_as_float(qg[0][1]) + __uint_as_float(qg[1][1]));
    if (lane < 32) *(LAS unsigned*)(L + L_Q + (w >> 1) * 320 + (w & 1) * 128 + lane * 4) = pk2(a, bq);
  }
  BARRIER();
  STAMP(3);
  float sc[2];
  {
    const int qb = L_Q + (2 * (n >> 3) + (n & 1)) * 320 + g * 16;
    const int kb = L_K + (n >> 2) * 8192 + w * 1024 + (n & 3) * 256 + ((g ^ n) * 16);
    f32x4 d0 = {0.f, 0.f, 0.f, 0.f}, d1 = {0.f, 0.f, 0.f, 0.f};
#pragma unroll
    for (int ks = 0; ks < 4; ++ks) {
      const v4u qa = *(LAS v4u*)(L + qb + ks * 64);
      d0 = mfma32(qa, *(LAS v4u*)(L + (kb ^ (ks << 6))), d0);
      d1 = mfma32(qa, *(LAS v4u*)(L + (kb ^ (ks << 6)) + 32768), d1);
    }
    f32x2 mx;
#pragma unroll
    for (int j = 0; j < 2; ++j) {
      sc[j] = rb(rb((g & 1) ? d1[j] : d0[j]) * 0.08838834764831845f);
      mx[j] = x16_max(row_max(sc[j]));
    }
    if ((lane & 31) == 0) *(LAS f32x2*)(L + L_RM + (g >> 1) * 64 + w * 8) = mx;
  }
  BARRIER();
  STAMP(14);
  const u16* gup = ((lane >> 2) & 1 ? wu : wg) + (size_t)(64 * w + (lane >> 3)) * FF + 32 * jf + 8 * ((lane & 3) ^ (g & 1));
  {
    const u16* p = is_wo ? wo + (size_t)((kvh * 4 + (i32 >> 2)) * 128 + 4 * w) * DM + (i32 & 3) * 128 + g * (DM - 128) + (vv0 >> 1) : is_ffn ? gup : x;
    const int st = is_wo ? 32 * DM : is_ffn ? 8 * FF : 0;
    const unsigned d0 = lbase + L_K + (is_wo ? w * 1024 : w * 8192), ds = is_wo ? 8192 : 1024;
    const int alt = is_ffn ? (((lane & 2) ? -16 : 16)) : 0;
#pragma unroll
    for (int i = 0; i < 4; ++i) {
      if (GU_NT == W2_NT) dma_glb<W2_NT>(d0 + i * ds, p + i * st + ((i & 1) ? alt : 0));
      else if (is_ffn) dma_glb<GU_NT>(d0 + i * ds, p + i * st + ((i & 1) ? alt : 0));
      else dma_glb<W2_NT>(d0 + i * ds, p + i * st + ((i & 1) ? alt : 0));
    }
  }
  STAMP(15);
  float mh;
  {
    f32x4 t[4];
#pragma unroll
    for (int i = 0; i < 4; ++i) t[i] = *(LAS f32x4*)(L + L_RM + (g >> 1) * 64 + i * 16);
    const float M0 = fmaxf(fmaxf(fmaxf(t[0][0], t[0][2]), fmaxf(t[1][0], t[1][2])), fmaxf(fmaxf(t[2][0], t[2][2]), fmaxf(t[3][0], t[3][2])));
    const float M1 = fmaxf(fmaxf(fmaxf(t[0][1], t[0][3]), fmaxf(t[1][1], t[1][3])), fmaxf(fmaxf(t[2][1], t[2][3]), fmaxf(t[3][1], t[3][3])));
    const float p0 = __builtin_amdgcn_exp2f((sc[0] - M0) * LOG2E), p1 = __builtin_amdgcn_exp2f((sc[1] - M1) * LOG2E);
    const int po = L_P + (g >> 1) * 1152 + ((4 * (g & 1) + (n >> 2)) * 32 + 4 * w + (n & 3)) * 2;
    const unsigned pp = pk2(p0, p1);
    *(LAS u16*)(L + po) = (u16)pp;
    *(LAS u16*)(L + po + 576) = (u16)(pp >> 16);
    const f32x2 ls = {x16_sum(row_sum(p0)), x16_sum(row_sum(p1))};
    if ((lane & 31) == 0) *(LAS f32x2*)(L + L_RL + (g >> 1) * 64 + w * 8) = ls;
    mh = (g & 1) ? M1 : M0;
  }
  STAMP(4);
  const int tq = n >> 2, tp = n & 3;
  const int tr0 = (8 * g + tq) * 256 + (((2 * w + (tp >> 1)) ^ ((tq << 2) | ((2 * g) & 3))) * 16) + 8 * (tp & 1);
  const int tr1 = (8 * g + 4 + tq) * 256 + (((2 * w + (tp >> 1)) ^ ((tq << 2) | ((2 * g + 1) & 3))) * 16) + 8 * (tp & 1);
  VMWAIT(6);
  BARRIER();
  {
    v4u pa[8];
#pragma unroll
    for (int i = 0; i < 8; ++i) pa[i] = *(LAS v4u*)(L + L_P + (n >> 2) * 576 + i * 64 + g * 16);
    f32x4 t[4];
#pragma unroll
    for (int i = 0; i < 4; ++i) t[i] = *(LAS f32x4*)(L + L_RL + (g >> 1) * 64 + i * 16);
    f32x4 acc = {0.f, 0.f, 0.f, 0.f};
#pragma unroll
    for (int i = 0; i < 6; ++i) acc = mfma32(pa[i], tr_frag(L, L_V + tr0 + i * 8192, L_V + tr1 + i * 8192), acc);
    const f32x4 ts = (t[0] + t[1]) + (t[2] + t[3]);
    unsigned ml[2] = {nosent(__float_as_uint(mh)), nosent(__float_as_uint((g & 1) ? ts[1] + ts[3] : ts[0] + ts[2]))};
    int pao = (n & 3) == 0 ? WS_PA + c * 2048 + w * 256 + g * 64 + (n >> 2) * 16 : OOB;
    PIN(ml[0]); PIN(ml[1]); PIN(pao); PIN(acc); PIN(pa[6]); PIN(pa[7]);
    VMWAIT(4);
    STAMP(5);
    BARRIER();
#pragma unroll
    for (int i = 6; i < 8; ++i) acc = mfma32(pa[i], tr_frag(L, L_V + tr0 + i * 8192, L_V + tr1 + i * 8192), acc);
    const unsigned d0 = nosent(pk2(acc[0], dpp<SHL1>(acc[0])));
    st_sc1(rs, pao, v4u{d0, dppu<SHL2>(d0), ml[0], ml[1]});
  }
  STAMP(6);

  if (is_wo) {
    const int hh = i32 >> 2;
    int off[2];
#pragma unroll
    for (int i = 0; i < 2; ++i) off[i] = WS_PA + (kvh + 8 * (8 * (n & 3) + 2 * (n >> 2) + i)) * 2048 + w * 256 + hh * 64 + g * 16;
    VMWAIT(1);
    BARRIER();
    v4u bf[4];
#pragma unroll
    for (int i = 0; i < 4; ++i) { bf[i] = tr_frag(L, L_K + tr0 + i * 8192, L_K + tr1 + i * 8192); PIN(bf[i]); }
    STAMP(7);
    v4u pv[2];
    poll_fresh<2>(rs, off, pv);
    STAMP(8);
    const float m0 = __uint_as_float(pv[0][2]), m1 = __uint_as_float(pv[1][2]);
    float mm = vmax(m0, m1);
    mm = vmax(mm, dpp<ROR4>(mm)); mm = vmax(mm, dpp<ROR8>(mm));
    const float e0 = __builtin_amdgcn_exp2f((m0 - mm) * LOG2E), e1 = __builtin_amdgcn_exp2f((m1 - mm) * LOG2E);
    float t[5];
    t[0] = e0 * bflo(pv[0][0]) + e1 * bflo(pv[1][0]);
    t[1] = e0 * bfhi(pv[0][0]) + e1 * bfhi(pv[1][0]);
    t[2] = e0 * bflo(pv[0][1]) + e1 * bflo(pv[1][1]);
    t[3] = e0 * bfhi(pv[0][1]) + e1 * bfhi(pv[1][1]);
    t[4] = e0 * __uint_as_float(pv[0][3]) + e1 * __uint_as_float(pv[1][3]);
#pragma unroll
    for (int i = 0; i < 5; ++i) { t[i] += dpp<ROR4>(t[i]); t[i] += dpp<ROR8>(t[i]); }
    const float inv = __builtin_amdgcn_rcpf(t[4]);
    if (n < 4) *(LAS v2u*)(L + L_O + n * 320 + w * 32 + g * 8) = v2u{pk2(t[0] * inv, t[1] * inv), pk2(t[2] * inv, t[3] * inv)};
    BARRIER();
    f32x4 acc = {0.f, 0.f, 0.f, 0.f};
#pragma unroll
    for (int i = 0; i < 4; ++i) acc = mfma32(*(LAS v4u*)(L + L_O + (n & 3) * 320 + i * 64 + g * 16), bf[i], acc);
    st_sc1(rs, lane < 16 ? hp_off + lane * 16 : OOB, nosent4(acc));
    STAMP(9);
  } else if (is_ffn) {
    {
      const int alt = (lane & 2) ? -16 : 16;
#pragma unroll
      for (int i = 4; i < 8; ++i) dma_glb<GU_NT>(lbase + L_K + w * 8192 + i * 1024, gup + i * 8 * FF + ((i & 1) ? alt : 0));
    }
    BARRIER();
    {
      const v4u srd_d = make_srd(wd + (size_t)(32 * jf + 4 * w) * DM, 4 * DM * 2);
#pragma unroll
      for (int i = 0; i < 4; ++i) dma_buf<W2_NT>(ldst + L_V + i * 8192, vv0 + g * (DM * 2 - 256), srd_d, i * 256);
    }
    if (w < 4) wait_ready(rs, lane, tlo, thi);
    VMWAIT(0);
    BARRIER();
    v4u bf[16], bd[4];
    {
      const int q2 = L_V + (w >> 1) * 8192, x0 = ((8 * (w & 1)) ^ (2 * w)) << 4;
#pragma unroll
      for (int i = 0; i < 4; ++i) { bd[i] = tr_frag(L, q2 + (tr0 ^ x0 ^ (i << 5)), q2 + (tr1 ^ x0 ^ (i << 5))); PIN(bd[i]); }
    }
    int ao[4];
#pragma unroll
    for (int i = 0; i < 4; ++i) { ao[i] = (n >> 2) == i ? L_HH + (n & 3) * 64 + g * 16 : L_ZERO; PIN(ao[i]); }
    if (w < 4) {
      const int z = ((tq >> 1) & 1) | ((g & 1) << 1);
      const int o = L_K + (8 * g + tq) * 128 + ((4 * (tp >> 1) + (w ^ z)) * 16) + 8 * (tp & 1);
#pragma unroll
      for (int i = 0; i < 16; ++i) { bf[i] = tr_frag(L, o + i * 4096, o + i * 4096 + 512); PIN(bf[i]); }
      STAMP(7);
      const int off[1] = {WS_H + lane * 64 + w * 16};
      v4u hv[1];
      poll_fresh<1>(rs, off, hv);
      STAMP(8);
      float v[8];
#pragma unroll
      for (int k = 0; k < 4; ++k) { v[2 * k] = bflo(hv[0][k]); v[2 * k + 1] = bfhi(hv[0][k]); }
      const float mean = wave_sum(((v[0] + v[1]) + (v[2] + v[3])) + ((v[4] + v[5]) + (v[6] + v[7]))) * (1.0f / DM);
      float q0 = 0.f, q1 = 0.f;
#pragma unroll
      for (int k = 0; k < 8; k += 2) { const float d0 = v[k] - mean, d1 = v[k + 1] - mean; q0 = fmaf(d0, d0, q0); q1 = fmaf(d1, d1, q1); }
      const float q = wave_sum(q0 + q1);
      const float mb = rb(mean);
      const float inv = __builtin_amdgcn_rcpf(rb(__builtin_amdgcn_sqrtf(rb(rb(q * (1.0f / DM)) + 1e-5f))));
      v4u o4;
#pragma unroll
      for (int k = 0; k < 4; ++k) o4[k] = pk2(rb(v[2 * k] - mb) * inv, rb(v[2 * k + 1] - mb) * inv);
      *(LAS v4u*)(L + L_HN + w * 1088 + lane * 16) = o4;
    }
    BARRIER();
    if (w < 4) {
      f32x4 acc = {0.f, 0.f, 0.f, 0.f};
#pragma unroll
      for (int i = 0; i < 16; ++i) acc = mfma32(*(LAS v4u*)(L + L_HN + (n >> 2) * 1088 + i * 64 + g * 16), bf[i], acc);
      const float a = rb(acc[0]), u = dpp<ROR8>(a);
      const float sg = rb(__builtin_amdgcn_rcpf(1.0f + __builtin_amdgcn_exp2f(a * -LOG2E)));
      if (n < 8) *(LAS u16*)(L + L_HH + g * 64 + (8 * w + n) * 2) = (u16)pk2(sg * u, 0.f);
    }
    BARRIER();
    {
      f32x4 acc = {0.f, 0.f, 0.f, 0.f};
#pragma unroll
      for (int i = 0; i < 4; ++i) acc = mfma32(*(LAS v4u*)(L + ao[i]), bd[i], acc);
      st_sc1(rs, WS_OP + jf * 8192 + w * 1024 + lane * 16, nosent4(acc));
    }
    STAMP(9);
  } else if (is_red && w == 0) {
    const int r2 = lane >> 3, k = lane & 7, bb = r2 & 3;
    const float xv = bflo(x[bb * DM + 8 * rho + k]);
    int off1[4], off2[7];
#pragma unroll
    for (int i = 0; i < 4; ++i) off1[i] = WS_HP + (8 * i + r2) * 8192 + (8 * rho + k) * 16;
#pragma unroll
    for (int i = 0; i < 7; ++i) off2[i] = WS_OP + (8 * i + r2) * 8192 + (8 * rho + k) * 16;
    wait_ready(rs, lane, tlo, thi);
    STAMP(7);
    float hval;
    {
      v4u pv[4];
      poll_fresh<4>(rs, off1, pv);
      STAMP(8);
      f32x4 a = (__builtin_bit_cast(f32x4, pv[0]) + __builtin_bit_cast(f32x4, pv[1])) + (__builtin_bit_cast(f32x4, pv[2]) + __builtin_bit_cast(f32x4, pv[3]));
#pragma unroll
      for (int i = 0; i < 4; ++i) { a[i] += dpp<ROR8>(a[i]); a[i] = x32_sum(x16_sum(a[i])); }
      const float av = (bb & 2) ? ((bb & 1) ? a[3] : a[2]) : ((bb & 1) ? a[1] : a[0]);
      hval = rb(xv + rb(av));
      const unsigned dw = nosent(pk2(hval, dpp<SHL1>(hval)));
      const unsigned dw2 = dppu<SHL2>(dw);
      const unsigned dw4 = dppu<SHL4>(dw), dw6 = dppu<SHL4>(dw2);
      st_sc1(rs, (k == 0 && lane < 32) ? WS_H + rho * 64 + bb * 16 : OOB, v4u{dw, dw2, dw4, dw6});
    }
    STAMP(9);
    {
      v4u pv[7];
      poll_fresh<7>(rs, off2, pv);
      STAMP(10);
      f32x4 a = ((__builtin_bit_cast(f32x4, pv[0]) + __builtin_bit_cast(f32x4, pv[1])) + (__builtin_bit_cast(f32x4, pv[2]) + __builtin_bit_cast(f32x4, pv[3]))) +
                ((__builtin_bit_cast(f32x4, pv[4]) + __builtin_bit_cast(f32x4, pv[5])) + __builtin_bit_cast(f32x4, pv[6]));
#pragma unroll
      for (int i = 0; i < 4; ++i) { a[i] += dpp<ROR8>(a[i]); a[i] = x32_sum(x16_sum(a[i])); }
      const float av = (bb & 2) ? ((bb & 1) ? a[3] : a[2]) : ((bb & 1) ? a[1] : a[0]);
      const float ov = hval + rb(av);
      const unsigned dw = pk2(ov, dpp<SHL1>(ov));
      const unsigned dw2 = dppu<SHL2>(dw);
      const unsigned dw4 = dppu<SHL4>(dw), dw6 = dppu<SHL4>(dw2);
      if (k == 0 && lane < 32) *(GAS v4u*)(out + bb * DM + 8 * rho) = v4u{dw, dw2, dw4, dw6};
      if (c == 192 && lane == 0) __hip_atomic_store((GAS u64*)(ws + WS_EPOCH), tag, __ATOMIC_RELAXED, __HIP_MEMORY_SCOPE_AGENT);
    }
    STAMP(11);
  }
  STAMP_OUT();
}

template <int LDSB> __global__ void probe_k(unsigned* o) {
  unsigned long long t; asm volatile("s_memrealtime %0\n s_waitcnt lgkmcnt(0)" : "=s"(t));
  __shared__ unsigned sm[LDSB / 4];
  sm[threadIdx.x] = threadIdx.x;
  while (__builtin_amdgcn_s_memrealtime() < t + 2000) __builtin_amdgcn_s_sleep(1);
  if (sm[(threadIdx.x + 1) & 63] == 0x12345678u) o[4 << 20] = 1;
}
}

extern "C" void kernel_launch(void* const* d_in, const int* in_sizes, int n_in, void* d_out, int out_size, void* d_ws, size_t ws_size,
                              hipStream_t stream) {
  (void)in_sizes; (void)n_in; (void)out_size; (void)ws_size;
  probe_k<4096><<<1, 64, 0, stream>>>((unsigned*)d_ws);
  fused_block<<<256, NTHR, 0, stream>>>((const u16*)d_in[0], (const u16*)d_in[1], (const u16*)d_in[2], (const u16*)d_in[3], (const u16*)d_in[4],
                                        (const u16*)d_in[6], (const u16*)d_in[7], (const u16*)d_in[8], (u16*)d_out, (unsigned char*)d_ws);
}
```

```cpp
#define ENC 0
#include <hip/hip_runtime.h>
#include <hip/hip_bf16.h>
#include <stdint.h>

namespace {

typedef unsigned short u16;
typedef unsigned long long u64;
typedef unsigned v4u __attribute__((ext_vector_type(4)));
typedef unsigned v2u __attribute__((ext_vector_type(2)));
typedef float f32x4 __attribute__((ext_vector_type(4)));
typedef float f32x2 __attribute__((ext_vector_type(2)));
typedef short bf16x8 __attribute__((ext_vector_type(8)));
typedef short s16x4 __attribute__((ext_vector_type(4)));
typedef __bf16 bf2 __attribute__((ext_vector_type(2)));
#define LAS __attribute__((address_space(3)))
#define GAS __attribute__((address_space(1)))

constexpr int DM = 512, HKV = 8, SK = 8192, DH = 128, WIN = 2048, FF = 1792, NQ = 4096;
constexpr int NTHR = 576;

constexpr int WS_EPOCH = 0;
constexpr int WS_RDY = 4096;
constexpr int WS_QG = 8192;
constexpr int WS_PA = WS_QG + 512 * 1024;
constexpr int WS_HP = WS_PA + 512 * 1024;
constexpr int WS_H = WS_HP + 256 * 1024;
constexpr int WS_OP = WS_H + 4096;
constexpr int WS_END = WS_OP + 56 * 8192;
constexpr int OOB = 0x40000000;

constexpr int L_K = 0;
constexpr int L_V = 65536;
constexpr int L_WQ = 131072;
constexpr int L_HN = L_WQ;
constexpr int L_Q = L_WQ + 16384;
constexpr int L_XN = L_Q + 1280;
constexpr int L_O = L_XN;
constexpr int L_QP = L_XN + 1280;
constexpr int L_P = L_QP + 1024;
constexpr int L_RM = L_P + 2304;
constexpr int L_RL = L_RM + 128;
constexpr int L_HH = L_RL + 128;
constexpr int L_ZERO = L_HH + 256;
constexpr int L_CNT = L_ZERO + 16;
constexpr int L_BYTES = L_CNT + 16;

constexpr unsigned SENT = 0xFFFFFFFFu;
constexpr int SPIN_CAP = 1 << 16;
constexpr int AUX_SC1 = 16;
constexpr float LOG2E = 1.4426950408889634f;

#define KV_NT 1
#define WQ_NT 1
#define W2_NT 1
#define GU_NT 0

__device__ __forceinline__ float bflo(unsigned x) { return __builtin_bit_cast(float, x << 16); }
__device__ __forceinline__ float bfhi(unsigned x) { return __builtin_bit_cast(float, x & 0xffff0000u); }
__device__ __forceinline__ unsigned pk2(float lo, float hi) {
  bf2 v = {(__bf16)lo, (__bf16)hi};
  return __builtin_bit_cast(unsigned, v);
}
__device__ __forceinline__ float rb(float x) { return (float)(__bf16)x; }
__device__ __forceinline__ unsigned nosent(unsigned x) { return x < 0xFFFFFFFEu ? x : 0xFFFFFFFEu; }
__device__ __forceinline__ v4u nosent4(f32x4 v) {
  return v4u{nosent(__float_as_uint(v[0])), nosent(__float_as_uint(v[1])), nosent(__float_as_uint(v[2])), nosent(__float_as_uint(v[3]))};
}
__device__ __forceinline__ bool fresh(v4u v) { return max(max(v[0], v[1]), max(v[2], v[3])) != SENT; }
__device__ __forceinline__ float vmax(float a, float b) { float r; asm("v_max_f32 %0, %1, %2" : "=v"(r) : "v"(a), "v"(b)); return r; }

__device__ __forceinline__ f32x4 mfma32(v4u a, v4u b, f32x4 c) {
  return __builtin_amdgcn_mfma_f32_16x16x32_bf16(__builtin_bit_cast(bf16x8, a), __builtin_bit_cast(bf16x8, b), c, 0, 0, 0);
}
template <int CTRL> __device__ __forceinline__ float dpp(float x) {
  return __builtin_bit_cast(float, __builtin_amdgcn_mov_dpp(__builtin_bit_cast(int, x), CTRL, 0xf, 0xf, true));
}
template <int CTRL> __device__ __forceinline__ unsigned dppu(unsigned x) { return (unsigned)__builtin_amdgcn_mov_dpp((int)x, CTRL, 0xf, 0xf, true); }
constexpr int ROR8 = 0x128, ROR4 = 0x124, ROR2 = 0x122, ROR1 = 0x121, SHL1 = 0x101, SHL2 = 0x102, SHL4 = 0x104;
__device__ __forceinline__ float row_max(float x) { x = fmaxf(x, dpp<ROR8>(x)); x = fmaxf(x, dpp<ROR4>(x)); x = fmaxf(x, dpp<ROR2>(x)); return fmaxf(x, dpp<ROR1>(x)); }
__device__ __forceinline__ float row_sum(float x) { x += dpp<ROR8>(x); x += dpp<ROR4>(x); x += dpp<ROR2>(x); return x + dpp<ROR1>(x); }
__device__ __forceinline__ float x16_max(float x) { auto s = __builtin_amdgcn_permlane16_swap(__float_as_uint(x), __float_as_uint(x), false, false); return fmaxf(__uint_as_float(s[0]), __uint_as_float(s[1])); }
__device__ __forceinline__ float x16_sum(float x) { auto s = __builtin_amdgcn_permlane16_swap(__float_as_uint(x), __float_as_uint(x), false, false); return __uint_as_float(s[0]) + __uint_as_float(s[1]); }
__device__ __forceinline__ float x32_sum(float x) { auto s = __builtin_amdgcn_permlane32_swap(__float_as_uint(x), __float_as_uint(x), false, false); return __uint_as_float(s[0]) + __uint_as_float(s[1]); }
__device__ __forceinline__ float wave_sum(float x) { return x32_sum(x16_sum(row_sum(x))); }
__device__ __forceinline__ f32x4 sel2(bool c, f32x4 a, f32x4 b) {
  return f32x4{c ? b[0] : a[0], c ? b[1] : a[1], c ? b[2] : a[2], c ? b[3] : a[3]};
}
__device__ __forceinline__ f32x4 sel4(int g, f32x4 a, f32x4 b, f32x4 c, f32x4 d) { return sel2(g & 2, sel2(g & 1, a, b), sel2(g & 1, c, d)); }

#define PIN(x) asm volatile("" : "+v"(x))
#define SCHED() __builtin_amdgcn_sched_barrier(0)
#define LDS_FENCE() asm volatile("s_waitcnt lgkmcnt(0)" ::: "memory")
#define BARRIER() do { asm volatile("s_waitcnt lgkmcnt(0)\n\ts_barrier" ::: "memory"); } while (0)
#define VMWAIT(N) asm volatile("s_waitcnt vmcnt(" #N ")" ::: "memory")
#define STAMP(i) do { SCHED(); stamp[i] = __builtin_amdgcn_s_memrealtime(); SCHED(); } while (0)
#define STAMP_OUT() do { if (c == 192 && w == 0) while (__builtin_amdgcn_s_memrealtime() < stamp[ENC] + 4000) __builtin_amdgcn_s_sleep(1); } while (0)

__device__ __forceinline__ v4u make_srd(const void* p, unsigned bytes) {
  const u64 a = (u64)p;
  return v4u{(unsigned)a, (unsigned)(a >> 32) & 0xffffu, bytes, 0x00020000u};
}
template <int NT> __device__ __forceinline__ void dma_buf(unsigned lds, int voff, v4u srd, int soff) {
  if (NT) asm volatile("s_mov_b32 m0, %0\n\ts_nop 0\n\tbuffer_load_dwordx4 %1, %2, %3 offen nt lds" :: "s"(lds), "v"(voff), "s"(srd), "s"(soff) : "m0", "memory");
  else asm volatile("s_mov_b32 m0, %0\n\ts_nop 0\n\tbuffer_load_dwordx4 %1, %2, %3 offen lds" :: "s"(lds), "v"(voff), "s"(srd), "s"(soff) : "m0", "memory");
}
template <int NT> __device__ __forceinline__ void dma_glb(unsigned lds, const void* p) {
  if (NT) asm volatile("s_mov_b32 m0, %0\n\ts_nop 0\n\tglobal_load_lds_dwordx4 %1, off nt" :: "s"(lds), "v"(p) : "m0", "memory");
  else asm volatile("s_mov_b32 m0, %0\n\ts_nop 0\n\tglobal_load_lds_dwordx4 %1, off" :: "s"(lds), "v"(p) : "m0", "memory");
}
__device__ __forceinline__ void ld16_asm(v4u& d, int voff, v4u srd, int soff) {
  asm volatile("buffer_load_dwordx4 %0, %1, %2, %3 offen" : "=v"(d) : "v"(voff), "s"(srd), "s"(soff) : "memory");
}
__device__ __forceinline__ void ld16_sc1_asm(v4u& d, int voff, v4u srd) {
  asm volatile("buffer_load_dwordx4 %0, %1, %2, 0 offen sc1" : "=v"(d) : "v"(voff), "s"(srd) : "memory");
}
__device__ __forceinline__ void ld8_sc1_asm(v2u& d, v4u srd) {
  asm volatile("buffer_load_dwordx2 %0, off, %1, 0 sc1" : "=v"(d) : "s"(srd) : "memory");
}
__device__ __forceinline__ void st16_sc1_asm(v4u v, int voff, v4u srd) {
  asm volatile("buffer_store_dwordx4 %0, %1, %2, 0 offen sc1\n\ts_nop 1" :: "v"(v), "v"(voff), "s"(srd) : "memory");
}

__device__ __forceinline__ v4u ld_sc1(__amdgpu_buffer_rsrc_t rs, int off) { return __builtin_amdgcn_raw_buffer_load_b128(rs, off, 0, AUX_SC1); }
__device__ __forceinline__ void st_sc1(__amdgpu_buffer_rsrc_t rs, int off, v4u v) { __builtin_amdgcn_raw_buffer_store_b128(v, rs, off, 0, AUX_SC1); }
__device__ __forceinline__ v2u tr_rd(LAS unsigned char* L, int off) {
  return __builtin_bit_cast(v2u, __builtin_amdgcn_ds_read_tr16_b64_v4i16((LAS s16x4*)(L + off)));
}
__device__ __forceinline__ v4u tr_frag(LAS unsigned char* L, int off0, int off1) {
  const v2u a = tr_rd(L, off0), b = tr_rd(L, off1);
  return v4u{a[0], a[1], b[0], b[1]};
}

template <int N> __device__ __forceinline__ void poll_fresh(__amdgpu_buffer_rsrc_t rs, const int (&off)[N], v4u (&v)[N]) {
  for (int it = 0; it < SPIN_CAP; ++it) {
    asm volatile("" ::: "memory");
    bool ok = true;
#pragma unroll
    for (int i = 0; i < N; ++i) v[i] = ld_sc1(rs, off[i]);
#pragma unroll
    for (int i = 0; i < N; ++i) ok &= fresh(v[i]);
    if (__all(ok)) break;
  }
}
__device__ __forceinline__ void wait_ready(__amdgpu_buffer_rsrc_t rs, int lane, unsigned tlo, unsigned thi) {
  for (int it = 0; it < SPIN_CAP; ++it) {
    asm volatile("" ::: "memory");
    const v4u r0 = ld_sc1(rs, WS_RDY + lane * 16), r1 = ld_sc1(rs, WS_RDY + 1024 + lane * 16);
    const bool ok = (r0[0] == tlo) & (r0[1] == thi) & (r0[2] == tlo) & (r0[3] == thi) & (r1[0] == tlo) & (r1[1] == thi) & (r1[2] == tlo) & (r1[3] == thi);
    if (__all(ok)) break;
  }
}

__global__ void __launch_bounds__(NTHR, 3)
fused_block(const u16* __restrict__ x, const u16* __restrict__ kc, const u16* __restrict__ vc, const u16* __restrict__ wq,
            const u16* __restrict__ wo, const u16* __restrict__ wg, const u16* __restrict__ wu, const u16* __restrict__ wd,
            u16* __restrict__ out, unsigned char* __restrict__ ws) {
  __shared__ __attribute__((aligned(16))) unsigned char smem_[L_BYTES];
  LAS unsigned char* L = (LAS unsigned char*)smem_;
  const unsigned lbase = (unsigned)(size_t)L;
  const int c = blockIdx.x, kvh = c & 7, i32 = c >> 3;
  const int lane = threadIdx.x & 63, wv = __builtin_amdgcn_readfirstlane(threadIdx.x >> 6);
  const int w = wv == 0 ? 8 : wv - 1;
  const int n = lane & 15, g = lane >> 4;
  const v4u srd_ws = make_srd(ws, WS_END);
  u64 stamp[16];
#pragma unroll
  for (int i = 0; i < 16; ++i) stamp[i] = 0;
  STAMP(0);

  if (wv == 0) {
    const int rq = i32 >> 3, cq = i32 & 7;
    const v4u srd_x = make_srd(x, 4 * DM * 2);
    const v4u srd_wq = make_srd(wq + (size_t)rq * 128 * NQ + kvh * 512 + cq * 64, 128 * NQ * 2);
    v2u ep;
    v4u xr[4];
    ld8_sc1_asm(ep, srd_ws);
#pragma unroll
    for (int i = 0; i < 4; ++i) ld16_asm(xr[i], g * 1024 + n * 16, srd_x, ((rq + i) & 3) * 256);
    {
      const int row = lane >> 3, v0 = row * (NQ * 2) + (((lane & 7) ^ (2 * ((row >> 1) & 1))) * 16);
#pragma unroll
      for (int i = 0; i < 16; ++i) dma_buf<WQ_NT>(lbase + L_WQ + i * 1024, (i & 1) ? (v0 ^ 64) : v0, srd_wq, i * 8 * NQ * 2);
    }
    if (lane == 0) { *(LAS v4u*)(L + L_ZERO) = v4u{0u, 0u, 0u, 0u}; *(LAS unsigned*)(L + L_CNT) = 0u; }
    BARRIER();
    asm volatile("s_waitcnt vmcnt(16)" : "+v"(ep), "+v"(xr[0]), "+v"(xr[1]), "+v"(xr[2]), "+v"(xr[3]) :: "memory");
    {
      float v[32], s = 0.f;
#pragma unroll
      for (int i = 0; i < 4; ++i)
#pragma unroll
        for (int k = 0; k < 4; ++k) { v[8 * i + 2 * k] = bflo(xr[i][k]); v[8 * i + 2 * k + 1] = bfhi(xr[i][k]); }
#pragma unroll
      for (int k = 0; k < 32; k += 4) s += (v[k] + v[k + 1]) + (v[k + 2] + v[k + 3]);
      const float mean = row_sum(s) * (1.0f / DM);
      float q0 = 0.f, q1 = 0.f;
#pragma unroll
      for (int k = 0; k < 32; k += 2) { const float d0 = v[k] - mean, d1 = v[k + 1] - mean; q0 = fmaf(d0, d0, q0); q1 = fmaf(d1, d1, q1); }
      const float q = row_sum(q0 + q1);
      const float mb = rb(mean);
      const float inv = __builtin_amdgcn_rcpf(rb(__builtin_amdgcn_sqrtf(rb(rb(q * (1.0f / DM)) + 1e-5f))));
      v4u o;
#pragma unroll
      for (int k = 0; k < 4; ++k) o[k] = pk2(rb(v[2 * k] - mb) * inv, rb(v[2 * k + 1] - mb) * inv);
      *(LAS v4u*)(L + L_XN + g * 320 + n * 16) = o;
    }
    STAMP(1);
    asm volatile("s_waitcnt vmcnt(0) lgkmcnt(0)" ::: "memory");
    STAMP(4);
    f32x4 acc[4];
    {
      v4u xa[4];
#pragma unroll
      for (int ks = 0; ks < 4; ++ks) xa[ks] = *(LAS v4u*)(L + L_XN + (n & 3) * 320 + ks * 64 + g * 16);
      const int q4 = n >> 2, p = n & 3, y = ((q4 >> 1) & 1) | ((g & 1) << 1);
      const int rbase = L_WQ + (8 * g + q4) * 128 + 8 * (p & 1);
#pragma unroll
      for (int t = 0; t < 4; ++t) {
        const int o = rbase + (((2 * t + (p >> 1)) ^ (2 * y)) * 16);
        acc[t] = f32x4{0.f, 0.f, 0.f, 0.f};
#pragma unroll
        for (int ks = 0; ks < 4; ++ks) acc[t] = mfma32(xa[ks], tr_frag(L, o + ks * 4096, o + ks * 4096 + 512), acc[t]);
      }
    }
    *(LAS f32x4*)(L + L_QP + lane * 16) = sel4(g, acc[0], acc[1], acc[2], acc[3]);
    LDS_FENCE();
    const f32x4 q0 = *(LAS f32x4*)(L + L_QP + (lane & 31) * 32), q1 = *(LAS f32x4*)(L + L_QP + (lane & 31) * 32 + 16);
    const u64 tag = (((u64)ep[1] << 32) | ep[0]) + 0x9E3779B97F4A7C15ull;
    const unsigned tlo = (unsigned)tag, thi = (unsigned)(tag >> 32);
    STAMP(5);
    for (int it = 0; it < (SPIN_CAP << 4); ++it) {
      if (__builtin_amdgcn_readfirstlane(*(volatile LAS unsigned*)(L + L_CNT)) >= 8u) break;
    }
    STAMP(2);
    {
      const bool up = lane >= 32;
      const int o = WS_QG + ((kvh * 4 + (up ? 1 : 0)) * 4 + rq) * 4096 + (32 * cq + (lane & 31)) * 16;
      st16_sc1_asm(v4u{__float_as_uint(up ? q0[1] : q0[0]), __float_as_uint(up ? q1[1] : q1[0]), tlo, thi}, o, srd_ws);
      st16_sc1_asm(v4u{__float_as_uint(up ? q0[3] : q0[2]), __float_as_uint(up ? q1[3] : q1[2]), tlo, thi}, o + 2 * 4 * 4096, srd_ws);
      if (lane == 0) __hip_atomic_store((GAS u64*)(ws + WS_RDY + c * 8), tag, __ATOMIC_RELAXED, __HIP_MEMORY_SCOPE_AGENT);
    }
    STAMP(3);
    STAMP_OUT();
    return;
  }

  const bool is_wo = i32 < 16, is_ffn = i32 >= 16 && i32 < 23, is_red = i32 >= 24;
  const int jf = kvh * 7 + (i32 - 16);
  const int rho = (i32 - 24) * 8 + kvh;
  const __amdgpu_buffer_rsrc_t rs = __builtin_amdgcn_make_buffer_rsrc(ws, 0, WS_END, 0x00020000);
  const int b = i32 >> 3, s = i32 & 7;
  const size_t kvbase = ((size_t)(b * HKV + kvh) * SK + (SK - WIN) + s * 256) * DH;
  const v4u srd_k = make_srd(kc + kvbase, 65536), srd_v = make_srd(vc + kvbase, 65536);
  const int rot = w + 37 * c;
  const int kv0 = g * 256 + ((n ^ g) * 16);
  const int vv0 = g * 256 + ((n ^ ((g << 2) | (w & 3))) * 16);
  const unsigned ldst = lbase + w * 1024;
  const int hp_off = WS_HP + (kvh * 4 + (i32 >> 2)) * 8192 + (i32 & 3) * 2048 + w * 256;
  const int qoff = WS_QG + ((kvh * 4 + b) * 4 + (lane >> 5)) * 4096 + (32 * w + (lane & 31)) * 16;
  asm volatile("" :: "s"(wo), "s"(wg), "s"(wu), "s"(wd), "s"(out), "s"(x));
  v2u ep;
  v4u qg[2];
  {
    const v4u sent = {SENT, SENT, SENT, SENT};
    const int o1 = is_wo ? (lane < 16 ? hp_off + lane * 16 : OOB) : is_ffn ? WS_OP + jf * 8192 + w * 1024 + lane * 16
                 : (is_red && w == 0 && lane < 4) ? WS_H + rho * 64 + lane * 16 : OOB;
    const int o0 = lane < 16 ? WS_PA + c * 2048 + w * 256 + lane * 16 : OOB;
    BARRIER();
    ld8_sc1_asm(ep, srd_ws);
    st16_sc1_asm(sent, o0, srd_ws);
    st16_sc1_asm(sent, o1, srd_ws);
  }
#define KLOAD(i) dma_buf<KV_NT>(ldst + L_K + (i) * 8192, kv0 ^ (((i) & 3) << 6), srd_k, ((8 * (i) + rot) & 63) << 10)
#define VLOAD(i) dma_buf<KV_NT>(ldst + L_V + (i) * 8192, vv0, srd_v, ((8 * (i) + rot) & 63) << 10)
  KLOAD(0); KLOAD(1); KLOAD(2); KLOAD(3);
  VMWAIT(3);
  STAMP(12);
  if (lane == 0) __hip_atomic_fetch_add((LAS unsigned*)(L + L_CNT), 1u, __ATOMIC_RELAXED, __HIP_MEMORY_SCOPE_WORKGROUP);
  KLOAD(4); VMWAIT(3); KLOAD(5); VMWAIT(3); KLOAD(6); VMWAIT(3); KLOAD(7);
  STAMP(1);
  VMWAIT(3); VLOAD(0); VMWAIT(3); VLOAD(1);
  ld16_sc1_asm(qg[0], qoff, srd_ws);
  ld16_sc1_asm(qg[1], qoff + 8192, srd_ws);
  VMWAIT(4); VLOAD(2); VMWAIT(4); VLOAD(3); VMWAIT(4); VLOAD(4);
  asm volatile("s_waitcnt vmcnt(3)" : "+v"(ep), "+v"(qg[0]), "+v"(qg[1]) :: "memory");
  STAMP(13);
  const u64 tag = (((u64)ep[1] << 32) | ep[0]) + 0x9E3779B97F4A7C15ull;
  const unsigned tlo = (unsigned)tag, thi = (unsigned)(tag >> 32);
  for (int it = 0; it < SPIN_CAP; ++it) {
    const bool ok = (qg[0][2] == tlo) & (qg[0][3] == thi) & (qg[1][2] == tlo) & (qg[1][3] == thi);
    if (__all(ok)) break;
    asm volatile("buffer_load_dwordx4 %0, %2, %3, 0 offen sc1\n\tbuffer_load_dwordx4 %1, %4, %3, 0 offen sc1\n\ts_waitcnt vmcnt(0)"
                 : "=&v"(qg[0]), "=&v"(qg[1]) : "v"(qoff), "s"(srd_ws), "v"(qoff + 8192) : "memory");
  }
  VLOAD(5); VLOAD(6); VLOAD(7);
  STAMP(2);
  {
    const float a = x32_sum(__uint_as_float(qg[0][0]) + __uint_as_float(qg[1][0]));
    const float bq = x32_sum(__uint_as_float(qg[0][1]) + __uint_as_float(qg[1][1]));
    if (lane < 32) *(LAS unsigned*)(L + L_Q + (w >> 1) * 320 + (w & 1) * 128 + lane * 4) = pk2(a, bq);
  }
  BARRIER();
  STAMP(3);
  float sc[2];
  {
    const int qb = L_Q + (2 * (n >> 3) + (n & 1)) * 320 + g * 16;
    const int kb = L_K + (n >> 2) * 8192 + w * 1024 + (n & 3) * 256 + ((g ^ n) * 16);
    f32x4 d0 = {0.f, 0.f, 0.f, 0.f}, d1 = {0.f, 0.f, 0.f, 0.f};
#pragma unroll
    for (int ks = 0; ks < 4; ++ks) {
      const v4u qa = *(LAS v4u*)(L + qb + ks * 64);
      d0 = mfma32(qa, *(LAS v4u*)(L + (kb ^ (ks << 6))), d0);
      d1 = mfma32(qa, *(LAS v4u*)(L + (kb ^ (ks << 6)) + 32768), d1);
    }
    f32x2 mx;
#pragma unroll
    for (int j = 0; j < 2; ++j) {
      sc[j] = rb(rb((g & 1) ? d1[j] : d0[j]) * 0.08838834764831845f);
      mx[j] = x16_max(row_max(sc[j]));
    }
    if ((lane & 31) == 0) *(LAS f32x2*)(L + L_RM + (g >> 1) * 64 + w * 8) = mx;
  }
  BARRIER();
  STAMP(14);
  const u16* gup = ((lane >> 2) & 1 ? wu : wg) + (size_t)(64 * w + (lane >> 3)) * FF + 32 * jf + 8 * ((lane & 3) ^ (g & 1));
  {
    const u16* p = is_wo ? wo + (size_t)((kvh * 4 + (i32 >> 2)) * 128 + 4 * w) * DM + (i32 & 3) * 128 + g * (DM - 128) + (vv0 >> 1) : is_ffn ? gup : x;
    const int st = is_wo ? 32 * DM : is_ffn ? 8 * FF : 0;
    const unsigned d0 = lbase + L_K + (is_wo ? w * 1024 : w * 8192), ds = is_wo ? 8192 : 1024;
    const int alt = is_ffn ? (((lane & 2) ? -16 : 16)) : 0;
#pragma unroll
    for (int i = 0; i < 4; ++i) {
      if (GU_NT == W2_NT) dma_glb<W2_NT>(d0 + i * ds, p + i * st + ((i & 1) ? alt : 0));
      else if (is_ffn) dma_glb<GU_NT>(d0 + i * ds, p + i * st + ((i & 1) ? alt : 0));
      else dma_glb<W2_NT>(d0 + i * ds, p + i * st + ((i & 1) ? alt : 0));
    }
  }
  STAMP(15);
  float mh;
  {
    f32x4 t[4];
#pragma unroll
    for (int i = 0; i < 4; ++i) t[i] = *(LAS f32x4*)(L + L_RM + (g >> 1) * 64 + i * 16);
    const float M0 = fmaxf(fmaxf(fmaxf(t[0][0], t[0][2]), fmaxf(t[1][0], t[1][2])), fmaxf(fmaxf(t[2][0], t[2][2]), fmaxf(t[3][0], t[3][2])));
    const float M1 = fmaxf(fmaxf(fmaxf(t[0][1], t[0][3]), fmaxf(t[1][1], t[1][3])), fmaxf(fmaxf(t[2][1], t[2][3]), fmaxf(t[3][1], t[3][3])));
    const float p0 = __builtin_amdgcn_exp2f((sc[0] - M0) * LOG2E), p1 = __builtin_amdgcn_exp2f((sc[1] - M1) * LOG2E);
    const int po = L_P + (g >> 1) * 1152 + ((4 * (g & 1) + (n >> 2)) * 32 + 4 * w + (n & 3)) * 2;
    const unsigned pp = pk2(p0, p1);
    *(LAS u16*)(L + po) = (u16)pp;
    *(LAS u16*)(L + po + 576) = (u16)(pp >> 16);
    const f32x2 ls = {x16_sum(row_sum(p0)), x16_sum(row_sum(p1))};
    if ((lane & 31) == 0) *(LAS f32x2*)(L + L_RL + (g >> 1) * 64 + w * 8) = ls;
    mh = (g & 1) ? M1 : M0;
  }
  STAMP(4);
  const int tq = n >> 2, tp = n & 3;
  const int tr0 = (8 * g + tq) * 256 + (((2 * w + (tp >> 1)) ^ ((tq << 2) | ((2 * g) & 3))) * 16) + 8 * (tp & 1);
  const int tr1 = (8 * g + 4 + tq) * 256 + (((2 * w + (tp >> 1)) ^ ((tq << 2) | ((2 * g + 1) & 3))) * 16) + 8 * (tp & 1);
  VMWAIT(6);
  BARRIER();
  {
    v4u pa[8];
#pragma unroll
    for (int i = 0; i < 8; ++i) pa[i] = *(LAS v4u*)(L + L_P + (n >> 2) * 576 + i * 64 + g * 16);
    f32x4 t[4];
#pragma unroll
    for (int i = 0; i < 4; ++i) t[i] = *(LAS f32x4*)(L + L_RL + (g >> 1) * 64 + i * 16);
    f32x4 acc = {0.f, 0.f, 0.f, 0.f};
#pragma unroll
    for (int i = 0; i < 6; ++i) acc = mfma32(pa[i], tr_frag(L, L_V + tr0 + i * 8192, L_V + tr1 + i * 8192), acc);
    const f32x4 ts = (t[0] + t[1]) + (t[2] + t[3]);
    unsigned ml[2] = {nosent(__float_as_uint(mh)), nosent(__float_as_uint((g & 1) ? ts[1] + ts[3] : ts[0] + ts[2]))};
    int pao = (n & 3) == 0 ? WS_PA + c * 2048 + w * 256 + g * 64 + (n >> 2) * 16 : OOB;
    PIN(ml[0]); PIN(ml[1]); PIN(pao); PIN(acc); PIN(pa[6]); PIN(pa[7]);
    VMWAIT(4);
    STAMP(5);
    BARRIER();
#pragma unroll
    for (int i = 6; i < 8; ++i) acc = mfma32(pa[i], tr_frag(L, L_V + tr0 + i * 8192, L_V + tr1 + i * 8192), acc);
    const unsigned d0 = nosent(pk2(acc[0], dpp<SHL1>(acc[0])));
    st_sc1(rs, pao, v4u{d0, dppu<SHL2>(d0), ml[0], ml[1]});
  }
  STAMP(6);

  if (is_wo) {
    const int hh = i32 >> 2;
    int off[2];
#pragma unroll
    for (int i = 0; i < 2; ++i) off[i] = WS_PA + (kvh + 8 * (8 * (n & 3) + 2 * (n >> 2) + i)) * 2048 + w * 256 + hh * 64 + g * 16;
    VMWAIT(1);
    BARRIER();
    v4u bf[4];
#pragma unroll
    for (int i = 0; i < 4; ++i) { bf[i] = tr_frag(L, L_K + tr0 + i * 8192, L_K + tr1 + i * 8192); PIN(bf[i]); }
    STAMP(7);
    v4u pv[2];
    poll_fresh<2>(rs, off, pv);
    STAMP(8);
    const float m0 = __uint_as_float(pv[0][2]), m1 = __uint_as_float(pv[1][2]);
    float mm = vmax(m0, m1);
    mm = vmax(mm, dpp<ROR4>(mm)); mm = vmax(mm, dpp<ROR8>(mm));
    const float e0 = __builtin_amdgcn_exp2f((m0 - mm) * LOG2E), e1 = __builtin_amdgcn_exp2f((m1 - mm) * LOG2E);
    float t[5];
    t[0] = e0 * bflo(pv[0][0]) + e1 * bflo(pv[1][0]);
    t[1] = e0 * bfhi(pv[0][0]) + e1 * bfhi(pv[1][0]);
    t[2] = e0 * bflo(pv[0][1]) + e1 * bflo(pv[1][1]);
    t[3] = e0 * bfhi(pv[0][1]) + e1 * bfhi(pv[1][1]);
    t[4] = e0 * __uint_as_float(pv[0][3]) + e1 * __uint_as_float(pv[1][3]);
#pragma unroll
    for (int i = 0; i < 5; ++i) { t[i] += dpp<ROR4>(t[i]); t[i] += dpp<ROR8>(t[i]); }
    const float inv = __builtin_amdgcn_rcpf(t[4]);
    if (n < 4) *(LAS v2u*)(L + L_O + n * 320 + w * 32 + g * 8) = v2u{pk2(t[0] * inv, t[1] * inv), pk2(t[2] * inv, t[3] * inv)};
    BARRIER();
    f32x4 acc = {0.f, 0.f, 0.f, 0.f};
#pragma unroll
    for (int i = 0; i < 4; ++i) acc = mfma32(*(LAS v4u*)(L + L_O + (n & 3) * 320 + i * 64 + g * 16), bf[i], acc);
    st_sc1(rs, lane < 16 ? hp_off + lane * 16 : OOB, nosent4(acc));
    STAMP(9);
  } else if (is_ffn) {
    {
      const int alt = (lane & 2) ? -16 : 16;
#pragma unroll
      for (int i = 4; i < 8; ++i) dma_glb<GU_NT>(lbase + L_K + w * 8192 + i * 1024, gup + i * 8 * FF + ((i & 1) ? alt : 0));
    }
    BARRIER();
    {
      const v4u srd_d = make_srd(wd + (size_t)(32 * jf + 4 * w) * DM, 4 * DM * 2);
#pragma unroll
      for (int i = 0; i < 4; ++i) dma_buf<W2_NT>(ldst + L_V + i * 8192, vv0 + g * (DM * 2 - 256), srd_d, i * 256);
    }
    if (w < 4) wait_ready(rs, lane, tlo, thi);
    VMWAIT(0);
    BARRIER();
    v4u bf[16], bd[4];
    {
      const int q2 = L_V + (w >> 1) * 8192, x0 = ((8 * (w & 1)) ^ (2 * w)) << 4;
#pragma unroll
      for (int i = 0; i < 4; ++i) { bd[i] = tr_frag(L, q2 + (tr0 ^ x0 ^ (i << 5)), q2 + (tr1 ^ x0 ^ (i << 5))); PIN(bd[i]); }
    }
    int ao[4];
#pragma unroll
    for (int i = 0; i < 4; ++i) { ao[i] = (n >> 2) == i ? L_HH + (n & 3) * 64 + g * 16 : L_ZERO; PIN(ao[i]); }
    if (w < 4) {
      const int z = ((tq >> 1) & 1) | ((g & 1) << 1);
      const int o = L_K + (8 * g + tq) * 128 + ((4 * (tp >> 1) + (w ^ z)) * 16) + 8 * (tp & 1);
#pragma unroll
      for (int i = 0; i < 16; ++i) { bf[i] = tr_frag(L, o + i * 4096, o + i * 4096 + 512); PIN(bf[i]); }
      STAMP(7);
      const int off[1] = {WS_H + lane * 64 + w * 16};
      v4u hv[1];
      poll_fresh<1>(rs, off, hv);
      STAMP(8);
      float v[8];
#pragma unroll
      for (int k = 0; k < 4; ++k) { v[2 * k] = bflo(hv[0][k]); v[2 * k + 1] = bfhi(hv[0][k]); }
      const float mean = wave_sum(((v[0] + v[1]) + (v[2] + v[3])) + ((v[4] + v[5]) + (v[6] + v[7]))) * (1.0f / DM);
      float q0 = 0.f, q1 = 0.f;
#pragma unroll
      for (int k = 0; k < 8; k += 2) { const float d0 = v[k] - mean, d1 = v[k + 1] - mean; q0 = fmaf(d0, d0, q0); q1 = fmaf(d1, d1, q1); }
      const float q = wave_sum(q0 + q1);
      const float mb = rb(mean);
      const float inv = __builtin_amdgcn_rcpf(rb(__builtin_amdgcn_sqrtf(rb(rb(q * (1.0f / DM)) + 1e-5f))));
      v4u o4;
#pragma unroll
      for (int k = 0; k < 4; ++k) o4[k] = pk2(rb(v[2 * k] - mb) * inv, rb(v[2 * k + 1] - mb) * inv);
      *(LAS v4u*)(L + L_HN + w * 1088 + lane * 16) = o4;
    }
    BARRIER();
    if (w < 4) {
      f32x4 acc = {0.f, 0.f, 0.f, 0.f};
#pragma unroll
      for (int i = 0; i < 16; ++i) acc = mfma32(*(LAS v4u*)(L + L_HN + (n >> 2) * 1088 + i * 64 + g * 16), bf[i], acc);
      const float a = rb(acc[0]), u = dpp<ROR8>(a);
      const float sg = rb(__builtin_amdgcn_rcpf(1.0f + __builtin_amdgcn_exp2f(a * -LOG2E)));
      if (n < 8) *(LAS u16*)(L + L_HH + g * 64 + (8 * w + n) * 2) = (u16)pk2(sg * u, 0.f);
    }
    BARRIER();
    {
      f32x4 acc = {0.f, 0.f, 0.f, 0.f};
#pragma unroll
      for (int i = 0; i < 4; ++i) acc = mfma32(*(LAS v4u*)(L + ao[i]), bd[i], acc);
      st_sc1(rs, WS_OP + jf * 8192 + w * 1024 + lane * 16, nosent4(acc));
    }
    STAMP(9);
  } else if (is_red && w == 0) {
    const int r2 = lane >> 3, k = lane & 7, bb = r2 & 3;
    const float xv = bflo(x[bb * DM + 8 * rho + k]);
    int off1[4], off2[7];
#pragma unroll
    for (int i = 0; i < 4; ++i) off1[i] = WS_HP + (8 * i + r2) * 8192 + (8 * rho + k) * 16;
#pragma unroll
    for (int i = 0; i < 7; ++i) off2[i] = WS_OP + (8 * i + r2) * 8192 + (8 * rho + k) * 16;
    wait_ready(rs, lane, tlo, thi);
    STAMP(7);
    float hval;
    {
      v4u pv[4];
      poll_fresh<4>(rs, off1, pv);
      STAMP(8);
      f32x4 a = (__builtin_bit_cast(f32x4, pv[0]) + __builtin_bit_cast(f32x4, pv[1])) + (__builtin_bit_cast(f32x4, pv[2]) + __builtin_bit_cast(f32x4, pv[3]));
#pragma unroll
      for (int i = 0; i < 4; ++i) { a[i] += dpp<ROR8>(a[i]); a[i] = x32_sum(x16_sum(a[i])); }
      const float av = (bb & 2) ? ((bb & 1) ? a[3] : a[2]) : ((bb & 1) ? a[1] : a[0]);
      hval = rb(xv + rb(av));
      const unsigned dw = nosent(pk2(hval, dpp<SHL1>(hval)));
      const unsigned dw2 = dppu<SHL2>(dw);
      const unsigned dw4 = dppu<SHL4>(dw), dw6 = dppu<SHL4>(dw2);
      st_sc1(rs, (k == 0 && lane < 32) ? WS_H + rho * 64 + bb * 16 : OOB, v4u{dw, dw2, dw4, dw6});
    }
    STAMP(9);
    {
      v4u pv[7];
      poll_fresh<7>(rs, off2, pv);
      STAMP(10);
      f32x4 a = ((__builtin_bit_cast(f32x4, pv[0]) + __builtin_bit_cast(f32x4, pv[1])) + (__builtin_bit_cast(f32x4, pv[2]) + __builtin_bit_cast(f32x4, pv[3]))) +
                ((__builtin_bit_cast(f32x4, pv[4]) + __builtin_bit_cast(f32x4, pv[5])) + __builtin_bit_cast(f32x4, pv[6]));
#pragma unroll
      for (int i = 0; i < 4; ++i) { a[i] += dpp<ROR8>(a[i]); a[i] = x32_sum(x16_sum(a[i])); }
      const float av = (bb & 2) ? ((bb & 1) ? a[3] : a[2]) : ((bb & 1) ? a[1] : a[0]);
      const float ov = hval + rb(av);
      const unsigned dw = pk2(ov, dpp<SHL1>(ov));
      const unsigned dw2 = dppu<SHL2>(dw);
      const unsigned dw4 = dppu<SHL4>(dw), dw6 = dppu<SHL4>(dw2);
      if (k == 0 && lane < 32) *(GAS v4u*)(out + bb * DM + 8 * rho) = v4u{dw, dw2, dw4, dw6};
      if (c == 192 && lane == 0) __hip_atomic_store((GAS u64*)(ws + WS_EPOCH), tag, __ATOMIC_RELAXED, __HIP_MEMORY_SCOPE_AGENT);
    }
    STAMP(11);
  }
  STAMP_OUT();
}

}

extern "C" void kernel_launch(void* const* d_in, const int* in_sizes, int n_in, void* d_out, int out_size, void* d_ws, size_t ws_size,
                              hipStream_t stream) {
  (void)in_sizes; (void)n_in; (void)out_size; (void)ws_size;
  fused_block<<<256, NTHR, 0, stream>>>((const u16*)d_in[0], (const u16*)d_in[1], (const u16*)d_in[2], (const u16*)d_in[3], (const u16*)d_in[4],
                                        (const u16*)d_in[6], (const u16*)d_in[7], (const u16*)d_in[8], (u16*)d_out, (unsigned char*)d_ws);
}
```

```cpp
#define KV_NT 0
#define W2_NT 0
#define GU_NT 0
#define WQ_POL ""
#include <hip/hip_runtime.h>
#include <hip/hip_bf16.h>
#include <stdint.h>

namespace {

typedef unsigned short u16;
typedef unsigned long long u64;
typedef unsigned v4u __attribute__((ext_vector_type(4)));
typedef unsigned v2u __attribute__((ext_vector_type(2)));
typedef float f32x4 __attribute__((ext_vector_type(4)));
typedef float f32x2 __attribute__((ext_vector_type(2)));
typedef short bf16x8 __attribute__((ext_vector_type(8)));
typedef short s16x4 __attribute__((ext_vector_type(4)));
typedef __bf16 bf2 __attribute__((ext_vector_type(2)));
#define LAS __attribute__((address_space(3)))
#define GAS __attribute__((address_space(1)))

constexpr int DM = 512, HKV = 8, SK = 8192, DH = 128, WIN = 2048, FF = 1792, NQ = 4096;
constexpr int NTHR = 640;

constexpr int WS_EPOCH = 0;
constexpr int WS_RDY = 4096;
constexpr int WS_QG = 8192;
constexpr int WS_PA = WS_QG + 512 * 1024;
constexpr int WS_HP = WS_PA + 512 * 1024;
constexpr int WS_H = WS_HP + 256 * 1024;
constexpr int WS_OP = WS_H + 65536;
constexpr int WS_END = WS_OP + 56 * 8192;
constexpr int OOB = 0x40000000;

constexpr int L_K = 0;
constexpr int L_V = 65536;
constexpr int L_WQ = 131072;
constexpr int L_HN = L_WQ;
constexpr int L_Q = L_WQ + 16384;
constexpr int L_XN = L_Q + 1280;
constexpr int L_O = L_XN;
constexpr int L_QP = L_XN + 1280;
constexpr int L_P = L_QP + 1024;
constexpr int L_RM = L_P + 2304;
constexpr int L_RL = L_RM + 128;
constexpr int L_HH = L_RL + 128;
constexpr int L_ZERO = L_HH + 256;
constexpr int L_FLAG = L_ZERO + 16;
constexpr int L_JUNK = L_FLAG + 16;
constexpr int L_BYTES = L_JUNK + 2048;

constexpr unsigned SENT = 0xFFFFFFFFu;
constexpr int SPIN_CAP = 1 << 16;
constexpr int AUX_SC1 = 16;
constexpr float LOG2E = 1.4426950408889634f;

#define POLL_SLEEP 2
#define ABL_NOW 0
#define CODE_BYTES 16384
#define QPP 1
#define QKP 5
#define QG_DELAY 30
#define WINDOW 3
#define POLL_DEPTH 3
#define P1_EARLY 1
#define GU_WAIT 1
#define PF_NT 0
#define PF_WIN1 0
#define PF_DELAY 0
#define WO_EARLY 1
#define WQ_NT 1

__device__ __forceinline__ float bflo(unsigned x) { return __builtin_bit_cast(float, x << 16); }
__device__ __forceinline__ float bfhi(unsigned x) { return __builtin_bit_cast(float, x & 0xffff0000u); }
__device__ __forceinline__ unsigned pk2(float lo, float hi) {
  bf2 v = {(__bf16)lo, (__bf16)hi};
  return __builtin_bit_cast(unsigned, v);
}
__device__ __forceinline__ float rb(float x) { return (float)(__bf16)x; }
__device__ __forceinline__ unsigned nosent(unsigned x) { return x < 0xFFFFFFFEu ? x : 0xFFFFFFFEu; }
__device__ __forceinline__ v4u nosent4(f32x4 v) {
  return v4u{nosent(__float_as_uint(v[0])), nosent(__float_as_uint(v[1])), nosent(__float_as_uint(v[2])), nosent(__float_as_uint(v[3]))};
}
__device__ __forceinline__ bool fresh(v4u v) { return max(max(v[0], v[1]), max(v[2], v[3])) != SENT; }
__device__ __forceinline__ float vmax(float a, float b) { float r; asm("v_max_f32 %0, %1, %2" : "=v"(r) : "v"(a), "v"(b)); return r; }

__device__ __forceinline__ float vmax3(float a, float b, float c) { float r; asm("v_max3_f32 %0, %1, %2, %3" : "=v"(r) : "v"(a), "v"(b), "v"(c)); return r; }
__device__ __forceinline__ void row_max2(float& a, float& b) {
  asm("s_nop 1\n\t"
      "v_max_f32_dpp %0, %0, %0 row_ror:8 row_mask:0xf bank_mask:0xf\n\tv_max_f32_dpp %1, %1, %1 row_ror:8 row_mask:0xf bank_mask:0xf\n\ts_nop 0\n\t"
      "v_max_f32_dpp %0, %0, %0 row_ror:4 row_mask:0xf bank_mask:0xf\n\tv_max_f32_dpp %1, %1, %1 row_ror:4 row_mask:0xf bank_mask:0xf\n\ts_nop 0\n\t"
      "v_max_f32_dpp %0, %0, %0 row_ror:2 row_mask:0xf bank_mask:0xf\n\tv_max_f32_dpp %1, %1, %1 row_ror:2 row_mask:0xf bank_mask:0xf\n\ts_nop 0\n\t"
      "v_max_f32_dpp %0, %0, %0 row_ror:1 row_mask:0xf bank_mask:0xf\n\tv_max_f32_dpp %1, %1, %1 row_ror:1 row_mask:0xf bank_mask:0xf\n\ts_nop 1"
      : "+v"(a), "+v"(b));
}

__device__ __forceinline__ float dot2(unsigned a, unsigned b, float c) {
  return __builtin_amdgcn_fdot2_f32_bf16(__builtin_bit_cast(bf2, a), __builtin_bit_cast(bf2, b), c, false);
}
__device__ __forceinline__ f32x4 mfma32(v4u a, v4u b, f32x4 c) {
  return __builtin_amdgcn_mfma_f32_16x16x32_bf16(__builtin_bit_cast(bf16x8, a), __builtin_bit_cast(bf16x8, b), c, 0, 0, 0);
}
template <int CTRL> __device__ __forceinline__ float dpp(float x) {
  return __builtin_bit_cast(float, __builtin_amdgcn_mov_dpp(__builtin_bit_cast(int, x), CTRL, 0xf, 0xf, true));
}
template <int CTRL> __device__ __forceinline__ unsigned dppu(unsigned x) { return (unsigned)__builtin_amdgcn_mov_dpp((int)x, CTRL, 0xf, 0xf, true); }
constexpr int ROR8 = 0x128, ROR4 = 0x124, ROR2 = 0x122, ROR1 = 0x121, SHL1 = 0x101, SHL2 = 0x102, SHL4 = 0x104;
__device__ __forceinline__ float row_max(float x) { x = fmaxf(x, dpp<ROR8>(x)); x = fmaxf(x, dpp<ROR4>(x)); x = fmaxf(x, dpp<ROR2>(x)); return fmaxf(x, dpp<ROR1>(x)); }
__device__ __forceinline__ float row_sum(float x) { x += dpp<ROR8>(x); x += dpp<ROR4>(x); x += dpp<ROR2>(x); return x + dpp<ROR1>(x); }
__device__ __forceinline__ float x16_max(float x) { auto s = __builtin_amdgcn_permlane16_swap(__float_as_uint(x), __float_as_uint(x), false, false); return vmax(__uint_as_float(s[0]), __uint_as_float(s[1])); }
__device__ __forceinline__ float x16_sum(float x) { auto s = __builtin_amdgcn_permlane16_swap(__float_as_uint(x), __float_as_uint(x), false, false); return __uint_as_float(s[0]) + __uint_as_float(s[1]); }
__device__ __forceinline__ float x32_sum(float x) { auto s = __builtin_amdgcn_permlane32_swap(__float_as_uint(x), __float_as_uint(x), false, false); return __uint_as_float(s[0]) + __uint_as_float(s[1]); }
__device__ __forceinline__ float wave_sum(float x) { return x32_sum(x16_sum(row_sum(x))); }
__device__ __forceinline__ f32x4 sel2(bool c, f32x4 a, f32x4 b) {
  return f32x4{c ? b[0] : a[0], c ? b[1] : a[1], c ? b[2] : a[2], c ? b[3] : a[3]};
}
__device__ __forceinline__ f32x4 sel4(int g, f32x4 a, f32x4 b, f32x4 c, f32x4 d) { return sel2(g & 2, sel2(g & 1, a, b), sel2(g & 1, c, d)); }

#define PIN(x) asm volatile("" : "+v"(x))
#define SCHED() __builtin_amdgcn_sched_barrier(0)
#define LDS_FENCE() asm volatile("s_waitcnt lgkmcnt(0)" ::: "memory")
#define BARRIER() do { asm volatile("s_waitcnt lgkmcnt(0)\n\ts_barrier" ::: "memory"); } while (0)
#define VMWAIT(N) asm volatile("s_waitcnt vmcnt(" #N ")" ::: "memory")
#define STAMP(i) do {} while (0)
#define STAMP_OUT() do {} while (0)

#define EXIT() do { STAMP_OUT(); asm volatile("s_endpgm" ::: "memory"); __builtin_unreachable(); } while (0)

__device__ __forceinline__ v4u make_srd(const void* p, unsigned bytes) {
  const u64 a = (u64)p;
  return v4u{(unsigned)a, (unsigned)(a >> 32) & 0xffffu, bytes, 0x00020000u};
}
template <int NT> __device__ __forceinline__ void dma_buf(unsigned lds, int voff, v4u srd, int soff) {
  if (NT) asm volatile("s_mov_b32 m0, %0\n\ts_nop 0\n\tbuffer_load_dwordx4 %1, %2, %3 offen nt lds" :: "s"(lds), "v"(voff), "s"(srd), "s"(soff) : "m0", "memory");
  else asm volatile("s_mov_b32 m0, %0\n\ts_nop 0\n\tbuffer_load_dwordx4 %1, %2, %3 offen lds" :: "s"(lds), "v"(voff), "s"(srd), "s"(soff) : "m0", "memory");
}
template <int NT> __device__ __forceinline__ void dma_glb(unsigned lds, const void* p) {
  if (NT) asm volatile("s_mov_b32 m0, %0\n\ts_nop 0\n\tglobal_load_lds_dwordx4 %1, off nt" :: "s"(lds), "v"(p) : "m0", "memory");
  else asm volatile("s_mov_b32 m0, %0\n\ts_nop 0\n\tglobal_load_lds_dwordx4 %1, off" :: "s"(lds), "v"(p) : "m0", "memory");
}
__device__ __forceinline__ void ld16_asm(v4u& d, int voff, v4u srd, int soff) {
  asm volatile("buffer_load_dwordx4 %0, %1, %2, %3 offen" : "=v"(d) : "v"(voff), "s"(srd), "s"(soff) : "memory");
}
__device__ __forceinline__ void ld16_sc1_asm(v4u& d, int voff, v4u srd) {
  asm volatile("buffer_load_dwordx4 %0, %1, %2, 0 offen sc1" : "=v"(d) : "v"(voff), "s"(srd) : "memory");
}
__device__ __forceinline__ void ld8_sc1_asm(v2u& d, v4u srd) {
  asm volatile("buffer_load_dwordx2 %0, off, %1, 0 sc1" : "=v"(d) : "s"(srd) : "memory");
}
__device__ __forceinline__ void st16_sc1_asm(v4u v, int voff, v4u srd) {
  asm volatile("buffer_store_dwordx4 %0, %1, %2, 0 offen sc1\n\ts_nop 1" :: "v"(v), "v"(voff), "s"(srd) : "memory");
}

__device__ __forceinline__ void st8_sc1_asm(v2u v, int voff, v4u srd) {
  asm volatile("buffer_store_dwordx2 %0, %1, %2, 0 offen sc1" :: "v"(v), "v"(voff), "s"(srd) : "memory");
}
__device__ __forceinline__ float sel4f(int b, f32x4 a) { const float lo = (b & 1) ? a[1] : a[0], hi = (b & 1) ? a[3] : a[2]; float r = (b & 2) ? hi : lo; return r; }

__device__ __forceinline__ v4u ld_sc1(__amdgpu_buffer_rsrc_t rs, int off) { return __builtin_amdgcn_raw_buffer_load_b128(rs, off, 0, AUX_SC1); }
__device__ __forceinline__ void st_sc1(__amdgpu_buffer_rsrc_t rs, int off, v4u v) { __builtin_amdgcn_raw_buffer_store_b128(v, rs, off, 0, AUX_SC1); }
__device__ __forceinline__ v2u tr_rd(LAS unsigned char* L, int off) {
  return __builtin_bit_cast(v2u, __builtin_amdgcn_ds_read_tr16_b64_v4i16((LAS s16x4*)(L + off)));
}
__device__ __forceinline__ v4u tr_frag(LAS unsigned char* L, int off0, int off1) {
  const v2u a = tr_rd(L, off0), b = tr_rd(L, off1);
  return v4u{a[0], a[1], b[0], b[1]};
}

__device__ __forceinline__ void ld16_sc1_rw(v4u& d, int voff, v4u srd) {
  asm volatile("buffer_load_dwordx4 %0, %1, %2, 0 offen sc1" : "+v"(d) : "v"(voff), "s"(srd) : "memory");
}
template <int CNT> __device__ __forceinline__ void vmwait_pin(v4u& a) { asm volatile("s_waitcnt vmcnt(%1)" : "+v"(a) : "n"(CNT) : "memory"); }
template <int N> __device__ __forceinline__ void poll_start(v4u srd, const int (&off)[N], v4u (&v)[N]) {
#pragma unroll
  for (int i = 0; i < N; ++i) v[i] = v4u{SENT, SENT, SENT, SENT};
#pragma unroll
  for (int k = 0; k < POLL_DEPTH - 1; ++k) {
#pragma unroll
    for (int i = 0; i < N; ++i) ld16_sc1_rw(v[i], off[i], srd);
    __builtin_amdgcn_s_sleep(POLL_SLEEP);
  }
}
template <int N> __device__ __forceinline__ void poll_finish(v4u srd, const int (&off)[N], v4u (&v)[N]) {
#pragma unroll 1
  for (int it = 0; it < SPIN_CAP; ++it) {
#pragma unroll
    for (int i = 0; i < N; ++i) ld16_sc1_rw(v[i], off[i], srd);
    vmwait_pin<(POLL_DEPTH - 1) * N>(v[0]);
#pragma unroll
    for (int i = 1; i < N; ++i) PIN(v[i]);
    unsigned m = max(max(v[0][0], v[0][1]), max(v[0][2], v[0][3]));
#pragma unroll
    for (int i = 1; i < N; ++i) m = max(max(m, max(v[i][0], v[i][1])), max(v[i][2], v[i][3]));
    if (__all(m != SENT)) break;
    __builtin_amdgcn_s_sleep(POLL_SLEEP);
  }
}
template <int N> __device__ __forceinline__ void poll_fresh(v4u srd, const int (&off)[N], v4u (&v)[N]) {
  poll_start<N>(srd, off, v);
  poll_finish<N>(srd, off, v);
}
template <int N> __device__ __forceinline__ void poll_drain(v4u (&v)[N]) {
  vmwait_pin<0>(v[0]);
#pragma unroll
  for (int i = 1; i < N; ++i) PIN(v[i]);
}
__device__ __forceinline__ void wait_ready(__amdgpu_buffer_rsrc_t rs, int lane, unsigned tlo, unsigned thi) {
#pragma unroll 1
  for (int it = 0; it < SPIN_CAP; ++it) {
    asm volatile("" ::: "memory");
    const v4u r0 = ld_sc1(rs, WS_RDY + lane * 16), r1 = ld_sc1(rs, WS_RDY + 1024 + lane * 16);
    const bool ok = (r0[0] == tlo) & (r0[1] == thi) & (r0[2] == tlo) & (r0[3] == thi) & (r1[0] == tlo) & (r1[1] == thi) & (r1[2] == tlo) & (r1[3] == thi);
    if (__all(ok)) break;
    __builtin_amdgcn_s_sleep(4);
  }
}

__global__ void __launch_bounds__(NTHR, 3)
fused_block(const u16* __restrict__ x, const u16* __restrict__ kc, const u16* __restrict__ vc, const u16* __restrict__ wq,
            const u16* __restrict__ wo, const u16* __restrict__ wg, const u16* __restrict__ wu, const u16* __restrict__ wd,
            u16* __restrict__ out, unsigned char* __restrict__ ws) {
  __shared__ __attribute__((aligned(16))) unsigned char smem_[L_BYTES];
  {
    u64 pc0;
    asm volatile("s_getpc_b64 %0" : "=s"(pc0));
    if (__builtin_amdgcn_readfirstlane(threadIdx.x >> 6) == 1) {
      const v4u srd_c = make_srd((const void*)(pc0 & ~255ull), CODE_BYTES);
      const int so = ((blockIdx.x >> 3) & 15) * 2048;
      asm volatile("s_mov_b32 m0, %0\n\ts_nop 0\n\tbuffer_load_dwordx4 %1, %2, %3 offen lds\n\tbuffer_load_dwordx4 %1, %2, %3 offen offset:1024 lds"
                   :: "s"((unsigned)(size_t)(LAS unsigned char*)smem_ + L_JUNK), "v"((threadIdx.x & 63) * 16), "s"(srd_c), "s"(so) : "m0", "memory");
    }
    __builtin_amdgcn_sched_barrier(0);
  }
  LAS unsigned char* L = (LAS unsigned char*)smem_;
  const unsigned lbase = (unsigned)(size_t)L;
  const int c = blockIdx.x, kvh = c & 7, i32 = c >> 3;
  const int lane = threadIdx.x & 63, wv = __builtin_amdgcn_readfirstlane(threadIdx.x >> 6);
  const int w = wv < 2 ? 8 + wv : wv - 2;
  const int n = lane & 15, g = lane >> 4;
  const v4u srd_ws = make_srd(ws, WS_END);
  STAMP(0);

  const int b = i32 >> 3, s = i32 & 7;
  const size_t kvbase = ((size_t)(b * HKV + kvh) * SK + (SK - WIN) + s * 256) * DH;
  const v4u srd_k = make_srd(kc + kvbase, 65536);
  const int rot = w + 37 * c;
  const int kv0 = g * 256 + ((n ^ g) * 16);
  const unsigned ldst = lbase + w * 1024;
  v2u ep;
  v4u xr[4];
#define KLOAD(i) dma_buf<KV_NT>(ldst + L_K + (i) * 8192, kv0 ^ (((i) & 3) << 6), srd_k, ((8 * (i) + rot) & 63) << 10)
  if (wv == 0) {
    const v4u srd_x = make_srd(x, 4 * DM * 2);
    const v4u srd_wq = make_srd(wq + (size_t)b * 128 * NQ + kvh * 512 + s * 64, 128 * NQ * 2);
    ld8_sc1_asm(ep, srd_ws);
#pragma unroll
    for (int i = 0; i < 4; ++i) ld16_asm(xr[i], g * 1024 + n * 16, srd_x, ((b + i) & 3) * 256);
    const int row = lane >> 3, v0 = row * (NQ * 2) + (((lane & 7) ^ (2 * ((row >> 1) & 1))) * 16);
    int so;
#define WQ2 "buffer_load_dwordx4 %1, %3, %0 offen " WQ_POL " lds\n\ts_addk_i32 m0, 0x400\n\ts_add_u32 %0, %0, %5\n\t" \
            "buffer_load_dwordx4 %2, %3, %0 offen " WQ_POL " lds\n\ts_addk_i32 m0, 0x400\n\ts_add_u32 %0, %0, %5\n\t"
    asm volatile("s_mov_b32 m0, %4\n\ts_mov_b32 %0, 0\n\t" WQ2 WQ2 WQ2 WQ2 WQ2 WQ2 WQ2 WQ2
                 : "=&s"(so) : "v"(v0), "v"(v0 ^ 64), "s"(srd_wq), "s"(lbase + L_WQ), "s"(8 * NQ * 2) : "m0", "scc", "memory");
    *(LAS unsigned*)(L + L_FLAG) = 0u;
    LDS_FENCE();
  }
  asm volatile("s_barrier" ::: "memory");
  if (wv >= 2) { KLOAD(0); KLOAD(1); KLOAD(2); }
  int wv2 = wv;
  asm volatile("" : "+s"(wv2));

  if (wv2 == 1) {
    v2u ep1;
    ld8_sc1_asm(ep1, srd_ws);
    const v4u sent = {SENT, SENT, SENT, SENT};
    st16_sc1_asm(sent, WS_PA + c * 2048 + lane * 16, srd_ws);
    st16_sc1_asm(sent, WS_PA + c * 2048 + 1024 + lane * 16, srd_ws);
    const int base = i32 < 16 ? WS_HP + (kvh * 4 + (i32 >> 2)) * 8192 + (i32 & 3) * 2048 : WS_OP + (kvh * 7 + i32 - 16) * 8192;
    const int len = i32 < 16 ? 2048 : i32 < 23 ? 8192 : 0;
#pragma unroll 1
    for (int o = 0; o < len; o += 1024) st16_sc1_asm(sent, base + o + lane * 16, srd_ws);
    st16_sc1_asm(sent, (i32 >= 24 && lane < 32) ? WS_H + (lane >> 2) * 8192 + ((i32 - 24) * 8 + kvh) * 128 + (lane & 3) * 16 : OOB, srd_ws);
    *(LAS v4u*)(L + L_ZERO) = v4u{0u, 0u, 0u, 0u};
    asm volatile("s_waitcnt vmcnt(0)" : "+v"(ep1) :: "memory");
    const u64 tag = (((u64)ep1[1] << 32) | ep1[0]) + 0x9E3779B97F4A7C15ull;
    st8_sc1_asm(v2u{(unsigned)tag, (unsigned)(tag >> 32)}, lane == 0 ? WS_RDY + (kvh * 32 + i32) * 8 : OOB, srd_ws);
    STAMP(1);
    EXIT();
  }
  if (wv2 == 0) {
    const int rq = b, cq = s;
    STAMP(7);
    asm volatile("s_waitcnt vmcnt(16)" : "+v"(ep), "+v"(xr[0]), "+v"(xr[1]), "+v"(xr[2]), "+v"(xr[3]) :: "memory");
    STAMP(9);
    {
      float s0 = 0.f, s1 = 0.f, q0 = 0.f, q1 = 0.f;
#pragma unroll
      for (int i = 0; i < 4; ++i)
#pragma unroll
        for (int k = 0; k < 4; k += 2) {
          s0 = dot2(xr[i][k], 0x3F803F80u, s0); s1 = dot2(xr[i][k + 1], 0x3F803F80u, s1);
          q0 = dot2(xr[i][k], xr[i][k], q0); q1 = dot2(xr[i][k + 1], xr[i][k + 1], q1);
        }
      const float mean = row_sum(s0 + s1) * (1.0f / DM);
      const float q = fmaxf(fmaf(-mean * DM, mean, row_sum(q0 + q1)), 0.f);
      float v[8];
#pragma unroll
      for (int k = 0; k < 4; ++k) { v[2 * k] = bflo(xr[0][k]); v[2 * k + 1] = bfhi(xr[0][k]); }
      const float mb = rb(mean);
      const float inv = __builtin_amdgcn_rcpf(rb(__builtin_amdgcn_sqrtf(rb(rb(q * (1.0f / DM)) + 1e-5f))));
      v4u o;
#pragma unroll
      for (int k = 0; k < 4; ++k) o[k] = pk2(rb(v[2 * k] - mb) * inv, rb(v[2 * k + 1] - mb) * inv);
      *(LAS v4u*)(L + L_XN + g * 320 + n * 16) = o;
    }
    STAMP(1);
    LDS_FENCE();
    f32x4 acc[4];
    {
      v4u xa[4];
#pragma unroll
      for (int ks = 0; ks < 4; ++ks) xa[ks] = *(LAS v4u*)(L + L_XN + (n & 3) * 320 + ks * 64 + g * 16);
      const int q4 = n >> 2, p = n & 3, y = ((q4 >> 1) & 1) | ((g & 1) << 1);
      const int rbase = L_WQ + (8 * g + q4) * 128 + 8 * (p & 1);
      int o[4];
#pragma unroll
      for (int t = 0; t < 4; ++t) { o[t] = rbase + (((2 * t + (p >> 1)) ^ (2 * y)) * 16); acc[t] = f32x4{0.f, 0.f, 0.f, 0.f}; }
#pragma unroll
      for (int ks = 0; ks < 4; ++ks) {
        if (ks == 0) VMWAIT(12); else if (ks == 1) VMWAIT(8); else if (ks == 2) VMWAIT(4); else VMWAIT(0);
        if (ks == 3) STAMP(4);
#pragma unroll
        for (int t = 0; t < 4; ++t) acc[t] = mfma32(xa[ks], tr_frag(L, o[t] + ks * 4096, o[t] + ks * 4096 + 512), acc[t]);
      }
    }
    *(LAS f32x4*)(L + L_QP + lane * 16) = sel4(g, acc[0], acc[1], acc[2], acc[3]);
    LDS_FENCE();
    const f32x4 q0 = *(LAS f32x4*)(L + L_QP + (lane & 31) * 32), q1 = *(LAS f32x4*)(L + L_QP + (lane & 31) * 32 + 16);
    const u64 tag = (((u64)ep[1] << 32) | ep[0]) + 0x9E3779B97F4A7C15ull;
    const unsigned tlo = (unsigned)tag, thi = (unsigned)(tag >> 32);
    STAMP(5);
    STAMP(2);
    {
      const bool up = lane >= 32;
      const int o = WS_QG + ((kvh * 4 + (up ? 1 : 0)) * 4 + rq) * 4096 + (32 * cq + (lane & 31)) * 16;
      st16_sc1_asm(v4u{__float_as_uint(up ? q0[1] : q0[0]), __float_as_uint(up ? q1[1] : q1[0]), tlo, thi}, o, srd_ws);
      st16_sc1_asm(v4u{__float_as_uint(up ? q0[3] : q0[2]), __float_as_uint(up ? q1[3] : q1[2]), tlo, thi}, o + 2 * 4 * 4096, srd_ws);
    }
    STAMP(3);
    __builtin_amdgcn_s_sleep(QG_DELAY);
    const __amdgpu_buffer_rsrc_t rs0 = __builtin_amdgcn_make_buffer_rsrc(ws, 0, WS_END, 0x00020000);
    const int qo = WS_QG + (kvh * 4 + b) * 16384 + lane * 16;
    v4u qg[16];
#pragma unroll 1
    for (int it = 0; it < SPIN_CAP; ++it) {
      asm volatile("" ::: "memory");
      bool ok = true;
#pragma unroll
      for (int i = 0; i < 16; ++i) qg[i] = ld_sc1(rs0, qo + i * 1024);
#pragma unroll
      for (int i = 0; i < 16; ++i) ok &= (qg[i][2] == tlo) & (qg[i][3] == thi);
      if (__all(ok)) break;
    }
#pragma unroll
    for (int u = 0; u < 4; ++u) {
      const float a = (__uint_as_float(qg[u][0]) + __uint_as_float(qg[4 + u][0])) + (__uint_as_float(qg[8 + u][0]) + __uint_as_float(qg[12 + u][0]));
      const float bq = (__uint_as_float(qg[u][1]) + __uint_as_float(qg[4 + u][1])) + (__uint_as_float(qg[8 + u][1]) + __uint_as_float(qg[12 + u][1]));
      *(LAS unsigned*)(L + L_Q + u * 320 + lane * 4) = pk2(a, bq);
    }
    LDS_FENCE();
    *(volatile LAS unsigned*)(L + L_FLAG) = 1u;
    STAMP(6);
    EXIT();
  }

  const bool is_wo = i32 < 16, is_ffn = i32 >= 16 && i32 < 23, is_red = i32 >= 24;
  const int jf = kvh * 7 + (i32 - 16);
  const int rho = (i32 - 24) * 8 + kvh;
  const __amdgpu_buffer_rsrc_t rs = __builtin_amdgcn_make_buffer_rsrc(ws, 0, WS_END, 0x00020000);
  const v4u srd_v = make_srd(vc + kvbase, 65536);
  const int vv0 = g * 256 + ((n ^ ((g << 2) | (w & 3))) * 16);
  const int hp_off = WS_HP + (kvh * 4 + (i32 >> 2)) * 8192 + (i32 & 3) * 2048 + w * 256;
  asm volatile("" :: "s"(wo), "s"(wg), "s"(wu), "s"(wd), "s"(out), "s"(x));
#define VLOAD(i) dma_buf<KV_NT>(ldst + L_V + (i) * 8192, vv0, srd_v, ((8 * (i) + rot) & 63) << 10)
  v2u ep2;
  ld8_sc1_asm(ep2, srd_ws);
#define WW() VMWAIT(2)
  VMWAIT(3);
  STAMP(12);
  KLOAD(3);
  VMWAIT(3); KLOAD(4); VMWAIT(3); KLOAD(5);
  asm volatile("s_waitcnt vmcnt(2)" : "+v"(ep2) :: "memory");
  KLOAD(6);
  WW(); KLOAD(7);
  STAMP(1);
  WW(); VLOAD(0); WW(); VLOAD(1); WW(); VLOAD(2);
  constexpr int VNEXT = 3;
  const u64 tag = (((u64)ep2[1] << 32) | ep2[0]) + 0x9E3779B97F4A7C15ull;
  const unsigned tlo = (unsigned)tag, thi = (unsigned)(tag >> 32);
  v4u kf[8];
  {
    const int kb = L_K + (n >> 2) * 8192 + w * 1024 + (n & 3) * 256 + ((g ^ n) * 16);
#pragma unroll
    for (int ks = 0; ks < 4; ++ks) { kf[ks] = *(LAS v4u*)(L + (kb ^ (ks << 6))); kf[4 + ks] = *(LAS v4u*)(L + (kb ^ (ks << 6)) + 32768); }
  }
  int qb = L_Q + (2 * (n >> 3) + (n & 1)) * 320 + g * 16;
  int rmw = L_RM + (g >> 1) * 64 + w * 8, rmr = L_RM + (g >> 1) * 64;
  int po = L_P + (g >> 1) * 1152 + ((4 * (g & 1) + (n >> 2)) * 32 + 4 * w + (n & 3)) * 2;
  const u16* wp0 = is_wo ? wo + (size_t)((kvh * 4 + (i32 >> 2)) * 128 + 4 * w) * DM + (i32 & 3) * 128 + g * (DM - 128) + (vv0 >> 1)
                         : (lane >= 32 ? wu : wg) + (size_t)(64 * w + (i32 & 7) + (i32 & 8) * 4) * FF + ((224 * kvh) & ~63) + 8 * (lane & 31);
  PIN(wp0);
  const int tq = n >> 2, tp = n & 3;
  const int tr0 = (8 * g + tq) * 256 + (((2 * w + (tp >> 1)) ^ ((tq << 2) | ((2 * g) & 3))) * 16) + 8 * (tp & 1);
  const int tr1 = (8 * g + 4 + tq) * 256 + (((2 * w + (tp >> 1)) ^ ((tq << 2) | ((2 * g + 1) & 3))) * 16) + 8 * (tp & 1);
  int tv0 = L_V + tr0, tv1 = L_V + tr1, pab = L_P + (n >> 2) * 576 + g * 16;
  PIN(tv0); PIN(tv1); PIN(pab);
  PIN(qb); PIN(rmw); PIN(rmr); PIN(po);
#pragma unroll
  for (int i = 0; i < 8; ++i) PIN(kf[i]);
#pragma unroll
  for (int i = VNEXT; i <= QKP; ++i) { WW(); VLOAD(i); }
  STAMP(13);
#pragma unroll 1
  for (int it = 0; it < (SPIN_CAP << 4); ++it) {
    if (__builtin_amdgcn_readfirstlane(*(volatile LAS unsigned*)(L + L_FLAG)) != 0u) break;
  }
  asm volatile("" ::: "memory");
  STAMP(3);
  float sc[2];
  {
    v4u qa[4];
#pragma unroll
    for (int ks = 0; ks < 4; ++ks) qa[ks] = *(LAS v4u*)(L + qb + ks * 64);
    f32x4 d0 = {0.f, 0.f, 0.f, 0.f}, d1 = {0.f, 0.f, 0.f, 0.f};
#pragma unroll
    for (int ks = 0; ks < 4; ++ks) { d0 = mfma32(qa[ks], kf[ks], d0); d1 = mfma32(qa[ks], kf[4 + ks], d1); }
#pragma unroll
    for (int j = 0; j < 2; ++j) sc[j] = rb(rb((g & 1) ? d1[j] : d0[j]) * 0.08838834764831845f);
    float m0 = sc[0], m1 = sc[1];
    row_max2(m0, m1);
    const f32x2 mx = {x16_max(m0), x16_max(m1)};
    *(LAS f32x2*)(L + rmw) = mx;
  }
#pragma unroll
  for (int i = QKP + 1; i < 8; ++i) VLOAD(i);
  STAMP(2);
  BARRIER();
  float mh;
  {
    f32x4 t[4];
#pragma unroll
    for (int i = 0; i < 4; ++i) t[i] = *(LAS f32x4*)(L + rmr + i * 16);
    const float M0 = vmax3(vmax3(t[0][0], t[0][2], t[1][0]), vmax3(t[1][2], t[2][0], t[2][2]), vmax(t[3][0], t[3][2]));
    const float M1 = vmax3(vmax3(t[0][1], t[0][3], t[1][1]), vmax3(t[1][3], t[2][1], t[2][3]), vmax(t[3][1], t[3][3]));
    const float p0 = __builtin_amdgcn_exp2f((sc[0] - M0) * LOG2E), p1 = __builtin_amdgcn_exp2f((sc[1] - M1) * LOG2E);
    const unsigned pp = pk2(p0, p1);
    *(LAS u16*)(L + po) = (u16)pp;
    *(LAS u16*)(L + po + 576) = (u16)(pp >> 16);
    const f32x2 ls = {x16_sum(row_sum(p0)), x16_sum(row_sum(p1))};
    *(LAS f32x2*)(L + rmw + (L_RL - L_RM)) = ls;
    mh = (g & 1) ? M1 : M0;
  }
  const bool chk = is_wo && w == 0;
  const int roff = WS_RDY + kvh * 256 + n * 16;
  v4u rdy;
  ld16_sc1_asm(rdy, chk ? roff : OOB, srd_ws);
#pragma unroll
  for (int i = 0; i < 4; ++i) dma_glb<W2_NT>(ldst + L_K + i * 8192, is_wo ? wp0 + i * 32 * DM : x);
  VMWAIT(7);
  STAMP(14);
  BARRIER();
  {
    v4u pa[8];
#pragma unroll
    for (int i = 0; i < 8; ++i) pa[i] = *(LAS v4u*)(L + pab + i * 64);
    f32x4 t[4];
#pragma unroll
    for (int i = 0; i < 4; ++i) t[i] = *(LAS f32x4*)(L + rmr + (L_RL - L_RM) + i * 16);
    f32x4 acc = {0.f, 0.f, 0.f, 0.f};
#pragma unroll
    for (int i = 0; i < 6; ++i) acc = mfma32(pa[i], tr_frag(L, tv0 + i * 8192, tv1 + i * 8192), acc);
    const f32x4 ts = (t[0] + t[1]) + (t[2] + t[3]);
    unsigned ml[2] = {nosent(__float_as_uint(mh)), nosent(__float_as_uint((g & 1) ? ts[1] + ts[3] : ts[0] + ts[2]))};
    int pao = (n & 3) == 0 ? WS_PA + c * 2048 + w * 256 + g * 64 + (n >> 2) * 16 : OOB;
    PIN(ml[0]); PIN(ml[1]); PIN(pao); PIN(acc); PIN(pa[6]); PIN(pa[7]);
    STAMP(15);
    asm volatile("s_waitcnt vmcnt(4)" : "+v"(rdy) :: "memory");
    STAMP(5);
    BARRIER();
#pragma unroll
    for (int i = 6; i < 8; ++i) acc = mfma32(pa[i], tr_frag(L, tv0 + i * 8192, tv1 + i * 8192), acc);
    const unsigned d0 = nosent(pk2(acc[0], dpp<SHL1>(acc[0])));
    st_sc1(rs, pao, v4u{d0, dppu<SHL2>(d0), ml[0], ml[1]});
  }
  STAMP(6);

  if (is_wo) {
    const int hh = i32 >> 2;
    int off[2];
#pragma unroll
    for (int i = 0; i < 2; ++i) off[i] = WS_PA + (kvh + 8 * (8 * (n & 3) + 2 * (n >> 2) + i)) * 2048 + w * 256 + hh * 64 + g * 16;
    if (chk) {
#pragma unroll 1
      for (int it = 0; it < SPIN_CAP; ++it) {
        if (__all((rdy[0] == tlo) & (rdy[1] == thi) & (rdy[2] == tlo) & (rdy[3] == thi))) break;
        asm volatile("" ::: "memory");
        rdy = ld_sc1(rs, roff);
      }
    }
    if (WO_EARLY) BARRIER();
    v4u pv[2];
    if (WO_EARLY) poll_start<2>(srd_ws, off, pv);
    if (WO_EARLY) asm volatile("s_waitcnt vmcnt(%0)" :: "n"(1 + 2 * (POLL_DEPTH - 1)) : "memory"); else VMWAIT(1);
    BARRIER();
    v4u bf[4];
#pragma unroll
    for (int i = 0; i < 4; ++i) bf[i] = tr_frag(L, L_K + tr0 + i * 8192, L_K + tr1 + i * 8192);
    PIN(bf[0]); PIN(bf[1]); PIN(bf[2]); PIN(bf[3]);
    STAMP(7);
    int ob = L_O + (n & 3) * 320 + g * 16, ow = L_O + (n & 3) * 320 + w * 32 + g * 8, so = lane < 16 ? hp_off + lane * 16 : OOB;
    PIN(ob); PIN(ow); PIN(so);
    if (WO_EARLY) poll_finish<2>(srd_ws, off, pv); else poll_fresh<2>(srd_ws, off, pv);
    STAMP(8);
    const float m0 = __uint_as_float(pv[0][2]), m1 = __uint_as_float(pv[1][2]);
    float mm = vmax(m0, m1);
    mm = vmax(mm, dpp<ROR4>(mm)); mm = vmax(mm, dpp<ROR8>(mm));
    const float e0 = __builtin_amdgcn_exp2f((m0 - mm) * LOG2E), e1 = __builtin_amdgcn_exp2f((m1 - mm) * LOG2E);
    float t[5];
    t[0] = e0 * bflo(pv[0][0]) + e1 * bflo(pv[1][0]);
    t[1] = e0 * bfhi(pv[0][0]) + e1 * bfhi(pv[1][0]);
    t[2] = e0 * bflo(pv[0][1]) + e1 * bflo(pv[1][1]);
    t[3] = e0 * bfhi(pv[0][1]) + e1 * bfhi(pv[1][1]);
    t[4] = e0 * __uint_as_float(pv[0][3]) + e1 * __uint_as_float(pv[1][3]);
#pragma unroll
    for (int i = 0; i < 5; ++i) { t[i] += dpp<ROR4>(t[i]); t[i] += dpp<ROR8>(t[i]); }
    const float inv = __builtin_amdgcn_rcpf(t[4]);
    *(LAS v2u*)(L + ow) = v2u{pk2(t[0] * inv, t[1] * inv), pk2(t[2] * inv, t[3] * inv)};
    BARRIER();
    v4u oa[4];
#pragma unroll
    for (int i = 0; i < 4; ++i) oa[i] = *(LAS v4u*)(L + ob + i * 64);
    SCHED();
    f32x4 acc = {0.f, 0.f, 0.f, 0.f};
#pragma unroll
    for (int i = 0; i < 4; ++i) acc = mfma32(oa[i], bf[i], acc);
    st_sc1(rs, so, nosent4(acc));
    STAMP(9);
    poll_drain<2>(pv);
    EXIT();
  }
  __builtin_amdgcn_s_sleep(PF_DELAY);
#pragma unroll
  for (int i = 0; i < 4; ++i) { dma_glb<PF_NT>(ldst + L_K + 32768, wp0 + i * 8 * FF); if (PF_WIN1 && i == 1) VMWAIT(0); }
  if (is_ffn) {
    const u16* gup = ((lane >> 2) & 1 ? wu : wg) + (size_t)(64 * w + (lane >> 3)) * FF + 32 * jf + 8 * ((lane & 3) ^ (g & 1));
    const int alt = (lane & 2) ? -16 : 16;
    {
      const v4u srd_d = make_srd(wd + (size_t)(32 * jf + 4 * w) * DM, 4 * DM * 2);
#pragma unroll
      for (int i = 0; i < 4; ++i) dma_buf<W2_NT>(ldst + L_K + i * 8192, vv0 + g * (DM * 2 - 256), srd_d, i * 256);
    }
    STAMP(20);
    VMWAIT(4);
    STAMP(21);
    BARRIER();
    STAMP(22);
#pragma unroll
    for (int i = 0; i < 8; ++i) dma_glb<GU_NT>(lbase + L_V + w * 8192 + i * 1024, gup + i * 8 * FF + ((i & 1) ? alt : 0));
    STAMP(10);
    v4u bd[4];
    int ao[4], so = WS_OP + jf * 8192 + w * 1024 + lane * 16;
#pragma unroll
    for (int i = 0; i < 4; ++i) { ao[i] = (n >> 2) == i ? L_HH + (n & 3) * 64 + g * 16 : L_ZERO; PIN(ao[i]); }
    PIN(so);
    STAMP(23);
    if (w >= 4) wait_ready(rs, lane, tlo, thi);
    STAMP(24);
    VMWAIT(0);
    STAMP(11);
    BARRIER();
    STAMP(25);
    v4u hv[1];
    int uo = L_V + (8 * g + tq) * 128 + ((4 * (tp >> 1) + ((w & 3) ^ (((tq >> 1) & 1) | ((g & 1) << 1)))) * 16) + 8 * (tp & 1);
    int d0 = L_K + (w >> 1) * 8192 + (tr0 ^ (((8 * (w & 1)) ^ (2 * w)) << 4)), d1 = L_K + (w >> 1) * 8192 + (tr1 ^ (((8 * (w & 1)) ^ (2 * w)) << 4));
    PIN(uo); PIN(d0); PIN(d1);
#define DOWN() do { BARRIER(); v4u a4[4]; \
      _Pragma("unroll") for (int i = 0; i < 4; ++i) a4[i] = *(LAS v4u*)(L + ao[i]); \
      SCHED(); f32x4 d = {0.f, 0.f, 0.f, 0.f}; \
      _Pragma("unroll") for (int i = 0; i < 4; ++i) d = mfma32(a4[i], bd[i], d); \
      st_sc1(rs, so, nosent4(d)); SCHED(); STAMP(9); } while (0)
    if (w >= 4) {
      f32x4 c4 = {0.f, 0.f, 0.f, 0.f};
#pragma unroll
      for (int i = 0; i < 16; ++i) c4 = mfma32(v4u{0x3F803F80u, 0x3F803F80u, 0x3F803F80u, 0x3F803F80u}, tr_frag(L, uo + i * 4096, uo + i * 4096 + 512), c4);
      *(LAS float*)(L + L_QP + (w & 3) * 64 + n * 4) = c4[0];
#pragma unroll
      for (int i = 0; i < 4; ++i) bd[i] = tr_frag(L, d0 ^ (i << 5), d1 ^ (i << 5));
      int hr = L_HN + (w & 3) * 1088 + lane * 16;
      PIN(hr); PIN(bd[0]); PIN(bd[1]); PIN(bd[2]); PIN(bd[3]);
      STAMP(7);
      const int off[1] = {WS_H + kvh * 8192 + lane * 128 + (w & 3) * 16};
      poll_fresh<1>(srd_ws, off, hv);
      STAMP(8);
      *(LAS v4u*)(L + hr) = hv[0];
      BARRIER();
      const float s0 = dot2(hv[0][0], 0x3F803F80u, dot2(hv[0][1], 0x3F803F80u, 0.f)) + dot2(hv[0][2], 0x3F803F80u, dot2(hv[0][3], 0x3F803F80u, 0.f));
      const float q0 = dot2(hv[0][0], hv[0][0], dot2(hv[0][1], hv[0][1], 0.f)) + dot2(hv[0][2], hv[0][2], dot2(hv[0][3], hv[0][3], 0.f));
      const float mean = wave_sum(s0) * (1.0f / DM);
      const float q = fmaxf(fmaf(-mean * DM, mean, wave_sum(q0)), 0.f);
      const float inv = __builtin_amdgcn_rcpf(rb(__builtin_amdgcn_sqrtf(rb(rb(q * (1.0f / DM)) + 1e-5f))));
      *(LAS f32x2*)(L + L_RM + (w & 3) * 8) = f32x2{rb(mean), inv};
      STAMP(27);
      BARRIER();
      DOWN();
      poll_drain<1>(hv);
      EXIT();
    }
    {
      v4u bf[16];
#pragma unroll
      for (int i = 0; i < 16; ++i) bf[i] = tr_frag(L, uo + i * 4096, uo + i * 4096 + 512);
#pragma unroll
      for (int i = 0; i < 4; ++i) bd[i] = tr_frag(L, d0 ^ (i << 5), d1 ^ (i << 5));
      int hb = L_HN + (n >> 2) * 1088 + g * 16, hw = n < 8 ? L_HH + g * 64 + (8 * w + n) * 2 : L_JUNK;
      PIN(hb); PIN(hw); PIN(bd[0]); PIN(bd[1]); PIN(bd[2]); PIN(bd[3]);
#pragma unroll
      for (int i = 0; i < 16; ++i) PIN(bf[i]);
      BARRIER();
      STAMP(26);
      v4u ha[8];
#pragma unroll
      for (int i = 0; i < 8; ++i) ha[i] = *(LAS v4u*)(L + hb + i * 64);
      const float cs = *(LAS float*)(L + L_QP + w * 64 + n * 4);
      SCHED();
      f32x4 ac[2] = {{0.f, 0.f, 0.f, 0.f}, {0.f, 0.f, 0.f, 0.f}};
#pragma unroll
      for (int i = 0; i < 8; ++i) { ac[i & 1] = mfma32(ha[i], bf[i], ac[i & 1]); ha[i] = *(LAS v4u*)(L + hb + (8 + i) * 64); SCHED(); }
#pragma unroll
      for (int i = 0; i < 8; ++i) ac[i & 1] = mfma32(ha[i], bf[8 + i], ac[i & 1]);
      float acc0 = ac[0][0] + ac[1][0];
      PIN(acc0);
      STAMP(28);
      BARRIER();
      const f32x2 st = *(LAS f32x2*)(L + L_RM + g * 8);
      const float a = rb(fmaf(-st[0], cs, acc0) * st[1]), u = dpp<ROR8>(a);
      const float sg = rb(__builtin_amdgcn_rcpf(1.0f + __builtin_amdgcn_exp2f(a * -LOG2E)));
      *(LAS u16*)(L + hw) = (u16)pk2(sg * u, 0.f);
      STAMP(29);
      DOWN();
      EXIT();
    }
  }
  if (is_red && w == 0) {
    const int r2 = lane >> 3, k = lane & 7, bb = r2 & 3;
    const float xv = bflo(x[bb * DM + 8 * rho + k]);
    int off1[4], off2[7];
#pragma unroll
    for (int i = 0; i < 4; ++i) off1[i] = WS_HP + (8 * i + r2) * 8192 + (8 * rho + k) * 16;
#pragma unroll
    for (int i = 0; i < 7; ++i) off2[i] = WS_OP + (8 * i + r2) * 8192 + (8 * rho + k) * 16;
    int ho = lane < 32 ? WS_H + k * 8192 + rho * 128 + bb * 16 : OOB, oo = (k == 0 && lane < 32) ? (bb * DM + 8 * rho) * 2 : OOB;
    PIN(ho); PIN(oo);
    wait_ready(rs, lane, tlo, thi);
    STAMP(7);
    float hval;
    {
      v4u pv[4];
      poll_fresh<4>(srd_ws, off1, pv);
      STAMP(8);
      f32x4 a = (__builtin_bit_cast(f32x4, pv[0]) + __builtin_bit_cast(f32x4, pv[1])) + (__builtin_bit_cast(f32x4, pv[2]) + __builtin_bit_cast(f32x4, pv[3]));
#pragma unroll
      for (int i = 0; i < 4; ++i) { a[i] += dpp<ROR8>(a[i]); a[i] = x32_sum(x16_sum(a[i])); }
      PIN(a);
      const float av = sel4f(bb, a);
      hval = rb(xv + rb(av));
      const unsigned e0 = nosent(pk2(dpp<0xA0>(hval), dpp<0xF5>(hval)));
      const unsigned qa = dppu<0x00>(e0), qb2 = dppu<0xAA>(e0);
      const unsigned oa = dppu<0x141>(qa), ob2 = dppu<0x141>(qb2);
      const bool hi = k & 4;
      st_sc1(rs, ho, v4u{hi ? oa : qa, hi ? ob2 : qb2, hi ? qa : oa, hi ? qb2 : ob2});
      SCHED();
      STAMP(9);
      poll_drain<4>(pv);
    }
    {
      v4u pv[7];
      poll_fresh<7>(srd_ws, off2, pv);
      STAMP(10);
      f32x4 a = ((__builtin_bit_cast(f32x4, pv[0]) + __builtin_bit_cast(f32x4, pv[1])) + (__builtin_bit_cast(f32x4, pv[2]) + __builtin_bit_cast(f32x4, pv[3]))) +
                ((__builtin_bit_cast(f32x4, pv[4]) + __builtin_bit_cast(f32x4, pv[5])) + __builtin_bit_cast(f32x4, pv[6]));
#pragma unroll
      for (int i = 0; i < 4; ++i) { a[i] += dpp<ROR8>(a[i]); a[i] = x32_sum(x16_sum(a[i])); }
      PIN(a);
      const float av = sel4f(bb, a);
      const float ov = hval + rb(av);
      const unsigned dw = pk2(ov, dpp<SHL1>(ov));
      const unsigned dw2 = dppu<SHL2>(dw);
      const unsigned dw4 = dppu<SHL4>(dw), dw6 = dppu<SHL4>(dw2);
      __builtin_amdgcn_raw_buffer_store_b128(v4u{dw, dw2, dw4, dw6}, __builtin_amdgcn_make_buffer_rsrc(out, 0, 4 * DM * 2, 0x00020000), oo, 0, 0);
      st8_sc1_asm(v2u{tlo, thi}, (c == 192 && lane == 0) ? WS_EPOCH : OOB, srd_ws);
      SCHED();
      STAMP(11);
      poll_drain<7>(pv);
    }
  }
  STAMP_OUT();
}

}

extern "C" void kernel_launch(void* const* d_in, const int* in_sizes, int n_in, void* d_out, int out_size, void* d_ws, size_t ws_size,
                              hipStream_t stream) {
  (void)in_sizes; (void)n_in; (void)out_size; (void)ws_size;
  fused_block<<<256, NTHR, 0, stream>>>((const u16*)d_in[0], (const u16*)d_in[1], (const u16*)d_in[2], (const u16*)d_in[3], (const u16*)d_in[4],
                                        (const u16*)d_in[6], (const u16*)d_in[7], (const u16*)d_in[8], (u16*)d_out, (unsigned char*)d_ws);
}
```

```cpp
#define POLL_DEPTH 1
#define POLL_SLEEP 1
#include <hip/hip_runtime.h>
#include <hip/hip_bf16.h>
#include <stdint.h>

namespace {

typedef unsigned short u16;
typedef unsigned long long u64;
typedef unsigned v4u __attribute__((ext_vector_type(4)));
typedef unsigned v2u __attribute__((ext_vector_type(2)));
typedef float f32x4 __attribute__((ext_vector_type(4)));
typedef float f32x2 __attribute__((ext_vector_type(2)));
typedef short bf16x8 __attribute__((ext_vector_type(8)));
typedef short s16x4 __attribute__((ext_vector_type(4)));
typedef __bf16 bf2 __attribute__((ext_vector_type(2)));
#define LAS __attribute__((address_space(3)))
#define GAS __attribute__((address_space(1)))

constexpr int DM = 512, HKV = 8, SK = 8192, DH = 128, WIN = 2048, FF = 1792, NQ = 4096;
constexpr int NTHR = 640;

constexpr int WS_EPOCH = 0;
constexpr int WS_RDY = 4096;
constexpr int WS_QG = 8192;
constexpr int WS_PA = WS_QG + 512 * 1024;
constexpr int WS_HP = WS_PA + 512 * 1024;
constexpr int WS_H = WS_HP + 512 * 1024;
constexpr int WS_OP = WS_H + 65536;
constexpr int WS_END = WS_OP + 56 * 8192;
constexpr int OOB = 0x40000000;

constexpr int L_K = 0;
constexpr int L_V = 65536;
constexpr int L_WQ = 131072;
constexpr int L_HN = L_WQ;
constexpr int L_Q = L_WQ + 16384;
constexpr int L_XN = L_Q + 1280;
constexpr int L_O = L_XN;
constexpr int L_QP = L_XN + 1280;
constexpr int L_P = L_QP + 1024;
constexpr int L_RM = L_P + 2304;
constexpr int L_RL = L_RM + 128;
constexpr int L_HH = L_RL + 128;
constexpr int L_ZERO = L_HH + 256;
constexpr int L_FLAG = L_ZERO + 128;
constexpr int L_JUNK = L_FLAG + 16;
constexpr int L_BYTES = L_JUNK + 2048;

constexpr unsigned SENT = 0xFFFFFFFFu;
constexpr int SPIN_CAP = 1 << 16;
constexpr int AUX_SC1 = 16;
constexpr float LOG2E = 1.4426950408889634f;

#define ABL_NOW 0
#define CODE_BYTES 17408
#define WQ_POL "nt"
#define QPP 1
#define QKP 5
#define QG_DELAY 30
#define WINDOW 3
#define P1_EARLY 1
#define GU_WAIT 1
#define PF_NT 0
#define PF_WIN1 0
#define PF_DELAY 0
#define WO_EARLY 1
#define WO_POS 0
#define KV_NT 1
#define WQ_NT 1
#define W2_NT 1
#define GU_NT 1

__device__ __forceinline__ float bflo(unsigned x) { return __builtin_bit_cast(float, x << 16); }
__device__ __forceinline__ float bfhi(unsigned x) { return __builtin_bit_cast(float, x & 0xffff0000u); }
__device__ __forceinline__ unsigned pk2(float lo, float hi) {
  bf2 v = {(__bf16)lo, (__bf16)hi};
  return __builtin_bit_cast(unsigned, v);
}
__device__ __forceinline__ float rb(float x) { return (float)(__bf16)x; }
__device__ __forceinline__ unsigned nosent(unsigned x) { return x < 0xFFFFFFFEu ? x : 0xFFFFFFFEu; }
__device__ __forceinline__ v4u nosent4(f32x4 v) {
  return v4u{nosent(__float_as_uint(v[0])), nosent(__float_as_uint(v[1])), nosent(__float_as_uint(v[2])), nosent(__float_as_uint(v[3]))};
}
__device__ __forceinline__ bool fresh(v4u v) { return max(max(v[0], v[1]), max(v[2], v[3])) != SENT; }
__device__ __forceinline__ float vmax(float a, float b) { float r; asm("v_max_f32 %0, %1, %2" : "=v"(r) : "v"(a), "v"(b)); return r; }

__device__ __forceinline__ float vmax3(float a, float b, float c) { float r; asm("v_max3_f32 %0, %1, %2, %3" : "=v"(r) : "v"(a), "v"(b), "v"(c)); return r; }
__device__ __forceinline__ void row_max2(float& a, float& b) {
  asm("s_nop 1\n\t"
      "v_max_f32_dpp %0, %0, %0 row_ror:8 row_mask:0xf bank_mask:0xf\n\tv_max_f32_dpp %1, %1, %1 row_ror:8 row_mask:0xf bank_mask:0xf\n\ts_nop 0\n\t"
      "v_max_f32_dpp %0, %0, %0 row_ror:4 row_mask:0xf bank_mask:0xf\n\tv_max_f32_dpp %1, %1, %1 row_ror:4 row_mask:0xf bank_mask:0xf\n\ts_nop 0\n\t"
      "v_max_f32_dpp %0, %0, %0 row_ror:2 row_mask:0xf bank_mask:0xf\n\tv_max_f32_dpp %1, %1, %1 row_ror:2 row_mask:0xf bank_mask:0xf\n\ts_nop 0\n\t"
      "v_max_f32_dpp %0, %0, %0 row_ror:1 row_mask:0xf bank_mask:0xf\n\tv_max_f32_dpp %1, %1, %1 row_ror:1 row_mask:0xf bank_mask:0xf\n\ts_nop 1"
      : "+v"(a), "+v"(b));
}

__device__ __forceinline__ float dot2(unsigned a, unsigned b, float c) {
  return __builtin_amdgcn_fdot2_f32_bf16(__builtin_bit_cast(bf2, a), __builtin_bit_cast(bf2, b), c, false);
}
__device__ __forceinline__ f32x4 mfma32(v4u a, v4u b, f32x4 c) {
  return __builtin_amdgcn_mfma_f32_16x16x32_bf16(__builtin_bit_cast(bf16x8, a), __builtin_bit_cast(bf16x8, b), c, 0, 0, 0);
}
template <int CTRL> __device__ __forceinline__ float dpp(float x) {
  return __builtin_bit_cast(float, __builtin_amdgcn_mov_dpp(__builtin_bit_cast(int, x), CTRL, 0xf, 0xf, true));
}
template <int CTRL> __device__ __forceinline__ unsigned dppu(unsigned x) { return (unsigned)__builtin_amdgcn_mov_dpp((int)x, CTRL, 0xf, 0xf, true); }
constexpr int ROR8 = 0x128, ROR4 = 0x124, ROR2 = 0x122, ROR1 = 0x121, SHL1 = 0x101, SHL2 = 0x102, SHL4 = 0x104;
__device__ __forceinline__ float row_max(float x) { x = fmaxf(x, dpp<ROR8>(x)); x = fmaxf(x, dpp<ROR4>(x)); x = fmaxf(x, dpp<ROR2>(x)); return fmaxf(x, dpp<ROR1>(x)); }
__device__ __forceinline__ float row_sum(float x) { x += dpp<ROR8>(x); x += dpp<ROR4>(x); x += dpp<ROR2>(x); return x + dpp<ROR1>(x); }
__device__ __forceinline__ float x16_max(float x) { auto s = __builtin_amdgcn_permlane16_swap(__float_as_uint(x), __float_as_uint(x), false, false); return vmax(__uint_as_float(s[0]), __uint_as_float(s[1])); }
__device__ __forceinline__ float x32_max(float x) { auto s = __builtin_amdgcn_permlane32_swap(__float_as_uint(x), __float_as_uint(x), false, false); return vmax(__uint_as_float(s[0]), __uint_as_float(s[1])); }
__device__ __forceinline__ float x16_sum(float x) { auto s = __builtin_amdgcn_permlane16_swap(__float_as_uint(x), __float_as_uint(x), false, false); return __uint_as_float(s[0]) + __uint_as_float(s[1]); }
__device__ __forceinline__ float x32_sum(float x) { auto s = __builtin_amdgcn_permlane32_swap(__float_as_uint(x), __float_as_uint(x), false, false); return __uint_as_float(s[0]) + __uint_as_float(s[1]); }
__device__ __forceinline__ float wave_sum(float x) { return x32_sum(x16_sum(row_sum(x))); }
__device__ __forceinline__ f32x4 sel2(bool c, f32x4 a, f32x4 b) {
  return f32x4{c ? b[0] : a[0], c ? b[1] : a[1], c ? b[2] : a[2], c ? b[3] : a[3]};
}
__device__ __forceinline__ f32x4 sel4(int g, f32x4 a, f32x4 b, f32x4 c, f32x4 d) { return sel2(g & 2, sel2(g & 1, a, b), sel2(g & 1, c, d)); }

#define PIN(x) asm volatile("" : "+v"(x))
#define SCHED() __builtin_amdgcn_sched_barrier(0)
#define LDS_FENCE() asm volatile("s_waitcnt lgkmcnt(0)" ::: "memory")
#define BARRIER() do { asm volatile("s_waitcnt lgkmcnt(0)\n\ts_barrier" ::: "memory"); } while (0)
#define VMWAIT(N) asm volatile("s_waitcnt vmcnt(" #N ")" ::: "memory")
#define STAMP(i) do {} while (0)
#define STAMP_OUT() do {} while (0)

#define EXIT() do { STAMP_OUT(); asm volatile("s_endpgm" ::: "memory"); __builtin_unreachable(); } while (0)

__device__ __forceinline__ v4u make_srd(const void* p, unsigned bytes) {
  const u64 a = (u64)p;
  return v4u{(unsigned)a, (unsigned)(a >> 32) & 0xffffu, bytes, 0x00020000u};
}
template <int NT> __device__ __forceinline__ void dma_buf(unsigned lds, int voff, v4u srd, int soff) {
  if (NT) asm volatile("s_mov_b32 m0, %0\n\ts_nop 0\n\tbuffer_load_dwordx4 %1, %2, %3 offen nt lds" :: "s"(lds), "v"(voff), "s"(srd), "s"(soff) : "m0", "memory");
  else asm volatile("s_mov_b32 m0, %0\n\ts_nop 0\n\tbuffer_load_dwordx4 %1, %2, %3 offen lds" :: "s"(lds), "v"(voff), "s"(srd), "s"(soff) : "m0", "memory");
}
template <int NT> __device__ __forceinline__ void dma_glb(unsigned lds, const void* p) {
  if (NT) asm volatile("s_mov_b32 m0, %0\n\ts_nop 0\n\tglobal_load_lds_dwordx4 %1, off nt" :: "s"(lds), "v"(p) : "m0", "memory");
  else asm volatile("s_mov_b32 m0, %0\n\ts_nop 0\n\tglobal_load_lds_dwordx4 %1, off" :: "s"(lds), "v"(p) : "m0", "memory");
}
__device__ __forceinline__ void ld16_asm(v4u& d, int voff, v4u srd, int soff) {
  asm volatile("buffer_load_dwordx4 %0, %1, %2, %3 offen" : "=v"(d) : "v"(voff), "s"(srd), "s"(soff) : "memory");
}
__device__ __forceinline__ void ld16_sc1_asm(v4u& d, int voff, v4u srd) {
  asm volatile("buffer_load_dwordx4 %0, %1, %2, 0 offen sc1" : "=v"(d) : "v"(voff), "s"(srd) : "memory");
}
__device__ __forceinline__ void ld8_sc1_asm(v2u& d, v4u srd) {
  asm volatile("buffer_load_dwordx2 %0, off, %1, 0 sc1" : "=v"(d) : "s"(srd) : "memory");
}
__device__ __forceinline__ void st16_sc1_asm(v4u v, int voff, v4u srd) {
  asm volatile("buffer_store_dwordx4 %0, %1, %2, 0 offen sc1\n\ts_nop 1" :: "v"(v), "v"(voff), "s"(srd) : "memory");
}

__device__ __forceinline__ void st8_sc1_asm(v2u v, int voff, v4u srd) {
  asm volatile("buffer_store_dwordx2 %0, %1, %2, 0 offen sc1" :: "v"(v), "v"(voff), "s"(srd) : "memory");
}
__device__ __forceinline__ float sel4f(int b, f32x4 a) { const float lo = (b & 1) ? a[1] : a[0], hi = (b & 1) ? a[3] : a[2]; float r = (b & 2) ? hi : lo; return r; }

__device__ __forceinline__ v4u ld_sc1(__amdgpu_buffer_rsrc_t rs, int off) { return __builtin_amdgcn_raw_buffer_load_b128(rs, off, 0, AUX_SC1); }
__device__ __forceinline__ void st_sc1(__amdgpu_buffer_rsrc_t rs, int off, v4u v) { __builtin_amdgcn_raw_buffer_store_b128(v, rs, off, 0, AUX_SC1); }
__device__ __forceinline__ v2u tr_rd(LAS unsigned char* L, int off) {
  return __builtin_bit_cast(v2u, __builtin_amdgcn_ds_read_tr16_b64_v4i16((LAS s16x4*)(L + off)));
}
__device__ __forceinline__ v4u tr_frag(LAS unsigned char* L, int off0, int off1) {
  const v2u a = tr_rd(L, off0), b = tr_rd(L, off1);
  return v4u{a[0], a[1], b[0], b[1]};
}

__device__ __forceinline__ void ld16_sc1_rw(v4u& d, int voff, v4u srd) {
  asm volatile("buffer_load_dwordx4 %0, %1, %2, 0 offen sc1" : "+v"(d) : "v"(voff), "s"(srd) : "memory");
}
template <int CNT> __device__ __forceinline__ void vmwait_pin(v4u& a) { asm volatile("s_waitcnt vmcnt(%1)" : "+v"(a) : "n"(CNT) : "memory"); }
template <int N> __device__ __forceinline__ void poll_start(v4u srd, const int (&off)[N], v4u (&v)[N]) {
#pragma unroll
  for (int i = 0; i < N; ++i) v[i] = v4u{SENT, SENT, SENT, SENT};
#pragma unroll
  for (int k = 0; k < POLL_DEPTH - 1; ++k) {
#pragma unroll
    for (int i = 0; i < N; ++i) ld16_sc1_rw(v[i], off[i], srd);
    __builtin_amdgcn_s_sleep(POLL_SLEEP);
  }
}
__device__ __forceinline__ void ld16_sc1_masked(v4u& d, int voff, v4u srd, u64 mask) {
  asm volatile("s_mov_b64 exec, %3\n\tbuffer_load_dwordx4 %0, %1, %2, 0 offen sc1\n\ts_mov_b64 exec, -1" : "+v"(d) : "v"(voff), "s"(srd), "s"(mask) : "memory");
}
template <int N> __device__ __forceinline__ void poll_finish(v4u srd, const int (&off)[N], v4u (&v)[N]) {
  u64 miss[N];
#pragma unroll
  for (int i = 0; i < N; ++i) miss[i] = ~0ull;
#pragma unroll 1
  for (int it = 0; it < SPIN_CAP; ++it) {
#pragma unroll
    for (int i = 0; i < N; ++i) ld16_sc1_masked(v[i], off[i], srd, miss[i]);
    vmwait_pin<(POLL_DEPTH - 1) * N>(v[0]);
#pragma unroll
    for (int i = 1; i < N; ++i) PIN(v[i]);
    u64 any = 0;
#pragma unroll
    for (int i = 0; i < N; ++i) { miss[i] = __builtin_amdgcn_ballot_w64(!fresh(v[i])); any |= miss[i]; }
    if (any == 0) break;
    __builtin_amdgcn_s_sleep(POLL_SLEEP);
  }
}
template <int N> __device__ __forceinline__ void poll_fresh(v4u srd, const int (&off)[N], v4u (&v)[N]) {
  poll_start<N>(srd, off, v);
  poll_finish<N>(srd, off, v);
}
template <int N> __device__ __forceinline__ void poll_drain(v4u (&v)[N]) {
  vmwait_pin<0>(v[0]);
#pragma unroll
  for (int i = 1; i < N; ++i) PIN(v[i]);
}
__device__ __forceinline__ void wait_ready(__amdgpu_buffer_rsrc_t rs, int lane, unsigned tlo, unsigned thi) {
#pragma unroll 1
  for (int it = 0; it < SPIN_CAP; ++it) {
    asm volatile("" ::: "memory");
    const v4u r0 = ld_sc1(rs, WS_RDY + lane * 16), r1 = ld_sc1(rs, WS_RDY + 1024 + lane * 16);
    const bool ok = (r0[0] == tlo) & (r0[1] == thi) & (r0[2] == tlo) & (r0[3] == thi) & (r1[0] == tlo) & (r1[1] == thi) & (r1[2] == tlo) & (r1[3] == thi);
    if (__all(ok)) break;
    __builtin_amdgcn_s_sleep(4);
  }
}

__global__ void __launch_bounds__(NTHR, 3)
fused_block(const u16* __restrict__ x, const u16* __restrict__ kc, const u16* __restrict__ vc, const u16* __restrict__ wq,
            const u16* __restrict__ wo, const u16* __restrict__ wg, const u16* __restrict__ wu, const u16* __restrict__ wd,
            u16* __restrict__ out, unsigned char* __restrict__ ws) {
  __shared__ __attribute__((aligned(16))) unsigned char smem_[L_BYTES];
  {
    u64 pc0;
    asm volatile("s_getpc_b64 %0" : "=s"(pc0));
    if (__builtin_amdgcn_readfirstlane(threadIdx.x >> 6) == 1) {
      const v4u srd_c = make_srd((const void*)(pc0 & ~255ull), CODE_BYTES);
      const int so = ((blockIdx.x >> 3) & 15) * 2048;
      asm volatile("s_mov_b32 m0, %0\n\ts_nop 0\n\tbuffer_load_dwordx4 %1, %2, %3 offen lds\n\tbuffer_load_dwordx4 %1, %2, %3 offen offset:1024 lds"
                   :: "s"((unsigned)(size_t)(LAS unsigned char*)smem_ + L_JUNK), "v"((threadIdx.x & 63) * 16), "s"(srd_c), "s"(so) : "m0", "memory");
    }
    __builtin_amdgcn_sched_barrier(0);
  }
  LAS unsigned char* L = (LAS unsigned char*)smem_;
  const unsigned lbase = (unsigned)(size_t)L;
  const int c = blockIdx.x, kvh = c & 7, i32 = c >> 3;
  const int lane = threadIdx.x & 63, wv = __builtin_amdgcn_readfirstlane(threadIdx.x >> 6);
  const int w = wv < 2 ? 8 + wv : wv - 2;
  const int n = lane & 15, g = lane >> 4;
  const v4u srd_ws = make_srd(ws, WS_END);
  STAMP(0);

  const int b = i32 >> 3, s = i32 & 7;
  const size_t kvbase = ((size_t)(b * HKV + kvh) * SK + (SK - WIN) + s * 256) * DH;
  const v4u srd_k = make_srd(kc + kvbase, 65536);
  const int rot = w + 37 * c;
  const int kv0 = g * 256 + ((n ^ g) * 16);
  const unsigned ldst = lbase + w * 1024;
  v2u ep;
  v4u xr[4];
#define KLOAD(i) dma_buf<KV_NT>(ldst + L_K + (i) * 8192, kv0 ^ (((i) & 3) << 6), srd_k, ((8 * (i) + rot) & 63) << 10)
  if (wv == 0) {
    const v4u srd_x = make_srd(x, 4 * DM * 2);
    const v4u srd_wq = make_srd(wq + (size_t)b * 128 * NQ + kvh * 512 + s * 64, 128 * NQ * 2);
    ld8_sc1_asm(ep, srd_ws);
#pragma unroll
    for (int i = 0; i < 4; ++i) ld16_asm(xr[i], g * 1024 + n * 16, srd_x, ((b + i) & 3) * 256);
    const int row = lane >> 3, v0 = row * (NQ * 2) + (((lane & 7) ^ (2 * ((row >> 1) & 1))) * 16);
    int so;
#define WQ2 "buffer_load_dwordx4 %1, %3, %0 offen " WQ_POL " lds\n\ts_addk_i32 m0, 0x400\n\ts_add_u32 %0, %0, %5\n\t" \
            "buffer_load_dwordx4 %2, %3, %0 offen " WQ_POL " lds\n\ts_addk_i32 m0, 0x400\n\ts_add_u32 %0, %0, %5\n\t"
    asm volatile("s_mov_b32 m0, %4\n\ts_mov_b32 %0, 0\n\t" WQ2 WQ2 WQ2 WQ2 WQ2 WQ2 WQ2 WQ2
                 : "=&s"(so) : "v"(v0), "v"(v0 ^ 64), "s"(srd_wq), "s"(lbase + L_WQ), "s"(8 * NQ * 2) : "m0", "scc", "memory");
    *(LAS unsigned*)(L + L_FLAG) = 0u;
    LDS_FENCE();
  }
  asm volatile("s_barrier" ::: "memory");
  if (wv >= 2) { KLOAD(0); KLOAD(1); KLOAD(2); }
  int wv2 = wv;
  asm volatile("" : "+s"(wv2));

  if (wv2 == 1) {
    v2u ep1;
    ld8_sc1_asm(ep1, srd_ws);
    const v4u sent = {SENT, SENT, SENT, SENT};
    st16_sc1_asm(sent, WS_PA + c * 2048 + lane * 16, srd_ws);
    st16_sc1_asm(sent, WS_PA + c * 2048 + 1024 + lane * 16, srd_ws);
    const int base = i32 < 16 ? WS_HP + (kvh * 8 + (i32 >> 1)) * 8192 + (i32 & 1) * 4096 : WS_OP + (kvh * 7 + i32 - 16) * 8192;
    const int len = i32 < 16 ? 4096 : i32 < 23 ? 8192 : 0;
#pragma unroll 1
    for (int o = 0; o < len; o += 1024) st16_sc1_asm(sent, base + o + lane * 16, srd_ws);
    st16_sc1_asm(sent, (i32 >= 24 && lane < 32) ? WS_H + (lane >> 2) * 8192 + ((i32 - 24) * 8 + kvh) * 128 + (lane & 3) * 16 : OOB, srd_ws);
    *(LAS v4u*)(L + L_ZERO + (lane & 7) * 16) = v4u{0u, 0u, 0u, 0u};
    asm volatile("s_waitcnt vmcnt(0)" : "+v"(ep1) :: "memory");
    const u64 tag = (((u64)ep1[1] << 32) | ep1[0]) + 0x9E3779B97F4A7C15ull;
    st8_sc1_asm(v2u{(unsigned)tag, (unsigned)(tag >> 32)}, lane == 0 ? WS_RDY + (kvh * 32 + i32) * 8 : OOB, srd_ws);
    STAMP(1);
    EXIT();
  }
  if (wv2 == 0) {
    const int rq = b, cq = s;
    STAMP(7);
    asm volatile("s_waitcnt vmcnt(16)" : "+v"(ep), "+v"(xr[0]), "+v"(xr[1]), "+v"(xr[2]), "+v"(xr[3]) :: "memory");
    STAMP(9);
    {
      float s0 = 0.f, s1 = 0.f, q0 = 0.f, q1 = 0.f;
#pragma unroll
      for (int i = 0; i < 4; ++i)
#pragma unroll
        for (int k = 0; k < 4; k += 2) {
          s0 = dot2(xr[i][k], 0x3F803F80u, s0); s1 = dot2(xr[i][k + 1], 0x3F803F80u, s1);
          q0 = dot2(xr[i][k], xr[i][k], q0); q1 = dot2(xr[i][k + 1], xr[i][k + 1], q1);
        }
      const float mean = row_sum(s0 + s1) * (1.0f / DM);
      const float q = fmaxf(fmaf(-mean * DM, mean, row_sum(q0 + q1)), 0.f);
      float v[8];
#pragma unroll
      for (int k = 0; k < 4; ++k) { v[2 * k] = bflo(xr[0][k]); v[2 * k + 1] = bfhi(xr[0][k]); }
      const float mb = rb(mean);
      const float inv = __builtin_amdgcn_rcpf(rb(__builtin_amdgcn_sqrtf(rb(rb(q * (1.0f / DM)) + 1e-5f))));
      v4u o;
#pragma unroll
      for (int k = 0; k < 4; ++k) o[k] = pk2(rb(v[2 * k] - mb) * inv, rb(v[2 * k + 1] - mb) * inv);
      *(LAS v4u*)(L + L_XN + g * 320 + n * 16) = o;
    }
    STAMP(1);
    LDS_FENCE();
    f32x4 acc[4];
    {
      v4u xa[4];
#pragma unroll
      for (int ks = 0; ks < 4; ++ks) xa[ks] = *(LAS v4u*)(L + L_XN + (n & 3) * 320 + ks * 64 + g * 16);
      const int q4 = n >> 2, p = n & 3, y = ((q4 >> 1) & 1) | ((g & 1) << 1);
      const int rbase = L_WQ + (8 * g + q4) * 128 + 8 * (p & 1);
      int o[4];
#pragma unroll
      for (int t = 0; t < 4; ++t) { o[t] = rbase + (((2 * t + (p >> 1)) ^ (2 * y)) * 16); acc[t] = f32x4{0.f, 0.f, 0.f, 0.f}; }
#pragma unroll
      for (int ks = 0; ks < 4; ++ks) {
        if (ks == 0) VMWAIT(12); else if (ks == 1) VMWAIT(8); else if (ks == 2) VMWAIT(4); else VMWAIT(0);
        if (ks == 3) STAMP(4);
#pragma unroll
        for (int t = 0; t < 4; ++t) acc[t] = mfma32(xa[ks], tr_frag(L, o[t] + ks * 4096, o[t] + ks * 4096 + 512), acc[t]);
      }
    }
    *(LAS f32x4*)(L + L_QP + lane * 16) = sel4(g, acc[0], acc[1], acc[2], acc[3]);
    LDS_FENCE();
    const f32x4 q0 = *(LAS f32x4*)(L + L_QP + (lane & 31) * 32), q1 = *(LAS f32x4*)(L + L_QP + (lane & 31) * 32 + 16);
    const u64 tag = (((u64)ep[1] << 32) | ep[0]) + 0x9E3779B97F4A7C15ull;
    const unsigned tlo = (unsigned)tag, thi = (unsigned)(tag >> 32);
    STAMP(5);
    STAMP(2);
    {
      const bool up = lane >= 32;
      const int o = WS_QG + ((kvh * 4 + (up ? 1 : 0)) * 4 + rq) * 4096 + (32 * cq + (lane & 31)) * 16;
      st16_sc1_asm(v4u{__float_as_uint(up ? q0[1] : q0[0]), __float_as_uint(up ? q1[1] : q1[0]), tlo, thi}, o, srd_ws);
      st16_sc1_asm(v4u{__float_as_uint(up ? q0[3] : q0[2]), __float_as_uint(up ? q1[3] : q1[2]), tlo, thi}, o + 2 * 4 * 4096, srd_ws);
    }
    STAMP(3);
    __builtin_amdgcn_s_sleep(QG_DELAY);
    const __amdgpu_buffer_rsrc_t rs0 = __builtin_amdgcn_make_buffer_rsrc(ws, 0, WS_END, 0x00020000);
    const int qo = WS_QG + (kvh * 4 + b) * 16384 + lane * 16;
    v4u qg[16];
#pragma unroll 1
    for (int it = 0; it < SPIN_CAP; ++it) {
      asm volatile("" ::: "memory");
      bool ok = true;
#pragma unroll
      for (int i = 0; i < 16; ++i) qg[i] = ld_sc1(rs0, qo + i * 1024);
#pragma unroll
      for (int i = 0; i < 16; ++i) ok &= (qg[i][2] == tlo) & (qg[i][3] == thi);
      if (__all(ok)) break;
    }
#pragma unroll
    for (int u = 0; u < 4; ++u) {
      const float a = (__uint_as_float(qg[u][0]) + __uint_as_float(qg[4 + u][0])) + (__uint_as_float(qg[8 + u][0]) + __uint_as_float(qg[12 + u][0]));
      const float bq = (__uint_as_float(qg[u][1]) + __uint_as_float(qg[4 + u][1])) + (__uint_as_float(qg[8 + u][1]) + __uint_as_float(qg[12 + u][1]));
      *(LAS unsigned*)(L + L_Q + u * 320 + lane * 4) = pk2(a, bq);
    }
    *(LAS v2u*)(L + L_FLAG + 8) = v2u{tlo, thi};
    LDS_FENCE();
    *(volatile LAS unsigned*)(L + L_FLAG) = 1u;
    STAMP(6);
    EXIT();
  }

  const bool is_wo = i32 < 16, is_ffn = i32 >= 16 && i32 < 23, is_red = i32 >= 24;
  const int jf = kvh * 7 + (i32 - 16);
  const int rho = (i32 - 24) * 8 + kvh;
  const __amdgpu_buffer_rsrc_t rs = __builtin_amdgcn_make_buffer_rsrc(ws, 0, WS_END, 0x00020000);
  const v4u srd_v = make_srd(vc + kvbase, 65536);
  const int vv0 = g * 256 + ((n ^ ((g << 2) | (w & 3))) * 16);
  asm volatile("" :: "s"(wo), "s"(wg), "s"(wu), "s"(wd), "s"(out), "s"(x));
#define VLOAD(i) dma_buf<KV_NT>(ldst + L_V + (i) * 8192, vv0, srd_v, ((8 * (i) + rot) & 63) << 10)
#define WW() VMWAIT(2)
  WW();
  STAMP(12);
  KLOAD(3);
  WW(); KLOAD(4); WW(); KLOAD(5); WW(); KLOAD(6); WW(); KLOAD(7);
  STAMP(1);
  WW(); VLOAD(0); WW(); VLOAD(1); WW(); VLOAD(2);
  constexpr int VNEXT = 3;
  v4u kf[8];
  {
    const int kb = L_K + (n >> 2) * 8192 + w * 1024 + (n & 3) * 256 + ((g ^ n) * 16);
#pragma unroll
    for (int ks = 0; ks < 4; ++ks) { kf[ks] = *(LAS v4u*)(L + (kb ^ (ks << 6))); kf[4 + ks] = *(LAS v4u*)(L + (kb ^ (ks << 6)) + 32768); }
  }
  int qb = L_Q + (2 * (n >> 3) + (n & 1)) * 320 + g * 16;
  int rmw = L_RM + (g >> 1) * 64 + w * 8, rmr = L_RM + (g >> 1) * 64;
  int po = L_P + (g >> 1) * 1152 + ((4 * (g & 1) + (n >> 2)) * 32 + 4 * w + (n & 3)) * 2;
  const u16* wp0 = is_wo ? wo + (size_t)(kvh * 512 + (2 * (i32 >> 3) + (w >> 2)) * 128 + 32 * ((i32 >> 1) & 3) + 4 * (w & 3)) * DM + (i32 & 1) * 256 + g * (DM - 128) + (vv0 >> 1)
                         : (lane >= 32 ? wu : wg) + (size_t)(64 * w + (i32 & 7) + (i32 & 8) * 4) * FF + ((224 * kvh) & ~63) + 8 * (lane & 31);
  PIN(wp0);
  const int tq = n >> 2, tp = n & 3;
  int tr0 = (8 * g + tq) * 256 + (((2 * w + (tp >> 1)) ^ ((tq << 2) | ((2 * g) & 3))) * 16) + 8 * (tp & 1);
  int tr1 = (8 * g + 4 + tq) * 256 + (((2 * w + (tp >> 1)) ^ ((tq << 2) | ((2 * g + 1) & 3))) * 16) + 8 * (tp & 1);
  PIN(tr0); PIN(tr1);
  int tv0 = L_V + tr0, tv1 = L_V + tr1, pab = L_P + (n >> 2) * 576 + g * 16;
  PIN(tv0); PIN(tv1); PIN(pab);
  PIN(qb); PIN(rmw); PIN(rmr); PIN(po);
#pragma unroll
  for (int i = 0; i < 8; ++i) PIN(kf[i]);
#pragma unroll
  for (int i = VNEXT; i <= QKP; ++i) { WW(); VLOAD(i); }
  STAMP(13);
#pragma unroll 1
  for (int it = 0; it < (SPIN_CAP << 4); ++it) {
    if (__builtin_amdgcn_readfirstlane(*(volatile LAS unsigned*)(L + L_FLAG)) != 0u) break;
  }
  asm volatile("" ::: "memory");
  const v2u tagv = *(LAS v2u*)(L + L_FLAG + 8);
  const unsigned tlo = tagv[0], thi = tagv[1];
  STAMP(3);
  float sc[2];
  {
    v4u qa[4];
#pragma unroll
    for (int ks = 0; ks < 4; ++ks) qa[ks] = *(LAS v4u*)(L + qb + ks * 64);
    f32x4 d0 = {0.f, 0.f, 0.f, 0.f}, d1 = {0.f, 0.f, 0.f, 0.f};
#pragma unroll
    for (int ks = 0; ks < 4; ++ks) { d0 = mfma32(qa[ks], kf[ks], d0); d1 = mfma32(qa[ks], kf[4 + ks], d1); }
#pragma unroll
    for (int j = 0; j < 2; ++j) sc[j] = rb(rb((g & 1) ? d1[j] : d0[j]) * 0.08838834764831845f);
    float m0 = sc[0], m1 = sc[1];
    row_max2(m0, m1);
    const f32x2 mx = {x16_max(m0), x16_max(m1)};
    *(LAS f32x2*)(L + rmw) = mx;
  }
#pragma unroll
  for (int i = QKP + 1; i < 8; ++i) VLOAD(i);
  STAMP(2);
  BARRIER();
  float mh;
  {
    f32x4 t[4];
#pragma unroll
    for (int i = 0; i < 4; ++i) t[i] = *(LAS f32x4*)(L + rmr + i * 16);
    const float M0 = vmax3(vmax3(t[0][0], t[0][2], t[1][0]), vmax3(t[1][2], t[2][0], t[2][2]), vmax(t[3][0], t[3][2]));
    const float M1 = vmax3(vmax3(t[0][1], t[0][3], t[1][1]), vmax3(t[1][3], t[2][1], t[2][3]), vmax(t[3][1], t[3][3]));
    const float p0 = __builtin_amdgcn_exp2f((sc[0] - M0) * LOG2E), p1 = __builtin_amdgcn_exp2f((sc[1] - M1) * LOG2E);
    const unsigned pp = pk2(p0, p1);
    *(LAS u16*)(L + po) = (u16)pp;
    *(LAS u16*)(L + po + 576) = (u16)(pp >> 16);
    const f32x2 ls = {x16_sum(row_sum(p0)), x16_sum(row_sum(p1))};
    *(LAS f32x2*)(L + rmw + (L_RL - L_RM)) = ls;
    mh = (g & 1) ? M1 : M0;
  }
  const bool chk = is_wo && w == 0;
  const int roff = WS_RDY + kvh * 256 + n * 16;
  v4u rdy;
  ld16_sc1_asm(rdy, chk ? roff : OOB, srd_ws);
#define WOLOAD() do { _Pragma("unroll") for (int i = 0; i < 4; ++i) dma_glb<W2_NT>(ldst + L_K + i * 8192, is_wo ? wp0 + (i >> 1) * 16 * DM + (i & 1) * 128 : x); } while (0)
  WOLOAD();
  VMWAIT(7);
  STAMP(14);
  BARRIER();
    v4u pa[8];
#pragma unroll
    for (int i = 0; i < 8; ++i) pa[i] = *(LAS v4u*)(L + pab + i * 64);
    f32x4 t[4];
#pragma unroll
    for (int i = 0; i < 4; ++i) t[i] = *(LAS f32x4*)(L + rmr + (L_RL - L_RM) + i * 16);
    f32x4 acc = {0.f, 0.f, 0.f, 0.f};
#pragma unroll
    for (int i = 0; i < 6; ++i) acc = mfma32(pa[i], tr_frag(L, tv0 + i * 8192, tv1 + i * 8192), acc);
    const f32x4 ts = (t[0] + t[1]) + (t[2] + t[3]);
    unsigned ml[2] = {nosent(__float_as_uint(mh)), nosent(__float_as_uint((g & 1) ? ts[1] + ts[3] : ts[0] + ts[2]))};
    int off[1] = {WS_PA + (kvh + 8 * (8 * (w >> 1) + (lane >> 3))) * 2048 + (2 * ((i32 >> 1) & 3) + (w & 1)) * 256 + (i32 >> 3) * 128 + (lane & 7) * 16};
    int ob[2], wb0[2], wb1[2];
#pragma unroll
    for (int i = 0; i < 2; ++i) {
      ob[i] = (n >> 2) == i ? L_O + (n & 3) * 320 + g * 16 : L_ZERO;
      const int x = ((((4 * w + 2 * i) & 15) ^ (2 * w)) << 4);
      wb0[i] = L_K + (w >> 2) * 8192 + (tr0 ^ x); wb1[i] = L_K + (w >> 2) * 8192 + (tr1 ^ x);
      PIN(ob[i]); PIN(wb0[i]); PIN(wb1[i]);
    }
    int ow = L_O + (w >> 1) * 320 + (32 * (w & 1) + 4 * (n & 7) + (((g & 1) << 1) | (g >> 1))) * 2;
    int so = lane < 32 ? WS_HP + (kvh * 8 + (i32 >> 1)) * 8192 + ((i32 & 1) * 256 + 32 * w + lane) * 16 : OOB;
    PIN(off[0]); PIN(ow); PIN(so);
    int pao = (n & 3) == 0 ? WS_PA + c * 2048 + w * 256 + g * 64 + (n >> 2) * 16 : OOB;
    PIN(ml[0]); PIN(ml[1]); PIN(pao); PIN(acc); PIN(pa[6]); PIN(pa[7]);
    STAMP(15);
    asm volatile("s_waitcnt vmcnt(4)" : "+v"(rdy) :: "memory");
    if (chk) {
#pragma unroll 1
      for (int it = 0; it < SPIN_CAP; ++it) {
        if (__all((rdy[0] == tlo) & (rdy[1] == thi) & (rdy[2] == tlo) & (rdy[3] == thi))) break;
        ld16_sc1_rw(rdy, roff, srd_ws);
        vmwait_pin<0>(rdy);
      }
    }
    STAMP(5);
    BARRIER();
#pragma unroll
    for (int i = 6; i < 8; ++i) acc = mfma32(pa[i], tr_frag(L, tv0 + i * 8192, tv1 + i * 8192), acc);
    const unsigned d0 = nosent(pk2(acc[0], dpp<SHL1>(acc[0])));
    st_sc1(rs, pao, v4u{d0, dppu<SHL2>(d0), ml[0], ml[1]});
  STAMP(6);

  if (__builtin_expect(is_wo, 1)) {
    v4u pv[1];
    STAMP(7);
    poll_fresh<1>(srd_ws, off, pv);
    STAMP(8);
    const float m0 = __uint_as_float(pv[0][2]);
    float mm = vmax(m0, dpp<ROR8>(m0));
    mm = x32_max(x16_max(mm));
    const float e0 = __builtin_amdgcn_exp2f((m0 - mm) * LOG2E);
    float lw = e0 * __uint_as_float(pv[0][3]);
    const auto s01 = __builtin_amdgcn_permlane32_swap(__float_as_uint(e0 * bflo(pv[0][0])), __float_as_uint(e0 * bfhi(pv[0][0])), false, false);
    const auto s23 = __builtin_amdgcn_permlane32_swap(__float_as_uint(e0 * bflo(pv[0][1])), __float_as_uint(e0 * bfhi(pv[0][1])), false, false);
    const float u0 = __uint_as_float(s01[0]) + __uint_as_float(s01[1]), u1 = __uint_as_float(s23[0]) + __uint_as_float(s23[1]);
    const auto su = __builtin_amdgcn_permlane16_swap(__float_as_uint(u0), __float_as_uint(u1), false, false);
    float ov = __uint_as_float(su[0]) + __uint_as_float(su[1]);
    ov += dpp<ROR8>(ov);
    lw += dpp<ROR8>(lw);
    lw = x32_sum(x16_sum(lw));
    *(LAS u16*)(L + ow) = (u16)pk2(ov * __builtin_amdgcn_rcpf(lw), 0.f);
    BARRIER();
    v4u oa[4], bf[4];
#pragma unroll
    for (int i = 0; i < 4; ++i) {
      oa[i] = *(LAS v4u*)(L + ob[i & 1] + (i >> 1) * 64);
      bf[i] = tr_frag(L, wb0[i & 1] + (i >> 1) * 16384, wb1[i & 1] + (i >> 1) * 16384);
    }
    SCHED();
    f32x4 hacc = {0.f, 0.f, 0.f, 0.f};
#pragma unroll
    for (int i = 0; i < 4; ++i) hacc = mfma32(oa[i], bf[i], hacc);
    st_sc1(rs, so, nosent4(hacc));
    SCHED();
    STAMP(9);
    poll_drain<1>(pv);
    EXIT();
  }
  __builtin_amdgcn_s_sleep(PF_DELAY);
#pragma unroll
  for (int i = 0; i < 4; ++i) { dma_glb<PF_NT>(ldst + L_K + 32768, wp0 + i * 8 * FF); if (PF_WIN1 && i == 1) VMWAIT(0); }
  if (is_ffn) {
    const u16* gup = ((lane >> 2) & 1 ? wu : wg) + (size_t)(64 * w + (lane >> 3)) * FF + 32 * jf + 8 * ((lane & 3) ^ (g & 1));
    const int alt = (lane & 2) ? -16 : 16;
    {
      const v4u srd_d = make_srd(wd + (size_t)(32 * jf + 4 * w) * DM, 4 * DM * 2);
#pragma unroll
      for (int i = 0; i < 4; ++i) dma_buf<W2_NT>(ldst + L_K + i * 8192, vv0 + g * (DM * 2 - 256), srd_d, i * 256);
    }
    STAMP(20);
    VMWAIT(4);
    STAMP(21);
    BARRIER();
    STAMP(22);
#pragma unroll
    for (int i = 0; i < 8; ++i) dma_glb<GU_NT>(lbase + L_V + w * 8192 + i * 1024, gup + i * 8 * FF + ((i & 1) ? alt : 0));
    STAMP(10);
    v4u bd[4];
    int ao[4], so2 = WS_OP + jf * 8192 + w * 1024 + lane * 16;
#pragma unroll
    for (int i = 0; i < 4; ++i) { ao[i] = (n >> 2) == i ? L_HH + (n & 3) * 64 + g * 16 : L_ZERO; PIN(ao[i]); }
    PIN(so2);
    STAMP(23);
    if (w == 4) wait_ready(rs, lane, tlo, thi);
    STAMP(24);
    VMWAIT(0);
    STAMP(11);
    BARRIER();
    STAMP(25);
    v4u hv[4];
    int uo = L_V + (8 * g + tq) * 128 + ((4 * (tp >> 1) + ((w & 3) ^ (((tq >> 1) & 1) | ((g & 1) << 1)))) * 16) + 8 * (tp & 1);
    int e0 = L_K + (w >> 1) * 8192 + (tr0 ^ (((8 * (w & 1)) ^ (2 * w)) << 4)), e1 = L_K + (w >> 1) * 8192 + (tr1 ^ (((8 * (w & 1)) ^ (2 * w)) << 4));
    PIN(uo); PIN(e0); PIN(e1);
#define DOWN() do { BARRIER(); v4u a4[4]; \
      _Pragma("unroll") for (int i = 0; i < 4; ++i) a4[i] = *(LAS v4u*)(L + ao[i]); \
      SCHED(); f32x4 d = {0.f, 0.f, 0.f, 0.f}; \
      _Pragma("unroll") for (int i = 0; i < 4; ++i) d = mfma32(a4[i], bd[i], d); \
      st_sc1(rs, so2, nosent4(d)); SCHED(); STAMP(9); } while (0)
    if (w >= 4) {
      f32x4 c4 = {0.f, 0.f, 0.f, 0.f};
#pragma unroll
      for (int i = 0; i < 16; ++i) c4 = mfma32(v4u{0x3F803F80u, 0x3F803F80u, 0x3F803F80u, 0x3F803F80u}, tr_frag(L, uo + i * 4096, uo + i * 4096 + 512), c4);
      *(LAS float*)(L + L_QP + (w & 3) * 64 + n * 4) = c4[0];
#pragma unroll
      for (int i = 0; i < 4; ++i) bd[i] = tr_frag(L, e0 ^ (i << 5), e1 ^ (i << 5));
      PIN(bd[0]); PIN(bd[1]); PIN(bd[2]); PIN(bd[3]);
      if (w > 4) {
        BARRIER();
        BARRIER();
        DOWN();
        EXIT();
      }
      int hr = L_HN + (lane & 3) * 1088 + (lane >> 2) * 16, off1[4];
#pragma unroll
      for (int i = 0; i < 4; ++i) { off1[i] = WS_H + kvh * 8192 + i * 2048 + (lane >> 2) * 128 + (lane & 3) * 16; PIN(off1[i]); }
      PIN(hr);
      STAMP(7);
      poll_fresh<4>(srd_ws, off1, hv);
      STAMP(8);
#pragma unroll
      for (int i = 0; i < 4; ++i) *(LAS v4u*)(L + hr + i * 256) = hv[i];
      BARRIER();
      float s0 = 0.f, s1 = 0.f, q0 = 0.f, q1 = 0.f;
#pragma unroll
      for (int i = 0; i < 4; ++i)
#pragma unroll
        for (int k = 0; k < 4; k += 2) {
          s0 = dot2(hv[i][k], 0x3F803F80u, s0); s1 = dot2(hv[i][k + 1], 0x3F803F80u, s1);
          q0 = dot2(hv[i][k], hv[i][k], q0); q1 = dot2(hv[i][k + 1], hv[i][k + 1], q1);
        }
      s0 += s1; q0 += q1;
      s0 += dpp<ROR4>(s0); q0 += dpp<ROR4>(q0);
      s0 += dpp<ROR8>(s0); q0 += dpp<ROR8>(q0);
      const float mean = x32_sum(x16_sum(s0)) * (1.0f / DM);
      const float q = fmaxf(fmaf(-mean * DM, mean, x32_sum(x16_sum(q0))), 0.f);
      const float inv = __builtin_amdgcn_rcpf(rb(__builtin_amdgcn_sqrtf(rb(rb(q * (1.0f / DM)) + 1e-5f))));
      *(LAS f32x2*)(L + L_RM + (lane & 3) * 8) = f32x2{rb(mean), inv};
      STAMP(27);
      BARRIER();
      DOWN();
      poll_drain<4>(hv);
      EXIT();
    }
    {
      v4u bf[16];
#pragma unroll
      for (int i = 0; i < 16; ++i) bf[i] = tr_frag(L, uo + i * 4096, uo + i * 4096 + 512);
#pragma unroll
      for (int i = 0; i < 4; ++i) bd[i] = tr_frag(L, e0 ^ (i << 5), e1 ^ (i << 5));
      int hb = L_HN + (n >> 2) * 1088 + g * 16, hw = n < 8 ? L_HH + g * 64 + (8 * w + n) * 2 : L_JUNK;
      PIN(hb); PIN(hw); PIN(bd[0]); PIN(bd[1]); PIN(bd[2]); PIN(bd[3]);
#pragma unroll
      for (int i = 0; i < 16; ++i) PIN(bf[i]);
      BARRIER();
      STAMP(26);
      v4u ha[8];
#pragma unroll
      for (int i = 0; i < 8; ++i) ha[i] = *(LAS v4u*)(L + hb + i * 64);
      const float cs = *(LAS float*)(L + L_QP + w * 64 + n * 4);
      SCHED();
      f32x4 ac[2] = {{0.f, 0.f, 0.f, 0.f}, {0.f, 0.f, 0.f, 0.f}};
#pragma unroll
      for (int i = 0; i < 8; ++i) { ac[i & 1] = mfma32(ha[i], bf[i], ac[i & 1]); ha[i] = *(LAS v4u*)(L + hb + (8 + i) * 64); SCHED(); }
#pragma unroll
      for (int i = 0; i < 8; ++i) ac[i & 1] = mfma32(ha[i], bf[8 + i], ac[i & 1]);
      float acc0 = ac[0][0] + ac[1][0];
      PIN(acc0);
      STAMP(28);
      BARRIER();
      const f32x2 st = *(LAS f32x2*)(L + L_RM + g * 8);
      const float a = rb(fmaf(-st[0], cs, acc0) * st[1]), u = dpp<ROR8>(a);
      const float sg = rb(__builtin_amdgcn_rcpf(1.0f + __builtin_amdgcn_exp2f(a * -LOG2E)));
      *(LAS u16*)(L + hw) = (u16)pk2(sg * u, 0.f);
      STAMP(29);
      DOWN();
      EXIT();
    }
  }
  if (is_red && w == 0) {
    const int r2 = lane >> 3, k = lane & 7, bb = r2 & 3;
    const float xv = bflo(x[bb * DM + 8 * rho + k]);
    int off1[8], off2[7];
#pragma unroll
    for (int i = 0; i < 8; ++i) off1[i] = WS_HP + (8 * i + r2) * 8192 + (8 * rho + k) * 16;
#pragma unroll
    for (int i = 0; i < 7; ++i) off2[i] = WS_OP + (8 * i + r2) * 8192 + (8 * rho + k) * 16;
    int ho = lane < 32 ? WS_H + k * 8192 + rho * 128 + bb * 16 : OOB, oo = (k == 0 && lane < 32) ? (bb * DM + 8 * rho) * 2 : OOB;
    PIN(ho); PIN(oo);
    wait_ready(rs, lane, tlo, thi);
    STAMP(7);
    float hval;
    {
      v4u pv[8];
      poll_fresh<8>(srd_ws, off1, pv);
      STAMP(8);
      f32x4 a = ((__builtin_bit_cast(f32x4, pv[0]) + __builtin_bit_cast(f32x4, pv[1])) + (__builtin_bit_cast(f32x4, pv[2]) + __builtin_bit_cast(f32x4, pv[3]))) +
                ((__builtin_bit_cast(f32x4, pv[4]) + __builtin_bit_cast(f32x4, pv[5])) + (__builtin_bit_cast(f32x4, pv[6]) + __builtin_bit_cast(f32x4, pv[7])));
#pragma unroll
      for (int i = 0; i < 4; ++i) { a[i] += dpp<ROR8>(a[i]); a[i] = x32_sum(x16_sum(a[i])); }
      PIN(a);
      const float av = sel4f(bb, a);
      hval = rb(xv + rb(av));
      const unsigned e0 = nosent(pk2(dpp<0xA0>(hval), dpp<0xF5>(hval)));
      const unsigned qa = dppu<0x00>(e0), qb2 = dppu<0xAA>(e0);
      const unsigned oa = dppu<0x141>(qa), ob2 = dppu<0x141>(qb2);
      const bool hi = k & 4;
      st_sc1(rs, ho, v4u{hi ? oa : qa, hi ? ob2 : qb2, hi ? qa : oa, hi ? qb2 : ob2});
      SCHED();
      STAMP(9);
      poll_drain<8>(pv);
    }
    {
      v4u pv[7];
      poll_fresh<7>(srd_ws, off2, pv);
      STAMP(10);
      f32x4 a = ((__builtin_bit_cast(f32x4, pv[0]) + __builtin_bit_cast(f32x4, pv[1])) + (__builtin_bit_cast(f32x4, pv[2]) + __builtin_bit_cast(f32x4, pv[3]))) +
                ((__builtin_bit_cast(f32x4, pv[4]) + __builtin_bit_cast(f32x4, pv[5])) + __builtin_bit_cast(f32x4, pv[6]));
#pragma unroll
      for (int i = 0; i < 4; ++i) { a[i] += dpp<ROR8>(a[i]); a[i] = x32_sum(x16_sum(a[i])); }
      PIN(a);
      const float av = sel4f(bb, a);
      const float ov = hval + rb(av);
      const unsigned dw = pk2(ov, dpp<SHL1>(ov));
      const unsigned dw2 = dppu<SHL2>(dw);
      const unsigned dw4 = dppu<SHL4>(dw), dw6 = dppu<SHL4>(dw2);
      __builtin_amdgcn_raw_buffer_store_b128(v4u{dw, dw2, dw4, dw6}, __builtin_amdgcn_make_buffer_rsrc(out, 0, 4 * DM * 2, 0x00020000), oo, 0, 0);
      st8_sc1_asm(v2u{tlo, thi}, (c == 192 && lane == 0) ? WS_EPOCH : OOB, srd_ws);
      SCHED();
      STAMP(11);
      poll_drain<7>(pv);
    }
  }
  STAMP_OUT();
}

}

extern "C" void kernel_launch(void* const* d_in, const int* in_sizes, int n_in, void* d_out, int out_size, void* d_ws, size_t ws_size,
                              hipStream_t stream) {
  (void)in_sizes; (void)n_in; (void)out_size; (void)ws_size;
  fused_block<<<256, NTHR, 0, stream>>>((const u16*)d_in[0], (const u16*)d_in[1], (const u16*)d_in[2], (const u16*)d_in[3], (const u16*)d_in[4],
                                        (const u16*)d_in[6], (const u16*)d_in[7], (const u16*)d_in[8], (u16*)d_out, (unsigned char*)d_ws);
}
```

```cpp
#include <hip/hip_runtime.h>
#include <hip/hip_bf16.h>
#include <stdint.h>

namespace {

typedef unsigned short u16;
typedef unsigned long long u64;
typedef unsigned v4u __attribute__((ext_vector_type(4)));
typedef unsigned v2u __attribute__((ext_vector_type(2)));
typedef float f32x4 __attribute__((ext_vector_type(4)));
typedef float f32x2 __attribute__((ext_vector_type(2)));
typedef short bf16x8 __attribute__((ext_vector_type(8)));
typedef short s16x4 __attribute__((ext_vector_type(4)));
typedef __bf16 bf2 __attribute__((ext_vector_type(2)));
#define LAS __attribute__((address_space(3)))
#define GAS __attribute__((address_space(1)))

constexpr int DM = 512, HKV = 8, SK = 8192, DH = 128, WIN = 2048, FF = 1792, NQ = 4096;
constexpr int NTHR = 640;

constexpr int WS_EPOCH = 0;
constexpr int WS_RDY = 4096;
constexpr int WS_QG = 8192;
constexpr int WS_PA = WS_QG + 512 * 1024;
constexpr int WS_HP = WS_PA + 512 * 1024;
constexpr int WS_H = WS_HP + 512 * 1024;
constexpr int WS_OP = WS_H + 65536;
constexpr int WS_END = WS_OP + 56 * 8192;
constexpr int OOB = 0x40000000;

constexpr int L_K = 0;
constexpr int L_V = 65536;
constexpr int L_WQ = 131072;
constexpr int L_HN = L_WQ;
constexpr int L_Q = L_WQ + 16384;
constexpr int L_XN = L_Q + 1280;
constexpr int L_O = L_XN;
constexpr int L_QP = L_XN + 1280;
constexpr int L_P = L_QP + 1024;
constexpr int L_RM = L_P + 2304;
constexpr int L_RL = L_RM + 128;
constexpr int L_HH = L_RL + 128;
constexpr int L_ZERO = L_HH + 256;
constexpr int L_FLAG = L_ZERO + 128;
constexpr int L_JUNK = L_FLAG + 16;
constexpr int L_BYTES = L_JUNK + 2048;

constexpr unsigned SENT = 0xFFFFFFFFu;
constexpr int SPIN_CAP = 1 << 16;
constexpr int AUX_SC1 = 16;
constexpr float LOG2E = 1.4426950408889634f;

#define POLL_SLEEP 1
#define ABL_NOW 0
#define CODE_BYTES $
#define WQ_POL "nt"
#define QPP 1
#define QKP 5
#define QG_DELAY 30
#define WINDOW 3
#define POLL_DEPTH 3
#define P1_EARLY 1
#define GU_WAIT 1
#define PF_NT 0
#define PF_WIN1 0
#define PF_DELAY 0
#define WO_EARLY 1
#define WO_POS 0
#define DEPTH_A 1
#define DEPTH_H 2
#define DEPTH_R 3
#define C_DELAY 10
#define PF_EARLY 1
#define QG_DEPTH 1
#define QG_GAP 16
#define K0_DELAY 0
#define K1_DELAY 0
#define KV_NT 1
#define WQ_NT 1
#define W2_NT 1
#define GU_NT 1

__device__ __forceinline__ float bflo(unsigned x) { return __builtin_bit_cast(float, x << 16); }
__device__ __forceinline__ float bfhi(unsigned x) { return __builtin_bit_cast(float, x & 0xffff0000u); }
__device__ __forceinline__ unsigned pk2(float lo, float hi) {
  bf2 v = {(__bf16)lo, (__bf16)hi};
  return __builtin_bit_cast(unsigned, v);
}
__device__ __forceinline__ float rb(float x) { return (float)(__bf16)x; }
__device__ __forceinline__ unsigned nosent(unsigned x) { return x < 0xFFFFFFFEu ? x : 0xFFFFFFFEu; }
__device__ __forceinline__ v4u nosent4(f32x4 v) {
  return v4u{nosent(__float_as_uint(v[0])), nosent(__float_as_uint(v[1])), nosent(__float_as_uint(v[2])), nosent(__float_as_uint(v[3]))};
}
__device__ __forceinline__ bool fresh(v4u v) { return max(max(v[0], v[1]), max(v[2], v[3])) != SENT; }
__device__ __forceinline__ float vmax(float a, float b) { float r; asm("v_max_f32 %0, %1, %2" : "=v"(r) : "v"(a), "v"(b)); return r; }

__device__ __forceinline__ float vmax3(float a, float b, float c) { float r; asm("v_max3_f32 %0, %1, %2, %3" : "=v"(r) : "v"(a), "v"(b), "v"(c)); return r; }
__device__ __forceinline__ void row_max2(float& a, float& b) {
  asm("s_nop 1\n\t"
      "v_max_f32_dpp %0, %0, %0 row_ror:8 row_mask:0xf bank_mask:0xf\n\tv_max_f32_dpp %1, %1, %1 row_ror:8 row_mask:0xf bank_mask:0xf\n\ts_nop 0\n\t"
      "v_max_f32_dpp %0, %0, %0 row_ror:4 row_mask:0xf bank_mask:0xf\n\tv_max_f32_dpp %1, %1, %1 row_ror:4 row_mask:0xf bank_mask:0xf\n\ts_nop 0\n\t"
      "v_max_f32_dpp %0, %0, %0 row_ror:2 row_mask:0xf bank_mask:0xf\n\tv_max_f32_dpp %1, %1, %1 row_ror:2 row_mask:0xf bank_mask:0xf\n\ts_nop 0\n\t"
      "v_max_f32_dpp %0, %0, %0 row_ror:1 row_mask:0xf bank_mask:0xf\n\tv_max_f32_dpp %1, %1, %1 row_ror:1 row_mask:0xf bank_mask:0xf\n\ts_nop 1"
      : "+v"(a), "+v"(b));
}

__device__ __forceinline__ float dot2(unsigned a, unsigned b, float c) {
  return __builtin_amdgcn_fdot2_f32_bf16(__builtin_bit_cast(bf2, a), __builtin_bit_cast(bf2, b), c, false);
}
__device__ __forceinline__ f32x4 mfma32(v4u a, v4u b, f32x4 c) {
  return __builtin_amdgcn_mfma_f32_16x16x32_bf16(__builtin_bit_cast(bf16x8, a), __builtin_bit_cast(bf16x8, b), c, 0, 0, 0);
}
template <int CTRL> __device__ __forceinline__ float dpp(float x) {
  return __builtin_bit_cast(float, __builtin_amdgcn_mov_dpp(__builtin_bit_cast(int, x), CTRL, 0xf, 0xf, true));
}
template <int CTRL> __device__ __forceinline__ unsigned dppu(unsigned x) { return (unsigned)__builtin_amdgcn_mov_dpp((int)x, CTRL, 0xf, 0xf, true); }
constexpr int ROR8 = 0x128, ROR4 = 0x124, ROR2 = 0x122, ROR1 = 0x121, SHL1 = 0x101, SHL2 = 0x102, SHL4 = 0x104;
__device__ __forceinline__ float row_max(float x) { x = fmaxf(x, dpp<ROR8>(x)); x = fmaxf(x, dpp<ROR4>(x)); x = fmaxf(x, dpp<ROR2>(x)); return fmaxf(x, dpp<ROR1>(x)); }
__device__ __forceinline__ float row_sum(float x) { x += dpp<ROR8>(x); x += dpp<ROR4>(x); x += dpp<ROR2>(x); return x + dpp<ROR1>(x); }
__device__ __forceinline__ float x16_max(float x) { auto s = __builtin_amdgcn_permlane16_swap(__float_as_uint(x), __float_as_uint(x), false, false); return vmax(__uint_as_float(s[0]), __uint_as_float(s[1])); }
__device__ __forceinline__ float x32_max(float x) { auto s = __builtin_amdgcn_permlane32_swap(__float_as_uint(x), __float_as_uint(x), false, false); return vmax(__uint_as_float(s[0]), __uint_as_float(s[1])); }
__device__ __forceinline__ float x16_sum(float x) { auto s = __builtin_amdgcn_permlane16_swap(__float_as_uint(x), __float_as_uint(x), false, false); return __uint_as_float(s[0]) + __uint_as_float(s[1]); }
__device__ __forceinline__ float x32_sum(float x) { auto s = __builtin_amdgcn_permlane32_swap(__float_as_uint(x), __float_as_uint(x), false, false); return __uint_as_float(s[0]) + __uint_as_float(s[1]); }
__device__ __forceinline__ float wave_sum(float x) { return x32_sum(x16_sum(row_sum(x))); }
__device__ __forceinline__ f32x4 sel2(bool c, f32x4 a, f32x4 b) {
  return f32x4{c ? b[0] : a[0], c ? b[1] : a[1], c ? b[2] : a[2], c ? b[3] : a[3]};
}
__device__ __forceinline__ f32x4 sel4(int g, f32x4 a, f32x4 b, f32x4 c, f32x4 d) { return sel2(g & 2, sel2(g & 1, a, b), sel2(g & 1, c, d)); }

#define PIN(x) asm volatile("" : "+v"(x))
#define SCHED() __builtin_amdgcn_sched_barrier(0)
#define LDS_FENCE() asm volatile("s_waitcnt lgkmcnt(0)" ::: "memory")
#define BARRIER() do { asm volatile("s_waitcnt lgkmcnt(0)\n\ts_barrier" ::: "memory"); } while (0)
#define VMWAIT(N) asm volatile("s_waitcnt vmcnt(" #N ")" ::: "memory")
#define STAMP(i) do {} while (0)
#define STAMP_OUT() do {} while (0)

#define EXIT() do { STAMP_OUT(); asm volatile("s_endpgm" ::: "memory"); __builtin_unreachable(); } while (0)

__device__ __forceinline__ v4u make_srd(const void* p, unsigned bytes) {
  const u64 a = (u64)p;
  return v4u{(unsigned)a, (unsigned)(a >> 32) & 0xffffu, bytes, 0x00020000u};
}
template <int NT> __device__ __forceinline__ void dma_buf(unsigned lds, int voff, v4u srd, int soff) {
  if (NT) asm volatile("s_mov_b32 m0, %0\n\ts_nop 0\n\tbuffer_load_dwordx4 %1, %2, %3 offen nt lds" :: "s"(lds), "v"(voff), "s"(srd), "s"(soff) : "m0", "memory");
  else asm volatile("s_mov_b32 m0, %0\n\ts_nop 0\n\tbuffer_load_dwordx4 %1, %2, %3 offen lds" :: "s"(lds), "v"(voff), "s"(srd), "s"(soff) : "m0", "memory");
}
template <int NT> __device__ __forceinline__ void dma_glb(unsigned lds, const void* p) {
  if (NT) asm volatile("s_mov_b32 m0, %0\n\ts_nop 0\n\tglobal_load_lds_dwordx4 %1, off nt" :: "s"(lds), "v"(p) : "m0", "memory");
  else asm volatile("s_mov_b32 m0, %0\n\ts_nop 0\n\tglobal_load_lds_dwordx4 %1, off" :: "s"(lds), "v"(p) : "m0", "memory");
}
__device__ __forceinline__ void ld16_asm(v4u& d, int voff, v4u srd, int soff) {
  asm volatile("buffer_load_dwordx4 %0, %1, %2, %3 offen" : "=v"(d) : "v"(voff), "s"(srd), "s"(soff) : "memory");
}
__device__ __forceinline__ void ld16_sc1_asm(v4u& d, int voff, v4u srd) {
  asm volatile("buffer_load_dwordx4 %0, %1, %2, 0 offen sc1" : "=v"(d) : "v"(voff), "s"(srd) : "memory");
}
__device__ __forceinline__ void ld8_sc1_asm(v2u& d, v4u srd) {
  asm volatile("buffer_load_dwordx2 %0, off, %1, 0 sc1" : "=v"(d) : "s"(srd) : "memory");
}
__device__ __forceinline__ void st16_sc1_asm(v4u v, int voff, v4u srd) {
  asm volatile("buffer_store_dwordx4 %0, %1, %2, 0 offen sc1\n\ts_nop 1" :: "v"(v), "v"(voff), "s"(srd) : "memory");
}

__device__ __forceinline__ void st8_sc1_asm(v2u v, int voff, v4u srd) {
  asm volatile("buffer_store_dwordx2 %0, %1, %2, 0 offen sc1" :: "v"(v), "v"(voff), "s"(srd) : "memory");
}
__device__ __forceinline__ float sel4f(int b, f32x4 a) { const float lo = (b & 1) ? a[1] : a[0], hi = (b & 1) ? a[3] : a[2]; float r = (b & 2) ? hi : lo; return r; }

__device__ __forceinline__ v4u ld_sc1(__amdgpu_buffer_rsrc_t rs, int off) { return __builtin_amdgcn_raw_buffer_load_b128(rs, off, 0, AUX_SC1); }
__device__ __forceinline__ void st_sc1(__amdgpu_buffer_rsrc_t rs, int off, v4u v) { __builtin_amdgcn_raw_buffer_store_b128(v, rs, off, 0, AUX_SC1); }
__device__ __forceinline__ v2u tr_rd(LAS unsigned char* L, int off) {
  return __builtin_bit_cast(v2u, __builtin_amdgcn_ds_read_tr16_b64_v4i16((LAS s16x4*)(L + off)));
}
__device__ __forceinline__ v4u tr_frag(LAS unsigned char* L, int off0, int off1) {
  const v2u a = tr_rd(L, off0), b = tr_rd(L, off1);
  return v4u{a[0], a[1], b[0], b[1]};
}

__device__ __forceinline__ void ld16_sc1_rw(v4u& d, int voff, v4u srd) {
  asm volatile("buffer_load_dwordx4 %0, %1, %2, 0 offen sc1" : "+v"(d) : "v"(voff), "s"(srd) : "memory");
}
template <int CNT> __device__ __forceinline__ void vmwait_pin(v4u& a) { asm volatile("s_waitcnt vmcnt(%1)" : "+v"(a) : "n"(CNT) : "memory"); }
template <int N, int D> __device__ __forceinline__ void poll_start(v4u srd, const int (&off)[N], v4u (&v)[N]) {
#pragma unroll
  for (int i = 0; i < N; ++i) v[i] = v4u{SENT, SENT, SENT, SENT};
#pragma unroll
  for (int k = 0; k < D - 1; ++k) {
#pragma unroll
    for (int i = 0; i < N; ++i) ld16_sc1_rw(v[i], off[i], srd);
    __builtin_amdgcn_s_sleep(POLL_SLEEP);
  }
}
__device__ __forceinline__ void ld16_sc1_masked(v4u& d, int voff, v4u srd, u64 mask) {
  asm volatile("s_mov_b64 exec, %3\n\tbuffer_load_dwordx4 %0, %1, %2, 0 offen sc1\n\ts_mov_b64 exec, -1" : "+v"(d) : "v"(voff), "s"(srd), "s"(mask) : "memory");
}
template <int N, int D> __device__ __forceinline__ void poll_finish(v4u srd, const int (&off)[N], v4u (&v)[N]) {
  u64 miss[N];
#pragma unroll
  for (int i = 0; i < N; ++i) miss[i] = ~0ull;
#pragma unroll 1
  for (int it = 0; it < SPIN_CAP; ++it) {
#pragma unroll
    for (int i = 0; i < N; ++i) ld16_sc1_masked(v[i], off[i], srd, miss[i]);
    vmwait_pin<(D - 1) * N>(v[0]);
#pragma unroll
    for (int i = 1; i < N; ++i) PIN(v[i]);
    u64 any = 0;
#pragma unroll
    for (int i = 0; i < N; ++i) { miss[i] = __builtin_amdgcn_ballot_w64(!fresh(v[i])); any |= miss[i]; }
    if (any == 0) break;
    __builtin_amdgcn_s_sleep(POLL_SLEEP);
  }
}
template <int N, int D> __device__ __forceinline__ void poll_fresh(v4u srd, const int (&off)[N], v4u (&v)[N]) {
  poll_start<N, D>(srd, off, v);
  poll_finish<N, D>(srd, off, v);
}
template <int N> __device__ __forceinline__ void poll_drain(v4u (&v)[N]) {
  vmwait_pin<0>(v[0]);
#pragma unroll
  for (int i = 1; i < N; ++i) PIN(v[i]);
}
__device__ __forceinline__ void wait_ready(__amdgpu_buffer_rsrc_t rs, int lane, unsigned tlo, unsigned thi) {
#pragma unroll 1
  for (int it = 0; it < SPIN_CAP; ++it) {
    asm volatile("" ::: "memory");
    const v4u r0 = ld_sc1(rs, WS_RDY + lane * 16), r1 = ld_sc1(rs, WS_RDY + 1024 + lane * 16);
    const bool ok = (r0[0] == tlo) & (r0[1] == thi) & (r0[2] == tlo) & (r0[3] == thi) & (r1[0] == tlo) & (r1[1] == thi) & (r1[2] == tlo) & (r1[3] == thi);
    if (__all(ok)) break;
    __builtin_amdgcn_s_sleep(4);
  }
}

__global__ void __launch_bounds__(NTHR, 3)
fused_block(const u16* __restrict__ x, const u16* __restrict__ kc, const u16* __restrict__ vc, const u16* __restrict__ wq,
            const u16* __restrict__ wo, const u16* __restrict__ wg, const u16* __restrict__ wu, const u16* __restrict__ wd,
            u16* __restrict__ out, unsigned char* __restrict__ ws) {
  __shared__ __attribute__((aligned(16))) unsigned char smem_[L_BYTES];
  {
    u64 pc0;
    asm volatile("s_getpc_b64 %0" : "=s"(pc0));
    if (__builtin_amdgcn_readfirstlane(threadIdx.x >> 6) == 1) {
      const v4u srd_c = make_srd((const void*)(pc0 & ~255ull), CODE_BYTES);
      const int so = ((blockIdx.x >> 3) & 15) * 2048;
      asm volatile("s_mov_b32 m0, %0\n\ts_nop 0\n\tbuffer_load_dwordx4 %1, %2, %3 offen lds\n\tbuffer_load_dwordx4 %1, %2, %3 offen offset:1024 lds"
                   :: "s"((unsigned)(size_t)(LAS unsigned char*)smem_ + L_JUNK), "v"((threadIdx.x & 63) * 16), "s"(srd_c), "s"(so) : "m0", "memory");
    }
    __builtin_amdgcn_sched_barrier(0);
  }
  LAS unsigned char* L = (LAS unsigned char*)smem_;
  const unsigned lbase = (unsigned)(size_t)L;
  const int c = blockIdx.x, kvh = c & 7, i32 = c >> 3;
  const int lane = threadIdx.x & 63, wv = __builtin_amdgcn_readfirstlane(threadIdx.x >> 6);
  const int w = wv < 2 ? 8 + wv : wv - 2;
  const int n = lane & 15, g = lane >> 4;
  const v4u srd_ws = make_srd(ws, WS_END);
  STAMP(0);

  const int b = i32 >> 3, s = i32 & 7;
  const size_t kvbase = ((size_t)(b * HKV + kvh) * SK + (SK - WIN) + s * 256) * DH;
  const v4u srd_k = make_srd(kc + kvbase, 65536);
  const int rot = w + 37 * c;
  const int kv0 = g * 256 + ((n ^ g) * 16);
  const unsigned ldst = lbase + w * 1024;
  v2u ep;
  v4u xr[4];
#define KLOAD(i) dma_buf<KV_NT>(ldst + L_K + (i) * 8192, kv0 ^ (((i) & 3) << 6), srd_k, ((8 * (i) + rot) & 63) << 10)
  if (wv == 0) {
    const v4u srd_x = make_srd(x, 4 * DM * 2);
    const v4u srd_wq = make_srd(wq + (size_t)b * 128 * NQ + kvh * 512 + s * 64, 128 * NQ * 2);
    ld8_sc1_asm(ep, srd_ws);
#pragma unroll
    for (int i = 0; i < 4; ++i) ld16_asm(xr[i], g * 1024 + n * 16, srd_x, ((b + i) & 3) * 256);
    const int row = lane >> 3, v0 = row * (NQ * 2) + (((lane & 7) ^ (2 * ((row >> 1) & 1))) * 16);
    int so;
#define WQ2 "buffer_load_dwordx4 %1, %3, %0 offen " WQ_POL " lds\n\ts_addk_i32 m0, 0x400\n\ts_add_u32 %0, %0, %5\n\t" \
            "buffer_load_dwordx4 %2, %3, %0 offen " WQ_POL " lds\n\ts_addk_i32 m0, 0x400\n\ts_add_u32 %0, %0, %5\n\t"
    asm volatile("s_mov_b32 m0, %4\n\ts_mov_b32 %0, 0\n\t" WQ2 WQ2 WQ2 WQ2 WQ2 WQ2 WQ2 WQ2
                 : "=&s"(so) : "v"(v0), "v"(v0 ^ 64), "s"(srd_wq), "s"(lbase + L_WQ), "s"(8 * NQ * 2) : "m0", "scc", "memory");
    *(LAS unsigned*)(L + L_FLAG) = 0u;
    LDS_FENCE();
  }
  asm volatile("s_barrier" ::: "memory");
  if (wv >= 2) { __builtin_amdgcn_s_sleep(K0_DELAY); KLOAD(0); __builtin_amdgcn_s_sleep(K1_DELAY); KLOAD(1); KLOAD(2); }
  int wv2 = wv;
  asm volatile("" : "+s"(wv2));

  if (wv2 == 1) {
    v2u ep1;
    ld8_sc1_asm(ep1, srd_ws);
    const v4u sent = {SENT, SENT, SENT, SENT};
    st16_sc1_asm(sent, WS_PA + c * 2048 + lane * 16, srd_ws);
    st16_sc1_asm(sent, WS_PA + c * 2048 + 1024 + lane * 16, srd_ws);
    const int base = i32 < 16 ? WS_HP + (kvh * 8 + (i32 >> 1)) * 8192 + (i32 & 1) * 4096 : WS_OP + (kvh * 7 + i32 - 16) * 8192;
    const int len = i32 < 16 ? 4096 : i32 < 23 ? 8192 : 0;
#pragma unroll 1
    for (int o = 0; o < len; o += 1024) st16_sc1_asm(sent, base + o + lane * 16, srd_ws);
    st16_sc1_asm(sent, (i32 >= 24 && lane < 32) ? WS_H + (lane >> 2) * 8192 + ((i32 - 24) * 8 + kvh) * 128 + (lane & 3) * 16 : OOB, srd_ws);
    *(LAS v4u*)(L + L_ZERO + (lane & 7) * 16) = v4u{0u, 0u, 0u, 0u};
    asm volatile("s_waitcnt vmcnt(0)" : "+v"(ep1) :: "memory");
    const u64 tag = (((u64)ep1[1] << 32) | ep1[0]) + 0x9E3779B97F4A7C15ull;
    st8_sc1_asm(v2u{(unsigned)tag, (unsigned)(tag >> 32)}, lane == 0 ? WS_RDY + (kvh * 32 + i32) * 8 : OOB, srd_ws);
    STAMP(1);
    EXIT();
  }
  if (wv2 == 0) {
    const int rq = b, cq = s;
    STAMP(7);
    asm volatile("s_waitcnt vmcnt(16)" : "+v"(ep), "+v"(xr[0]), "+v"(xr[1]), "+v"(xr[2]), "+v"(xr[3]) :: "memory");
    STAMP(9);
    {
      float s0 = 0.f, s1 = 0.f, q0 = 0.f, q1 = 0.f;
#pragma unroll
      for (int i = 0; i < 4; ++i)
#pragma unroll
        for (int k = 0; k < 4; k += 2) {
          s0 = dot2(xr[i][k], 0x3F803F80u, s0); s1 = dot2(xr[i][k + 1], 0x3F803F80u, s1);
          q0 = dot2(xr[i][k], xr[i][k], q0); q1 = dot2(xr[i][k + 1], xr[i][k + 1], q1);
        }
      const float mean = row_sum(s0 + s1) * (1.0f / DM);
      const float q = fmaxf(fmaf(-mean * DM, mean, row_sum(q0 + q1)), 0.f);
      float v[8];
#pragma unroll
      for (int k = 0; k < 4; ++k) { v[2 * k] = bflo(xr[0][k]); v[2 * k + 1] = bfhi(xr[0][k]); }
      const float mb = rb(mean);
      const float inv = __builtin_amdgcn_rcpf(rb(__builtin_amdgcn_sqrtf(rb(rb(q * (1.0f / DM)) + 1e-5f))));
      v4u o;
#pragma unroll
      for (int k = 0; k < 4; ++k) o[k] = pk2(rb(v[2 * k] - mb) * inv, rb(v[2 * k + 1] - mb) * inv);
      *(LAS v4u*)(L + L_XN + g * 320 + n * 16) = o;
    }
    STAMP(1);
    LDS_FENCE();
    f32x4 acc[4];
    {
      v4u xa[4];
#pragma unroll
      for (int ks = 0; ks < 4; ++ks) xa[ks] = *(LAS v4u*)(L + L_XN + (n & 3) * 320 + ks * 64 + g * 16);
      const int q4 = n >> 2, p = n & 3, y = ((q4 >> 1) & 1) | ((g & 1) << 1);
      const int rbase = L_WQ + (8 * g + q4) * 128 + 8 * (p & 1);
      int o[4];
#pragma unroll
      for (int t = 0; t < 4; ++t) { o[t] = rbase + (((2 * t + (p >> 1)) ^ (2 * y)) * 16); acc[t] = f32x4{0.f, 0.f, 0.f, 0.f}; }
#pragma unroll
      for (int ks = 0; ks < 4; ++ks) {
        if (ks == 0) VMWAIT(12); else if (ks == 1) VMWAIT(8); else if (ks == 2) VMWAIT(4); else VMWAIT(0);
        if (ks == 3) STAMP(4);
#pragma unroll
        for (int t = 0; t < 4; ++t) acc[t] = mfma32(xa[ks], tr_frag(L, o[t] + ks * 4096, o[t] + ks * 4096 + 512), acc[t]);
      }
    }
    *(LAS f32x4*)(L + L_QP + lane * 16) = sel4(g, acc[0], acc[1], acc[2], acc[3]);
    LDS_FENCE();
    const f32x4 q0 = *(LAS f32x4*)(L + L_QP + (lane & 31) * 32), q1 = *(LAS f32x4*)(L + L_QP + (lane & 31) * 32 + 16);
    const u64 tag = (((u64)ep[1] << 32) | ep[0]) + 0x9E3779B97F4A7C15ull;
    const unsigned tlo = (unsigned)tag, thi = (unsigned)(tag >> 32);
    STAMP(5);
    STAMP(2);
    {
      const bool up = lane >= 32;
      const int o = WS_QG + ((kvh * 4 + (up ? 1 : 0)) * 4 + rq) * 4096 + (32 * cq + (lane & 31)) * 16;
      st16_sc1_asm(v4u{__float_as_uint(up ? q0[1] : q0[0]), __float_as_uint(up ? q1[1] : q1[0]), tlo, thi}, o, srd_ws);
      st16_sc1_asm(v4u{__float_as_uint(up ? q0[3] : q0[2]), __float_as_uint(up ? q1[3] : q1[2]), tlo, thi}, o + 2 * 4 * 4096, srd_ws);
    }
    STAMP(3);
    __builtin_amdgcn_s_sleep(QG_DELAY);
    const int qo = WS_QG + (kvh * 4 + b) * 16384 + lane * 16;
    v4u qg[16];
    u64 miss[16];
#pragma unroll
    for (int i = 0; i < 16; ++i) { asm volatile("" : "=v"(qg[i])); miss[i] = ~0ull; }
#define QPASS() do { _Pragma("unroll") for (int i = 0; i < 16; ++i) \
      asm volatile("s_mov_b64 exec, %4\n\tbuffer_load_dwordx4 %0, %1, %2, %3 offen offset:%5 sc1\n\ts_mov_b64 exec, -1" \
                   : "+v"(qg[i]) : "v"(qo), "s"(srd_ws), "s"((i >> 2) * 4096), "s"(miss[i]), "n"((i & 3) * 1024) : "memory"); } while (0)
#pragma unroll 1
    for (int it = 0; it < SPIN_CAP; ++it) {
      QPASS();
      vmwait_pin<16 * (QG_DEPTH - 1)>(qg[0]);
#pragma unroll
      for (int i = 1; i < 16; ++i) PIN(qg[i]);
      u64 any = 0;
#pragma unroll
      for (int i = 0; i < 16; ++i) { miss[i] = __builtin_amdgcn_ballot_w64((((u64)qg[i][3] << 32) | qg[i][2]) != tag); any |= miss[i]; }
      if (any == 0) break;
    }
#pragma unroll
    for (int u = 0; u < 4; ++u) {
      const float a = (__uint_as_float(qg[u][0]) + __uint_as_float(qg[4 + u][0])) + (__uint_as_float(qg[8 + u][0]) + __uint_as_float(qg[12 + u][0]));
      const float bq = (__uint_as_float(qg[u][1]) + __uint_as_float(qg[4 + u][1])) + (__uint_as_float(qg[8 + u][1]) + __uint_as_float(qg[12 + u][1]));
      *(LAS unsigned*)(L + L_Q + u * 320 + lane * 4) = pk2(a, bq);
    }
    *(LAS v2u*)(L + L_FLAG + 8) = v2u{tlo, thi};
    LDS_FENCE();
    *(volatile LAS unsigned*)(L + L_FLAG) = 1u;
    poll_drain<16>(qg);
    STAMP(6);
    EXIT();
  }

  const bool is_wo = i32 < 16, is_ffn = i32 >= 16 && i32 < 23, is_red = i32 >= 24;
  const int jf = kvh * 7 + (i32 - 16);
  const int rho = (i32 - 24) * 8 + kvh;
  const __amdgpu_buffer_rsrc_t rs = __builtin_amdgcn_make_buffer_rsrc(ws, 0, WS_END, 0x00020000);
  const v4u srd_v = make_srd(vc + kvbase, 65536);
  const int vv0 = g * 256 + ((n ^ ((g << 2) | (w & 3))) * 16);
  asm volatile("" :: "s"(wo), "s"(wg), "s"(wu), "s"(wd), "s"(out), "s"(x));
#define VLOAD(i) dma_buf<KV_NT>(ldst + L_V + (i) * 8192, vv0, srd_v, ((8 * (i) + rot) & 63) << 10)
#define WW() VMWAIT(2)
  WW();
  STAMP(12);
  KLOAD(3);
  WW(); KLOAD(4); WW(); KLOAD(5); WW(); KLOAD(6); WW(); KLOAD(7);
  STAMP(1);
  WW(); VLOAD(0); WW(); VLOAD(1); WW(); VLOAD(2);
  constexpr int VNEXT = 3;
  v4u kf[8];
  {
    const int kb = L_K + (n >> 2) * 8192 + w * 1024 + (n & 3) * 256 + ((g ^ n) * 16);
#pragma unroll
    for (int ks = 0; ks < 4; ++ks) { kf[ks] = *(LAS v4u*)(L + (kb ^ (ks << 6))); kf[4 + ks] = *(LAS v4u*)(L + (kb ^ (ks << 6)) + 32768); }
  }
  int qb = L_Q + (2 * (n >> 3) + (n & 1)) * 320 + g * 16;
  int rmw = L_RM + (g >> 1) * 64 + w * 8, rmr = L_RM + (g >> 1) * 64;
  int po = L_P + (g >> 1) * 1152 + ((4 * (g & 1) + (n >> 2)) * 32 + 4 * w + (n & 3)) * 2;
  const u16* wp0 = is_wo ? wo + (size_t)(kvh * 512 + (2 * (i32 >> 3) + (w >> 2)) * 128 + 32 * ((i32 >> 1) & 3) + 4 * (w & 3)) * DM + (i32 & 1) * 256 + g * (DM - 128) + (vv0 >> 1)
                         : (lane >= 32 ? wu : wg) + (size_t)(64 * w + (i32 & 7) + (i32 & 8) * 4) * FF + ((224 * kvh) & ~63) + 8 * (lane & 31);
  PIN(wp0);
  const int tq = n >> 2, tp = n & 3;
  int tr0 = (8 * g + tq) * 256 + (((2 * w + (tp >> 1)) ^ ((tq << 2) | ((2 * g) & 3))) * 16) + 8 * (tp & 1);
  int tr1 = (8 * g + 4 + tq) * 256 + (((2 * w + (tp >> 1)) ^ ((tq << 2) | ((2 * g + 1) & 3))) * 16) + 8 * (tp & 1);
  PIN(tr0); PIN(tr1);
  int tv0 = L_V + tr0, tv1 = L_V + tr1, pab = L_P + (n >> 2) * 576 + g * 16;
  PIN(tv0); PIN(tv1); PIN(pab);
  PIN(qb); PIN(rmw); PIN(rmr); PIN(po);
#pragma unroll
  for (int i = 0; i < 8; ++i) PIN(kf[i]);
#pragma unroll
  for (int i = VNEXT; i <= QKP; ++i) { WW(); VLOAD(i); }
  STAMP(13);
#pragma unroll 1
  for (int it = 0; it < (SPIN_CAP << 4); ++it) {
    if (__builtin_amdgcn_readfirstlane(*(volatile LAS unsigned*)(L + L_FLAG)) != 0u) break;
  }
  asm volatile("" ::: "memory");
  const v2u tagv = *(LAS v2u*)(L + L_FLAG + 8);
  const unsigned tlo = tagv[0], thi = tagv[1];
  STAMP(3);
  float sc[2];
  {
    v4u qa[4];
#pragma unroll
    for (int ks = 0; ks < 4; ++ks) qa[ks] = *(LAS v4u*)(L + qb + ks * 64);
    f32x4 d0 = {0.f, 0.f, 0.f, 0.f}, d1 = {0.f, 0.f, 0.f, 0.f};
#pragma unroll
    for (int ks = 0; ks < 4; ++ks) { d0 = mfma32(qa[ks], kf[ks], d0); d1 = mfma32(qa[ks], kf[4 + ks], d1); }
#pragma unroll
    for (int j = 0; j < 2; ++j) sc[j] = rb(rb((g & 1) ? d1[j] : d0[j]) * 0.08838834764831845f);
    float m0 = sc[0], m1 = sc[1];
    row_max2(m0, m1);
    const f32x2 mx = {x16_max(m0), x16_max(m1)};
    *(LAS f32x2*)(L + rmw) = mx;
  }
#pragma unroll
  for (int i = QKP + 1; i < 8; ++i) VLOAD(i);
  STAMP(2);
  BARRIER();
  float mh;
  {
    f32x4 t[4];
#pragma unroll
    for (int i = 0; i < 4; ++i) t[i] = *(LAS f32x4*)(L + rmr + i * 16);
    const float M0 = vmax3(vmax3(t[0][0], t[0][2], t[1][0]), vmax3(t[1][2], t[2][0], t[2][2]), vmax(t[3][0], t[3][2]));
    const float M1 = vmax3(vmax3(t[0][1], t[0][3], t[1][1]), vmax3(t[1][3], t[2][1], t[2][3]), vmax(t[3][1], t[3][3]));
    const float p0 = __builtin_amdgcn_exp2f((sc[0] - M0) * LOG2E), p1 = __builtin_amdgcn_exp2f((sc[1] - M1) * LOG2E);
    const unsigned pp = pk2(p0, p1);
    *(LAS u16*)(L + po) = (u16)pp;
    *(LAS u16*)(L + po + 576) = (u16)(pp >> 16);
    const f32x2 ls = {x16_sum(row_sum(p0)), x16_sum(row_sum(p1))};
    *(LAS f32x2*)(L + rmw + (L_RL - L_RM)) = ls;
    mh = (g & 1) ? M1 : M0;
  }
  const bool chk = is_wo && w == 0;
  const int roff = WS_RDY + kvh * 256 + n * 16;
  v4u rdy;
  ld16_sc1_asm(rdy, chk ? roff : OOB, srd_ws);
#define WOLOAD() do { if (is_wo) { _Pragma("unroll") for (int i = 0; i < 4; ++i) dma_glb<W2_NT>(ldst + L_K + i * 8192, wp0 + (i >> 1) * 16 * DM + (i & 1) * 128); } \
  else { _Pragma("unroll") for (int i = 0; i < 4; ++i) dma_glb<PF_NT>(ldst + L_K + 32768, PF_EARLY ? wp0 + i * 8 * FF : x); } } while (0)
  WOLOAD();
  VMWAIT(7);
  STAMP(14);
  BARRIER();
    v4u pa[8];
#pragma unroll
    for (int i = 0; i < 8; ++i) pa[i] = *(LAS v4u*)(L + pab + i * 64);
    f32x4 t[4];
#pragma unroll
    for (int i = 0; i < 4; ++i) t[i] = *(LAS f32x4*)(L + rmr + (L_RL - L_RM) + i * 16);
    f32x4 acc = {0.f, 0.f, 0.f, 0.f};
#pragma unroll
    for (int i = 0; i < 6; ++i) acc = mfma32(pa[i], tr_frag(L, tv0 + i * 8192, tv1 + i * 8192), acc);
    const f32x4 ts = (t[0] + t[1]) + (t[2] + t[3]);
    unsigned ml[2] = {nosent(__float_as_uint(mh)), nosent(__float_as_uint((g & 1) ? ts[1] + ts[3] : ts[0] + ts[2]))};
    int off[1] = {WS_PA + (kvh + 8 * (8 * (w >> 1) + (lane >> 3))) * 2048 + (2 * ((i32 >> 1) & 3) + (w & 1)) * 256 + (i32 >> 3) * 128 + (lane & 7) * 16};
    int ob[2], wb0[2], wb1[2];
#pragma unroll
    for (int i = 0; i < 2; ++i) {
      ob[i] = (n >> 2) == i ? L_O + (n & 3) * 320 + g * 16 : L_ZERO;
      const int x = ((((4 * w + 2 * i) & 15) ^ (2 * w)) << 4);
      wb0[i] = L_K + (w >> 2) * 8192 + (tr0 ^ x); wb1[i] = L_K + (w >> 2) * 8192 + (tr1 ^ x);
      PIN(ob[i]); PIN(wb0[i]); PIN(wb1[i]);
    }
    int ow = L_O + (w >> 1) * 320 + (32 * (w & 1) + 4 * (n & 7) + (((g & 1) << 1) | (g >> 1))) * 2;
    int so = lane < 32 ? WS_HP + (kvh * 8 + (i32 >> 1)) * 8192 + ((i32 & 1) * 256 + 32 * w + lane) * 16 : OOB;
    PIN(off[0]); PIN(ow); PIN(so);
    int pao = (n & 3) == 0 ? WS_PA + c * 2048 + w * 256 + g * 64 + (n >> 2) * 16 : OOB;
    PIN(ml[0]); PIN(ml[1]); PIN(pao); PIN(acc); PIN(pa[6]); PIN(pa[7]);
    STAMP(15);
    asm volatile("s_waitcnt vmcnt(4)" : "+v"(rdy) :: "memory");
    if (chk) {
#pragma unroll 1
      for (int it = 0; it < SPIN_CAP; ++it) {
        if (__all((rdy[0] == tlo) & (rdy[1] == thi) & (rdy[2] == tlo) & (rdy[3] == thi))) break;
        ld16_sc1_rw(rdy, roff, srd_ws);
        vmwait_pin<0>(rdy);
      }
    }
    STAMP(5);
    BARRIER();
#pragma unroll
    for (int i = 6; i < 8; ++i) acc = mfma32(pa[i], tr_frag(L, tv0 + i * 8192, tv1 + i * 8192), acc);
    const unsigned d0 = nosent(pk2(acc[0], dpp<SHL1>(acc[0])));
    st_sc1(rs, pao, v4u{d0, dppu<SHL2>(d0), ml[0], ml[1]});
  STAMP(6);

  if (__builtin_expect(is_wo, 1)) {
    v4u pv[1];
    STAMP(7);
    poll_fresh<1, DEPTH_A>(srd_ws, off, pv);
    STAMP(8);
    const float m0 = __uint_as_float(pv[0][2]);
    float mm = vmax(m0, dpp<ROR8>(m0));
    mm = x32_max(x16_max(mm));
    const float e0 = __builtin_amdgcn_exp2f((m0 - mm) * LOG2E);
    float lw = e0 * __uint_as_float(pv[0][3]);
    const auto s01 = __builtin_amdgcn_permlane32_swap(__float_as_uint(e0 * bflo(pv[0][0])), __float_as_uint(e0 * bfhi(pv[0][0])), false, false);
    const auto s23 = __builtin_amdgcn_permlane32_swap(__float_as_uint(e0 * bflo(pv[0][1])), __float_as_uint(e0 * bfhi(pv[0][1])), false, false);
    const float u0 = __uint_as_float(s01[0]) + __uint_as_float(s01[1]), u1 = __uint_as_float(s23[0]) + __uint_as_float(s23[1]);
    const auto su = __builtin_amdgcn_permlane16_swap(__float_as_uint(u0), __float_as_uint(u1), false, false);
    float ov = __uint_as_float(su[0]) + __uint_as_float(su[1]);
    ov += dpp<ROR8>(ov);
    lw += dpp<ROR8>(lw);
    lw = x32_sum(x16_sum(lw));
    *(LAS u16*)(L + ow) = (u16)pk2(ov * __builtin_amdgcn_rcpf(lw), 0.f);
    BARRIER();
    v4u oa[4], bf[4];
#pragma unroll
    for (int i = 0; i < 4; ++i) {
      oa[i] = *(LAS v4u*)(L + ob[i & 1] + (i >> 1) * 64);
      bf[i] = tr_frag(L, wb0[i & 1] + (i >> 1) * 16384, wb1[i & 1] + (i >> 1) * 16384);
    }
    SCHED();
    f32x4 hacc = {0.f, 0.f, 0.f, 0.f};
#pragma unroll
    for (int i = 0; i < 4; ++i) hacc = mfma32(oa[i], bf[i], hacc);
    st_sc1(rs, so, nosent4(hacc));
    SCHED();
    STAMP(9);
    poll_drain<1>(pv);
    EXIT();
  }
  if (is_ffn) {
    const u16* gup = ((lane >> 2) & 1 ? wu : wg) + (size_t)(64 * w + (lane >> 3)) * FF + 32 * jf + 8 * ((lane & 3) ^ (g & 1));
    const int alt = (lane & 2) ? -16 : 16;
    const v4u srd_d = make_srd(wd + (size_t)(32 * jf + 4 * w) * DM, 4 * DM * 2);
#define DOWNLOAD() do { _Pragma("unroll") for (int i = 0; i < 4; ++i) dma_buf<W2_NT>(ldst + L_K + i * 8192, vv0 + g * (DM * 2 - 256), srd_d, i * 256); } while (0)
    STAMP(20);
    BARRIER();
    STAMP(22);
#pragma unroll
    for (int i = 0; i < 8; ++i) dma_glb<GU_NT>(lbase + L_V + w * 8192 + i * 1024, gup + i * 8 * FF + ((i & 1) ? alt : 0));
    DOWNLOAD();
    STAMP(10);
    *(LAS v2u*)(L + L_P + w * 128 + (lane & 15) * 8) = v2u{0x3F803F80u, 0x3F803F80u};
    v4u bd[4];
    int ao[4], so2 = WS_OP + jf * 8192 + w * 1024 + lane * 16;
#pragma unroll
    for (int i = 0; i < 4; ++i) { ao[i] = (n >> 2) == i ? L_HH + (n & 3) * 64 + g * 16 : L_ZERO; PIN(ao[i]); }
    int e0 = L_K + (w >> 1) * 8192 + (tr0 ^ (((8 * (w & 1)) ^ (2 * w)) << 4)), e1 = L_K + (w >> 1) * 8192 + (tr1 ^ (((8 * (w & 1)) ^ (2 * w)) << 4));
    PIN(so2); PIN(e0); PIN(e1);
#define DOWN() do { BARRIER(); v4u a4[4]; \
      _Pragma("unroll") for (int i = 0; i < 4; ++i) a4[i] = *(LAS v4u*)(L + ao[i]); \
      SCHED(); f32x4 d = {0.f, 0.f, 0.f, 0.f}; \
      _Pragma("unroll") for (int i = 0; i < 4; ++i) d = mfma32(a4[i], bd[i], d); \
      st_sc1(rs, so2, nosent4(d)); SCHED(); STAMP(9); } while (0)
#define BDLOAD() do { _Pragma("unroll") for (int i = 0; i < 4; ++i) bd[i] = tr_frag(L, e0 ^ (i << 5), e1 ^ (i << 5)); \
      PIN(bd[0]); PIN(bd[1]); PIN(bd[2]); PIN(bd[3]); } while (0)
    if (w == 4) {
      v4u hv[4];
      int hr = L_HN + (lane & 3) * 1088 + (lane >> 2) * 16, off1[4];
#pragma unroll
      for (int i = 0; i < 4; ++i) { off1[i] = WS_H + kvh * 8192 + i * 2048 + (lane >> 2) * 128 + (lane & 3) * 16; PIN(off1[i]); }
      PIN(hr);
      wait_ready(rs, lane, tlo, thi);
      VMWAIT(0);
      STAMP(11);
      BARRIER();
      STAMP(7);
      poll_fresh<4, DEPTH_H>(srd_ws, off1, hv);
      STAMP(8);
#pragma unroll
      for (int i = 0; i < 4; ++i) *(LAS v4u*)(L + hr + i * 256) = hv[i];
      BARRIER();
      float s0 = 0.f, s1 = 0.f, q0 = 0.f, q1 = 0.f;
#pragma unroll
      for (int i = 0; i < 4; ++i)
#pragma unroll
        for (int k = 0; k < 4; k += 2) {
          s0 = dot2(hv[i][k], 0x3F803F80u, s0); s1 = dot2(hv[i][k + 1], 0x3F803F80u, s1);
          q0 = dot2(hv[i][k], hv[i][k], q0); q1 = dot2(hv[i][k + 1], hv[i][k + 1], q1);
        }
      s0 += s1; q0 += q1;
      s0 += dpp<ROR4>(s0); q0 += dpp<ROR4>(q0);
      s0 += dpp<ROR8>(s0); q0 += dpp<ROR8>(q0);
      const float mean = x32_sum(x16_sum(s0)) * (1.0f / DM);
      const float q = fmaxf(fmaf(-mean * DM, mean, x32_sum(x16_sum(q0))), 0.f);
      const float inv = __builtin_amdgcn_rcpf(rb(__builtin_amdgcn_sqrtf(rb(rb(q * (1.0f / DM)) + 1e-5f))));
      *(LAS f32x2*)(L + L_RM + (lane & 3) * 8) = f32x2{rb(mean), inv};
      STAMP(27);
      BARRIER();
      BDLOAD();
      DOWN();
      poll_drain<4>(hv);
      EXIT();
    }
    VMWAIT(0);
    STAMP(11);
    BARRIER();
    if (w > 4) {
      BDLOAD();
      BARRIER();
      BARRIER();
      DOWN();
      EXIT();
    }
    {
      int uo = L_V + (8 * g + tq) * 128 + ((4 * (tp >> 1) + (w ^ (((tq >> 1) & 1) | ((g & 1) << 1)))) * 16) + 8 * (tp & 1);
      PIN(uo);
      v4u bf[16];
#pragma unroll
      for (int i = 0; i < 16; ++i) bf[i] = tr_frag(L, uo + i * 4096, uo + i * 4096 + 512);
      BDLOAD();
      int hb = (n & 3) == 1 ? L_P : L_HN + (n >> 2) * 1088 + g * 16, hw = n < 8 ? L_HH + g * 64 + (8 * w + n) * 2 : L_JUNK;
      PIN(hb); PIN(hw);
#pragma unroll
      for (int i = 0; i < 16; ++i) PIN(bf[i]);
      STAMP(25);
      BARRIER();
      STAMP(26);
      v4u ha[8];
#pragma unroll
      for (int i = 0; i < 8; ++i) ha[i] = *(LAS v4u*)(L + hb + i * 64);
      SCHED();
      f32x4 ac[2] = {{0.f, 0.f, 0.f, 0.f}, {0.f, 0.f, 0.f, 0.f}};
#pragma unroll
      for (int i = 0; i < 8; ++i) { ac[i & 1] = mfma32(ha[i], bf[i], ac[i & 1]); ha[i] = *(LAS v4u*)(L + hb + (8 + i) * 64); SCHED(); }
#pragma unroll
      for (int i = 0; i < 8; ++i) ac[i & 1] = mfma32(ha[i], bf[8 + i], ac[i & 1]);
      float acc0 = ac[0][0] + ac[1][0], cs = ac[0][1] + ac[1][1];
      PIN(acc0); PIN(cs);
      STAMP(28);
      BARRIER();
      const f32x2 st = *(LAS f32x2*)(L + L_RM + g * 8);
      const float a = rb(fmaf(-st[0], cs, acc0) * st[1]), u = dpp<ROR8>(a);
      const float sg = rb(__builtin_amdgcn_rcpf(1.0f + __builtin_amdgcn_exp2f(a * -LOG2E)));
      *(LAS u16*)(L + hw) = (u16)pk2(sg * u, 0.f);
      STAMP(29);
      DOWN();
      EXIT();
    }
  }
  if (is_red) {
    const int r2 = lane >> 3, k = lane & 7, bb = r2 & 3;
    const float xv = bflo(x[bb * DM + 8 * rho + k]);
    const unsigned pre = out[0];
    int off1[1] = {WS_HP + (8 * w + r2) * 8192 + (8 * rho + k) * 16}, off2[1] = {w < 7 ? WS_OP + (8 * w + r2) * 8192 + (8 * rho + k) * 16 : OOB};
    int ho = (w == 0 && lane < 32) ? WS_H + k * 8192 + rho * 128 + bb * 16 : OOB;
    int oo = (w == 0 && k == 0 && lane < 32) ? (bb * DM + 8 * rho) * 2 : OOB;
    int rw_ = L_P + (((((g & 1) << 1) | (g >> 1)) * 8 + k) * 8 + w) * 4, rr_ = L_P + (bb * 8 + k) * 32;
    PIN(ho); PIN(oo); PIN(rw_); PIN(rr_); PIN(off1[0]); PIN(off2[0]);
    asm volatile("" :: "v"(pre));
    if (w == 0) wait_ready(rs, lane, tlo, thi);
    BARRIER();
    STAMP(7);
#define PARTIAL_SUM(PV, AREA, RES) do { \
      const auto s01 = __builtin_amdgcn_permlane32_swap(PV[0], PV[1], false, false); \
      const auto s23 = __builtin_amdgcn_permlane32_swap(PV[2], PV[3], false, false); \
      const float u0 = __uint_as_float(s01[0]) + __uint_as_float(s01[1]), u1 = __uint_as_float(s23[0]) + __uint_as_float(s23[1]); \
      const auto su = __builtin_amdgcn_permlane16_swap(__float_as_uint(u0), __float_as_uint(u1), false, false); \
      float t_ = __uint_as_float(su[0]) + __uint_as_float(su[1]); \
      t_ += dpp<ROR8>(t_); \
      *(LAS float*)(L + rw_ + (AREA)) = t_; \
      BARRIER(); \
      const f32x4 ra = *(LAS f32x4*)(L + rr_ + (AREA)), rb_ = *(LAS f32x4*)(L + rr_ + (AREA) + 16); \
      RES = ((ra[0] + ra[1]) + (ra[2] + ra[3])) + ((rb_[0] + rb_[1]) + (rb_[2] + rb_[3])); } while (0)
    v4u pv[1], pw[1];
    poll_fresh<1, DEPTH_R>(srd_ws, off1, pv);
    STAMP(8);
    float av;
    PARTIAL_SUM(pv[0], 0, av);
    const float hval = rb(xv + rb(av));
    {
      const unsigned e0 = nosent(pk2(dpp<0xA0>(hval), dpp<0xF5>(hval)));
      const unsigned qa = dppu<0x00>(e0), qb2 = dppu<0xAA>(e0);
      const unsigned oa = dppu<0x141>(qa), ob2 = dppu<0x141>(qb2);
      const bool hi = k & 4;
      st_sc1(rs, ho, v4u{hi ? oa : qa, hi ? ob2 : qb2, hi ? qa : oa, hi ? qb2 : ob2});
    }
    SCHED();
    STAMP(9);
    poll_drain<1>(pv);
    __builtin_amdgcn_s_sleep(C_DELAY);
    poll_fresh<1, DEPTH_R>(srd_ws, off2, pw);
    STAMP(10);
    PARTIAL_SUM(pw[0], 1024, av);
    {
      const float ov = hval + rb(av);
      const unsigned dw = pk2(ov, dpp<SHL1>(ov));
      const unsigned dw2 = dppu<SHL2>(dw);
      const unsigned dw4 = dppu<SHL4>(dw), dw6 = dppu<SHL4>(dw2);
      __builtin_amdgcn_raw_buffer_store_b128(v4u{dw, dw2, dw4, dw6}, __builtin_amdgcn_make_buffer_rsrc(out, 0, 4 * DM * 2, 0x00020000), oo, 0, 0);
      st8_sc1_asm(v2u{tlo, thi}, (c == 192 && w == 0 && lane == 0) ? WS_EPOCH : OOB, srd_ws);
    }
    SCHED();
    STAMP(11);
    poll_drain<1>(pw);
  }
  STAMP_OUT();
}

}

extern "C" void kernel_launch(void* const* d_in, const int* in_sizes, int n_in, void* d_out, int out_size, void* d_ws, size_t ws_size,
                              hipStream_t stream) {
  (void)in_sizes; (void)n_in; (void)out_size; (void)ws_size;
  fused_block<<<256, NTHR, 0, stream>>>((const u16*)d_in[0], (const u16*)d_in[1], (const u16*)d_in[2], (const u16*)d_in[3], (const u16*)d_in[4],
                                        (const u16*)d_in[6], (const u16*)d_in[7], (const u16*)d_in[8], (u16*)d_out, (unsigned char*)d_ws);
}
```

```cpp
#include <hip/hip_runtime.h>
#include <hip/hip_bf16.h>
#include <stdint.h>

namespace {

typedef unsigned short u16;
typedef unsigned long long u64;
typedef unsigned v4u __attribute__((ext_vector_type(4)));
typedef unsigned v2u __attribute__((ext_vector_type(2)));
typedef float f32x4 __attribute__((ext_vector_type(4)));
typedef float f32x2 __attribute__((ext_vector_type(2)));
typedef short bf16x8 __attribute__((ext_vector_type(8)));
typedef short s16x4 __attribute__((ext_vector_type(4)));
typedef __bf16 bf2 __attribute__((ext_vector_type(2)));
#define LAS __attribute__((address_space(3)))
#define GAS __attribute__((address_space(1)))

constexpr int DM = 512, HKV = 8, SK = 8192, DH = 128, WIN = 2048, FF = 1792, NQ = 4096;
constexpr int NTHR = 640;

constexpr int WS_EPOCH = 0;
constexpr int WS_RDY = 4096;
constexpr int WS_QG = 8192;
constexpr int WS_PA = WS_QG + 512 * 1024;
constexpr int WS_HP = WS_PA + 512 * 1024;
constexpr int WS_H = WS_HP + 512 * 1024;
constexpr int WS_OP = WS_H + 65536;
constexpr int WS_END = WS_OP + 56 * 8192;
constexpr int OOB = 0x40000000;

constexpr int L_K = 0;
constexpr int L_V = 65536;
constexpr int L_WQ = 131072;
constexpr int L_HN = L_WQ;
constexpr int L_Q = L_WQ + 16384;
constexpr int L_XN = L_Q + 1280;
constexpr int L_O = L_XN;
constexpr int L_QP = L_XN + 1280;
constexpr int L_P = L_QP + 1024;
constexpr int L_RM = L_P + 2304;
constexpr int L_RL = L_RM + 128;
constexpr int L_HH = L_RL + 128;
constexpr int L_ZERO = L_HH + 256;
constexpr int L_FLAG = L_ZERO + 128;
constexpr int L_JUNK = L_FLAG + 16;
constexpr int L_BYTES = L_JUNK + 2048;

constexpr unsigned SENT = 0xFFFFFFFFu;
constexpr int SPIN_CAP = 1 << 16;
constexpr int AUX_SC1 = 16;
constexpr float LOG2E = 1.4426950408889634f;

#define POLL_SLEEP 1
#define ABL_NOW 0
#define CODE_BYTES $
#define WQ_POL "nt"
#define QPP 1
#define QKP 5
#define QG_DELAY 30
#define WINDOW 3
#define POLL_DEPTH 3
#define P1_EARLY 1
#define GU_WAIT 1
#define PF_NT 0
#define PF_WIN1 0
#define PF_DELAY 0
#define WO_EARLY 1
#define WO_POS 0
#define DEPTH_A 1
#define DEPTH_H 2
#define DEPTH_R 3
#define C_DELAY 10
#define PF_EARLY 1
#define QG_DEPTH 1
#define QG_GAP 16
#define K0_DELAY 0
#define K1_DELAY 0
#define PRETOUCH 1
#define KV_NT 1
#define WQ_NT 1
#define W2_NT 1
#define GU_NT 1

__device__ __forceinline__ float bflo(unsigned x) { return __builtin_bit_cast(float, x << 16); }
__device__ __forceinline__ float bfhi(unsigned x) { return __builtin_bit_cast(float, x & 0xffff0000u); }
__device__ __forceinline__ unsigned pk2(float lo, float hi) {
  bf2 v = {(__bf16)lo, (__bf16)hi};
  return __builtin_bit_cast(unsigned, v);
}
__device__ __forceinline__ float rb(float x) { return (float)(__bf16)x; }
__device__ __forceinline__ unsigned nosent(unsigned x) { return x < 0xFFFFFFFEu ? x : 0xFFFFFFFEu; }
__device__ __forceinline__ v4u nosent4(f32x4 v) {
  return v4u{nosent(__float_as_uint(v[0])), nosent(__float_as_uint(v[1])), nosent(__float_as_uint(v[2])), nosent(__float_as_uint(v[3]))};
}
__device__ __forceinline__ bool fresh(v4u v) { return max(max(v[0], v[1]), max(v[2], v[3])) != SENT; }
__device__ __forceinline__ float vmax(float a, float b) { float r; asm("v_max_f32 %0, %1, %2" : "=v"(r) : "v"(a), "v"(b)); return r; }

__device__ __forceinline__ float vmax3(float a, float b, float c) { float r; asm("v_max3_f32 %0, %1, %2, %3" : "=v"(r) : "v"(a), "v"(b), "v"(c)); return r; }
__device__ __forceinline__ void row_max2(float& a, float& b) {
  asm("s_nop 1\n\t"
      "v_max_f32_dpp %0, %0, %0 row_ror:8 row_mask:0xf bank_mask:0xf\n\tv_max_f32_dpp %1, %1, %1 row_ror:8 row_mask:0xf bank_mask:0xf\n\ts_nop 0\n\t"
      "v_max_f32_dpp %0, %0, %0 row_ror:4 row_mask:0xf bank_mask:0xf\n\tv_max_f32_dpp %1, %1, %1 row_ror:4 row_mask:0xf bank_mask:0xf\n\ts_nop 0\n\t"
      "v_max_f32_dpp %0, %0, %0 row_ror:2 row_mask:0xf bank_mask:0xf\n\tv_max_f32_dpp %1, %1, %1 row_ror:2 row_mask:0xf bank_mask:0xf\n\ts_nop 0\n\t"
      "v_max_f32_dpp %0, %0, %0 row_ror:1 row_mask:0xf bank_mask:0xf\n\tv_max_f32_dpp %1, %1, %1 row_ror:1 row_mask:0xf bank_mask:0xf\n\ts_nop 1"
      : "+v"(a), "+v"(b));
}

__device__ __forceinline__ float dot2(unsigned a, unsigned b, float c) {
  return __builtin_amdgcn_fdot2_f32_bf16(__builtin_bit_cast(bf2, a), __builtin_bit_cast(bf2, b), c, false);
}
__device__ __forceinline__ f32x4 mfma32(v4u a, v4u b, f32x4 c) {
  return __builtin_amdgcn_mfma_f32_16x16x32_bf16(__builtin_bit_cast(bf16x8, a), __builtin_bit_cast(bf16x8, b), c, 0, 0, 0);
}
template <int CTRL> __device__ __forceinline__ float dpp(float x) {
  return __builtin_bit_cast(float, __builtin_amdgcn_mov_dpp(__builtin_bit_cast(int, x), CTRL, 0xf, 0xf, true));
}
template <int CTRL> __device__ __forceinline__ unsigned dppu(unsigned x) { return (unsigned)__builtin_amdgcn_mov_dpp((int)x, CTRL, 0xf, 0xf, true); }
constexpr int ROR8 = 0x128, ROR4 = 0x124, ROR2 = 0x122, ROR1 = 0x121, SHL1 = 0x101, SHL2 = 0x102, SHL4 = 0x104;
__device__ __forceinline__ float row_max(float x) { x = fmaxf(x, dpp<ROR8>(x)); x = fmaxf(x, dpp<ROR4>(x)); x = fmaxf(x, dpp<ROR2>(x)); return fmaxf(x, dpp<ROR1>(x)); }
__device__ __forceinline__ float row_sum(float x) { x += dpp<ROR8>(x); x += dpp<ROR4>(x); x += dpp<ROR2>(x); return x + dpp<ROR1>(x); }
__device__ __forceinline__ float x16_max(float x) { auto s = __builtin_amdgcn_permlane16_swap(__float_as_uint(x), __float_as_uint(x), false, false); return vmax(__uint_as_float(s[0]), __uint_as_float(s[1])); }
__device__ __forceinline__ float x32_max(float x) { auto s = __builtin_amdgcn_permlane32_swap(__float_as_uint(x), __float_as_uint(x), false, false); return vmax(__uint_as_float(s[0]), __uint_as_float(s[1])); }
__device__ __forceinline__ float x16_sum(float x) { auto s = __builtin_amdgcn_permlane16_swap(__float_as_uint(x), __float_as_uint(x), false, false); return __uint_as_float(s[0]) + __uint_as_float(s[1]); }
__device__ __forceinline__ float x32_sum(float x) { auto s = __builtin_amdgcn_permlane32_swap(__float_as_uint(x), __float_as_uint(x), false, false); return __uint_as_float(s[0]) + __uint_as_float(s[1]); }
__device__ __forceinline__ float wave_sum(float x) { return x32_sum(x16_sum(row_sum(x))); }
__device__ __forceinline__ f32x4 sel2(bool c, f32x4 a, f32x4 b) {
  return f32x4{c ? b[0] : a[0], c ? b[1] : a[1], c ? b[2] : a[2], c ? b[3] : a[3]};
}
__device__ __forceinline__ f32x4 sel4(int g, f32x4 a, f32x4 b, f32x4 c, f32x4 d) { return sel2(g & 2, sel2(g & 1, a, b), sel2(g & 1, c, d)); }

#define PIN(x) asm volatile("" : "+v"(x))
#define SCHED() __builtin_amdgcn_sched_barrier(0)
#define LDS_FENCE() asm volatile("s_waitcnt lgkmcnt(0)" ::: "memory")
#define BARRIER() do { asm volatile("s_waitcnt lgkmcnt(0)\n\ts_barrier" ::: "memory"); } while (0)
#define VMWAIT(N) asm volatile("s_waitcnt vmcnt(" #N ")" ::: "memory")
#define STAMP(i) do {} while (0)
#define STAMP_OUT() do {} while (0)

#define EXIT() do { STAMP_OUT(); asm volatile("s_endpgm" ::: "memory"); __builtin_unreachable(); } while (0)

__device__ __forceinline__ v4u make_srd(const void* p, unsigned bytes) {
  const u64 a = (u64)p;
  return v4u{(unsigned)a, (unsigned)(a >> 32) & 0xffffu, bytes, 0x00020000u};
}
template <int NT> __device__ __forceinline__ void dma_buf(unsigned lds, int voff, v4u srd, int soff) {
  if (NT) asm volatile("s_mov_b32 m0, %0\n\ts_nop 0\n\tbuffer_load_dwordx4 %1, %2, %3 offen nt lds" :: "s"(lds), "v"(voff), "s"(srd), "s"(soff) : "m0", "memory");
  else asm volatile("s_mov_b32 m0, %0\n\ts_nop 0\n\tbuffer_load_dwordx4 %1, %2, %3 offen lds" :: "s"(lds), "v"(voff), "s"(srd), "s"(soff) : "m0", "memory");
}
template <int NT> __device__ __forceinline__ void dma_glb(unsigned lds, const void* p) {
  if (NT) asm volatile("s_mov_b32 m0, %0\n\ts_nop 0\n\tglobal_load_lds_dwordx4 %1, off nt" :: "s"(lds), "v"(p) : "m0", "memory");
  else asm volatile("s_mov_b32 m0, %0\n\ts_nop 0\n\tglobal_load_lds_dwordx4 %1, off" :: "s"(lds), "v"(p) : "m0", "memory");
}
__device__ __forceinline__ void ld16_asm(v4u& d, int voff, v4u srd, int soff) {
  asm volatile("buffer_load_dwordx4 %0, %1, %2, %3 offen" : "=v"(d) : "v"(voff), "s"(srd), "s"(soff) : "memory");
}
__device__ __forceinline__ void ld16_sc1_asm(v4u& d, int voff, v4u srd) {
  asm volatile("buffer_load_dwordx4 %0, %1, %2, 0 offen sc1" : "=v"(d) : "v"(voff), "s"(srd) : "memory");
}
__device__ __forceinline__ void ld8_sc1_asm(v2u& d, v4u srd) {
  asm volatile("buffer_load_dwordx2 %0, off, %1, 0 sc1" : "=v"(d) : "s"(srd) : "memory");
}
__device__ __forceinline__ void st16_sc1_asm(v4u v, int voff, v4u srd) {
  asm volatile("buffer_store_dwordx4 %0, %1, %2, 0 offen sc1\n\ts_nop 1" :: "v"(v), "v"(voff), "s"(srd) : "memory");
}

__device__ __forceinline__ void st8_sc1_asm(v2u v, int voff, v4u srd) {
  asm volatile("buffer_store_dwordx2 %0, %1, %2, 0 offen sc1" :: "v"(v), "v"(voff), "s"(srd) : "memory");
}
__device__ __forceinline__ float sel4f(int b, f32x4 a) { const float lo = (b & 1) ? a[1] : a[0], hi = (b & 1) ? a[3] : a[2]; float r = (b & 2) ? hi : lo; return r; }

__device__ __forceinline__ v4u ld_sc1(__amdgpu_buffer_rsrc_t rs, int off) { return __builtin_amdgcn_raw_buffer_load_b128(rs, off, 0, AUX_SC1); }
__device__ __forceinline__ void st_sc1(__amdgpu_buffer_rsrc_t rs, int off, v4u v) { __builtin_amdgcn_raw_buffer_store_b128(v, rs, off, 0, AUX_SC1); }
__device__ __forceinline__ v2u tr_rd(LAS unsigned char* L, int off) {
  return __builtin_bit_cast(v2u, __builtin_amdgcn_ds_read_tr16_b64_v4i16((LAS s16x4*)(L + off)));
}
__device__ __forceinline__ v4u tr_frag(LAS unsigned char* L, int off0, int off1) {
  const v2u a = tr_rd(L, off0), b = tr_rd(L, off1);
  return v4u{a[0], a[1], b[0], b[1]};
}

__device__ __forceinline__ void ld16_sc1_rw(v4u& d, int voff, v4u srd) {
  asm volatile("buffer_load_dwordx4 %0, %1, %2, 0 offen sc1" : "+v"(d) : "v"(voff), "s"(srd) : "memory");
}
template <int CNT> __device__ __forceinline__ void vmwait_pin(v4u& a) { asm volatile("s_waitcnt vmcnt(%1)" : "+v"(a) : "n"(CNT) : "memory"); }
template <int N, int D> __device__ __forceinline__ void poll_start(v4u srd, const int (&off)[N], v4u (&v)[N]) {
#pragma unroll
  for (int i = 0; i < N; ++i) v[i] = v4u{SENT, SENT, SENT, SENT};
#pragma unroll
  for (int k = 0; k < D - 1; ++k) {
#pragma unroll
    for (int i = 0; i < N; ++i) ld16_sc1_rw(v[i], off[i], srd);
    __builtin_amdgcn_s_sleep(POLL_SLEEP);
  }
}
__device__ __forceinline__ void ld16_sc1_masked(v4u& d, int voff, v4u srd, u64 mask) {
  asm volatile("s_mov_b64 exec, %3\n\tbuffer_load_dwordx4 %0, %1, %2, 0 offen sc1\n\ts_mov_b64 exec, -1" : "+v"(d) : "v"(voff), "s"(srd), "s"(mask) : "memory");
}
template <int N, int D> __device__ __forceinline__ void poll_finish(v4u srd, const int (&off)[N], v4u (&v)[N]) {
  u64 miss[N];
#pragma unroll
  for (int i = 0; i < N; ++i) miss[i] = ~0ull;
#pragma unroll 1
  for (int it = 0; it < SPIN_CAP; ++it) {
#pragma unroll
    for (int i = 0; i < N; ++i) ld16_sc1_masked(v[i], off[i], srd, miss[i]);
    vmwait_pin<(D - 1) * N>(v[0]);
#pragma unroll
    for (int i = 1; i < N; ++i) PIN(v[i]);
    u64 any = 0;
#pragma unroll
    for (int i = 0; i < N; ++i) { miss[i] = __builtin_amdgcn_ballot_w64(!fresh(v[i])); any |= miss[i]; }
    if (any == 0) break;
    __builtin_amdgcn_s_sleep(POLL_SLEEP);
  }
}
template <int N, int D> __device__ __forceinline__ void poll_fresh(v4u srd, const int (&off)[N], v4u (&v)[N]) {
  poll_start<N, D>(srd, off, v);
  poll_finish<N, D>(srd, off, v);
}
template <int N> __device__ __forceinline__ void poll_drain(v4u (&v)[N]) {
  vmwait_pin<0>(v[0]);
#pragma unroll
  for (int i = 1; i < N; ++i) PIN(v[i]);
}
__device__ __forceinline__ void wait_ready(__amdgpu_buffer_rsrc_t rs, int lane, unsigned tlo, unsigned thi) {
#pragma unroll 1
  for (int it = 0; it < SPIN_CAP; ++it) {
    asm volatile("" ::: "memory");
    const v4u r0 = ld_sc1(rs, WS_RDY + lane * 16), r1 = ld_sc1(rs, WS_RDY + 1024 + lane * 16);
    const bool ok = (r0[0] == tlo) & (r0[1] == thi) & (r0[2] == tlo) & (r0[3] == thi) & (r1[0] == tlo) & (r1[1] == thi) & (r1[2] == tlo) & (r1[3] == thi);
    if (__all(ok)) break;
    __builtin_amdgcn_s_sleep(4);
  }
}

__global__ void __launch_bounds__(NTHR, 3)
fused_block(const u16* __restrict__ x, const u16* __restrict__ kc, const u16* __restrict__ vc, const u16* __restrict__ wq,
            const u16* __restrict__ wo, const u16* __restrict__ wg, const u16* __restrict__ wu, const u16* __restrict__ wd,
            u16* __restrict__ out, unsigned char* __restrict__ ws) {
  __shared__ __attribute__((aligned(16))) unsigned char smem_[L_BYTES];
  {
    u64 pc0;
    asm volatile("s_getpc_b64 %0" : "=s"(pc0));
    if (__builtin_amdgcn_readfirstlane(threadIdx.x >> 6) == 1) {
      const v4u srd_c = make_srd((const void*)(pc0 & ~255ull), CODE_BYTES);
      const int so = ((blockIdx.x >> 3) & 15) * 2048;
      asm volatile("s_mov_b32 m0, %0\n\ts_nop 0\n\tbuffer_load_dwordx4 %1, %2, %3 offen lds\n\tbuffer_load_dwordx4 %1, %2, %3 offen offset:1024 lds"
                   :: "s"((unsigned)(size_t)(LAS unsigned char*)smem_ + L_JUNK), "v"((threadIdx.x & 63) * 16), "s"(srd_c), "s"(so) : "m0", "memory");
    }
    __builtin_amdgcn_sched_barrier(0);
  }
  LAS unsigned char* L = (LAS unsigned char*)smem_;
  const unsigned lbase = (unsigned)(size_t)L;
  const int c = blockIdx.x, kvh = c & 7, i32 = c >> 3;
  const int lane = threadIdx.x & 63, wv = __builtin_amdgcn_readfirstlane(threadIdx.x >> 6);
  const int w = wv < 2 ? 8 + wv : wv - 2;
  const int n = lane & 15, g = lane >> 4;
  const v4u srd_ws = make_srd(ws, WS_END);
  STAMP(0);

  const int b = i32 >> 3, s = i32 & 7;
  const size_t kvbase = ((size_t)(b * HKV + kvh) * SK + (SK - WIN) + s * 256) * DH;
  const v4u srd_k = make_srd(kc + kvbase, 65536);
  const int rot = w + 37 * c;
  const int kv0 = g * 256 + ((n ^ g) * 16);
  const unsigned ldst = lbase + w * 1024;
  v2u ep;
  v4u xr[4];
#define KLOAD(i) dma_buf<KV_NT>(ldst + L_K + (i) * 8192, kv0 ^ (((i) & 3) << 6), srd_k, ((8 * (i) + rot) & 63) << 10)
  if (wv == 1 && (s == 3 || s == 5)) {
    const u16* p = (s == 3 ? kc : vc) + kvbase;
    asm volatile("s_mov_b32 m0, %0\n\ts_nop 0\n\tglobal_load_lds_dword %1, %2" :: "s"(lbase + L_JUNK), "v"(0), "s"(p) : "m0", "memory");
  }
  if (wv == 0) {
    const v4u srd_x = make_srd(x, 4 * DM * 2);
    const v4u srd_wq = make_srd(wq + (size_t)b * 128 * NQ + kvh * 512 + s * 64, 128 * NQ * 2);
    ld8_sc1_asm(ep, srd_ws);
#pragma unroll
    for (int i = 0; i < 4; ++i) ld16_asm(xr[i], g * 1024 + n * 16, srd_x, ((b + i) & 3) * 256);
    const int row = lane >> 3, v0 = row * (NQ * 2) + (((lane & 7) ^ (2 * ((row >> 1) & 1))) * 16);
    int so;
#define WQ2 "buffer_load_dwordx4 %1, %3, %0 offen " WQ_POL " lds\n\ts_addk_i32 m0, 0x400\n\ts_add_u32 %0, %0, %5\n\t" \
            "buffer_load_dwordx4 %2, %3, %0 offen " WQ_POL " lds\n\ts_addk_i32 m0, 0x400\n\ts_add_u32 %0, %0, %5\n\t"
    asm volatile("s_mov_b32 m0, %4\n\ts_mov_b32 %0, 0\n\t" WQ2 WQ2 WQ2 WQ2 WQ2 WQ2 WQ2 WQ2
                 : "=&s"(so) : "v"(v0), "v"(v0 ^ 64), "s"(srd_wq), "s"(lbase + L_WQ), "s"(8 * NQ * 2) : "m0", "scc", "memory");
    *(LAS unsigned*)(L + L_FLAG) = 0u;
    LDS_FENCE();
  }
  asm volatile("s_barrier" ::: "memory");
  if (wv >= 2) { __builtin_amdgcn_s_sleep(K0_DELAY); KLOAD(0); __builtin_amdgcn_s_sleep(K1_DELAY); KLOAD(1); KLOAD(2); }
  int wv2 = wv;
  asm volatile("" : "+s"(wv2));

  if (wv2 == 1) {
    v2u ep1;
    ld8_sc1_asm(ep1, srd_ws);
    const v4u sent = {SENT, SENT, SENT, SENT};
    st16_sc1_asm(sent, WS_PA + c * 2048 + lane * 16, srd_ws);
    st16_sc1_asm(sent, WS_PA + c * 2048 + 1024 + lane * 16, srd_ws);
    const int base = i32 < 16 ? WS_HP + (kvh * 8 + (i32 >> 1)) * 8192 + (i32 & 1) * 4096 : WS_OP + (kvh * 7 + i32 - 16) * 8192;
    const int len = i32 < 16 ? 4096 : i32 < 23 ? 8192 : 0;
#pragma unroll 1
    for (int o = 0; o < len; o += 1024) st16_sc1_asm(sent, base + o + lane * 16, srd_ws);
    st16_sc1_asm(sent, (i32 >= 24 && lane < 32) ? WS_H + (lane >> 2) * 8192 + ((i32 - 24) * 8 + kvh) * 128 + (lane & 3) * 16 : OOB, srd_ws);
    *(LAS v4u*)(L + L_ZERO + (lane & 7) * 16) = v4u{0u, 0u, 0u, 0u};
    asm volatile("s_waitcnt vmcnt(0)" : "+v"(ep1) :: "memory");
    const u64 tag = (((u64)ep1[1] << 32) | ep1[0]) + 0x9E3779B97F4A7C15ull;
    st8_sc1_asm(v2u{(unsigned)tag, (unsigned)(tag >> 32)}, lane == 0 ? WS_RDY + (kvh * 32 + i32) * 8 : OOB, srd_ws);
    STAMP(1);
    EXIT();
  }
  if (wv2 == 0) {
    const int rq = b, cq = s;
    STAMP(7);
    asm volatile("s_waitcnt vmcnt(16)" : "+v"(ep), "+v"(xr[0]), "+v"(xr[1]), "+v"(xr[2]), "+v"(xr[3]) :: "memory");
    STAMP(9);
    {
      float s0 = 0.f, s1 = 0.f, q0 = 0.f, q1 = 0.f;
#pragma unroll
      for (int i = 0; i < 4; ++i)
#pragma unroll
        for (int k = 0; k < 4; k += 2) {
          s0 = dot2(xr[i][k], 0x3F803F80u, s0); s1 = dot2(xr[i][k + 1], 0x3F803F80u, s1);
          q0 = dot2(xr[i][k], xr[i][k], q0); q1 = dot2(xr[i][k + 1], xr[i][k + 1], q1);
        }
      const float mean = row_sum(s0 + s1) * (1.0f / DM);
      const float q = fmaxf(fmaf(-mean * DM, mean, row_sum(q0 + q1)), 0.f);
      float v[8];
#pragma unroll
      for (int k = 0; k < 4; ++k) { v[2 * k] = bflo(xr[0][k]); v[2 * k + 1] = bfhi(xr[0][k]); }
      const float mb = rb(mean);
      const float inv = __builtin_amdgcn_rcpf(rb(__builtin_amdgcn_sqrtf(rb(rb(q * (1.0f / DM)) + 1e-5f))));
      v4u o;
#pragma unroll
      for (int k = 0; k < 4; ++k) o[k] = pk2(rb(v[2 * k] - mb) * inv, rb(v[2 * k + 1] - mb) * inv);
      *(LAS v4u*)(L + L_XN + g * 320 + n * 16) = o;
    }
    STAMP(1);
    LDS_FENCE();
    f32x4 acc[4];
    {
      v4u xa[4];
#pragma unroll
      for (int ks = 0; ks < 4; ++ks) xa[ks] = *(LAS v4u*)(L + L_XN + (n & 3) * 320 + ks * 64 + g * 16);
      const int q4 = n >> 2, p = n & 3, y = ((q4 >> 1) & 1) | ((g & 1) << 1);
      const int rbase = L_WQ + (8 * g + q4) * 128 + 8 * (p & 1);
      int o[4];
#pragma unroll
      for (int t = 0; t < 4; ++t) { o[t] = rbase + (((2 * t + (p >> 1)) ^ (2 * y)) * 16); acc[t] = f32x4{0.f, 0.f, 0.f, 0.f}; }
#pragma unroll
      for (int ks = 0; ks < 4; ++ks) {
        if (ks == 0) VMWAIT(12); else if (ks == 1) VMWAIT(8); else if (ks == 2) VMWAIT(4); else VMWAIT(0);
        if (ks == 3) STAMP(4);
#pragma unroll
        for (int t = 0; t < 4; ++t) acc[t] = mfma32(xa[ks], tr_frag(L, o[t] + ks * 4096, o[t] + ks * 4096 + 512), acc[t]);
      }
    }
    *(LAS f32x4*)(L + L_QP + lane * 16) = sel4(g, acc[0], acc[1], acc[2], acc[3]);
    LDS_FENCE();
    const f32x4 q0 = *(LAS f32x4*)(L + L_QP + (lane & 31) * 32), q1 = *(LAS f32x4*)(L + L_QP + (lane & 31) * 32 + 16);
    const u64 tag = (((u64)ep[1] << 32) | ep[0]) + 0x9E3779B97F4A7C15ull;
    const unsigned tlo = (unsigned)tag, thi = (unsigned)(tag >> 32);
    STAMP(5);
    STAMP(2);
    {
      const bool up = lane >= 32;
      const int o = WS_QG + ((kvh * 4 + (up ? 1 : 0)) * 4 + rq) * 4096 + (32 * cq + (lane & 31)) * 16;
      st16_sc1_asm(v4u{__float_as_uint(up ? q0[1] : q0[0]), __float_as_uint(up ? q1[1] : q1[0]), tlo, thi}, o, srd_ws);
      st16_sc1_asm(v4u{__float_as_uint(up ? q0[3] : q0[2]), __float_as_uint(up ? q1[3] : q1[2]), tlo, thi}, o + 2 * 4 * 4096, srd_ws);
    }
    STAMP(3);
    __builtin_amdgcn_s_sleep(QG_DELAY);
    const int qo = WS_QG + (kvh * 4 + b) * 16384 + lane * 16;
    v4u qg[16];
    u64 miss[16];
#pragma unroll
    for (int i = 0; i < 16; ++i) { asm volatile("" : "=v"(qg[i])); miss[i] = ~0ull; }
#define QPASS() do { _Pragma("unroll") for (int i = 0; i < 16; ++i) \
      asm volatile("s_mov_b64 exec, %4\n\tbuffer_load_dwordx4 %0, %1, %2, %3 offen offset:%5 sc1\n\ts_mov_b64 exec, -1" \
                   : "+v"(qg[i]) : "v"(qo), "s"(srd_ws), "s"((i >> 2) * 4096), "s"(miss[i]), "n"((i & 3) * 1024) : "memory"); } while (0)
#pragma unroll 1
    for (int it = 0; it < SPIN_CAP; ++it) {
      QPASS();
      vmwait_pin<16 * (QG_DEPTH - 1)>(qg[0]);
#pragma unroll
      for (int i = 1; i < 16; ++i) PIN(qg[i]);
      u64 any = 0;
#pragma unroll
      for (int i = 0; i < 16; ++i) { miss[i] = __builtin_amdgcn_ballot_w64((((u64)qg[i][3] << 32) | qg[i][2]) != tag); any |= miss[i]; }
      if (any == 0) break;
    }
#pragma unroll
    for (int u = 0; u < 4; ++u) {
      const float a = (__uint_as_float(qg[u][0]) + __uint_as_float(qg[4 + u][0])) + (__uint_as_float(qg[8 + u][0]) + __uint_as_float(qg[12 + u][0]));
      const float bq = (__uint_as_float(qg[u][1]) + __uint_as_float(qg[4 + u][1])) + (__uint_as_float(qg[8 + u][1]) + __uint_as_float(qg[12 + u][1]));
      *(LAS unsigned*)(L + L_Q + u * 320 + lane * 4) = pk2(a, bq);
    }
    *(LAS v2u*)(L + L_FLAG + 8) = v2u{tlo, thi};
    LDS_FENCE();
    *(volatile LAS unsigned*)(L + L_FLAG) = 1u;
    poll_drain<16>(qg);
    STAMP(6);
    EXIT();
  }

  const bool is_wo = i32 < 16, is_ffn = i32 >= 16 && i32 < 23, is_red = i32 >= 24;
  const int jf = kvh * 7 + (i32 - 16);
  const int rho = (i32 - 24) * 8 + kvh;
  const __amdgpu_buffer_rsrc_t rs = __builtin_amdgcn_make_buffer_rsrc(ws, 0, WS_END, 0x00020000);
  const v4u srd_v = make_srd(vc + kvbase, 65536);
  const int vv0 = g * 256 + ((n ^ ((g << 2) | (w & 3))) * 16);
  asm volatile("" :: "s"(wo), "s"(wg), "s"(wu), "s"(wd), "s"(out), "s"(x));
#define VLOAD(i) dma_buf<KV_NT>(ldst + L_V + (i) * 8192, vv0, srd_v, ((8 * (i) + rot) & 63) << 10)
#define WW() VMWAIT(2)
  WW();
  STAMP(12);
  KLOAD(3);
  WW(); KLOAD(4); WW(); KLOAD(5); WW(); KLOAD(6); WW(); KLOAD(7);
  STAMP(1);
  WW(); VLOAD(0); WW(); VLOAD(1); WW(); VLOAD(2);
  constexpr int VNEXT = 3;
  v4u kf[8];
  {
    const int kb = L_K + (n >> 2) * 8192 + w * 1024 + (n & 3) * 256 + ((g ^ n) * 16);
#pragma unroll
    for (int ks = 0; ks < 4; ++ks) { kf[ks] = *(LAS v4u*)(L + (kb ^ (ks << 6))); kf[4 + ks] = *(LAS v4u*)(L + (kb ^ (ks << 6)) + 32768); }
  }
  int qb = L_Q + (2 * (n >> 3) + (n & 1)) * 320 + g * 16;
  int rmw = L_RM + (g >> 1) * 64 + w * 8, rmr = L_RM + (g >> 1) * 64;
  int po = L_P + (g >> 1) * 1152 + ((4 * (g & 1) + (n >> 2)) * 32 + 4 * w + (n & 3)) * 2;
  const u16* wp0 = is_wo ? wo + (size_t)(kvh * 512 + (2 * (i32 >> 3) + (w >> 2)) * 128 + 32 * ((i32 >> 1) & 3) + 4 * (w & 3)) * DM + (i32 & 1) * 256 + g * (DM - 128) + (vv0 >> 1)
                         : (lane >= 32 ? wu : wg) + (size_t)(64 * w + (i32 & 7) + (i32 & 8) * 4) * FF + ((224 * kvh) & ~63) + 8 * (lane & 31);
  PIN(wp0);
  const int tq = n >> 2, tp = n & 3;
  int tr0 = (8 * g + tq) * 256 + (((2 * w + (tp >> 1)) ^ ((tq << 2) | ((2 * g) & 3))) * 16) + 8 * (tp & 1);
  int tr1 = (8 * g + 4 + tq) * 256 + (((2 * w + (tp >> 1)) ^ ((tq << 2) | ((2 * g + 1) & 3))) * 16) + 8 * (tp & 1);
  PIN(tr0); PIN(tr1);
  int tv0 = L_V + tr0, tv1 = L_V + tr1, pab = L_P + (n >> 2) * 576 + g * 16;
  PIN(tv0); PIN(tv1); PIN(pab);
  PIN(qb); PIN(rmw); PIN(rmr); PIN(po);
#pragma unroll
  for (int i = 0; i < 8; ++i) PIN(kf[i]);
#pragma unroll
  for (int i = VNEXT; i <= QKP; ++i) { WW(); VLOAD(i); }
  STAMP(13);
#pragma unroll 1
  for (int it = 0; it < (SPIN_CAP << 4); ++it) {
    if (__builtin_amdgcn_readfirstlane(*(volatile LAS unsigned*)(L + L_FLAG)) != 0u) break;
  }
  asm volatile("" ::: "memory");
  const v2u tagv = *(LAS v2u*)(L + L_FLAG + 8);
  const unsigned tlo = tagv[0], thi = tagv[1];
  STAMP(3);
  float sc[2];
  {
    v4u qa[4];
#pragma unroll
    for (int ks = 0; ks < 4; ++ks) qa[ks] = *(LAS v4u*)(L + qb + ks * 64);
    f32x4 d0 = {0.f, 0.f, 0.f, 0.f}, d1 = {0.f, 0.f, 0.f, 0.f};
#pragma unroll
    for (int ks = 0; ks < 4; ++ks) { d0 = mfma32(qa[ks], kf[ks], d0); d1 = mfma32(qa[ks], kf[4 + ks], d1); }
#pragma unroll
    for (int j = 0; j < 2; ++j) sc[j] = rb(rb((g & 1) ? d1[j] : d0[j]) * 0.08838834764831845f);
    float m0 = sc[0], m1 = sc[1];
    row_max2(m0, m1);
    const f32x2 mx = {x16_max(m0), x16_max(m1)};
    *(LAS f32x2*)(L + rmw) = mx;
  }
#pragma unroll
  for (int i = QKP + 1; i < 8; ++i) VLOAD(i);
  STAMP(2);
  BARRIER();
  float mh;
  {
    f32x4 t[4];
#pragma unroll
    for (int i = 0; i < 4; ++i) t[i] = *(LAS f32x4*)(L + rmr + i * 16);
    const float M0 = vmax3(vmax3(t[0][0], t[0][2], t[1][0]), vmax3(t[1][2], t[2][0], t[2][2]), vmax(t[3][0], t[3][2]));
    const float M1 = vmax3(vmax3(t[0][1], t[0][3], t[1][1]), vmax3(t[1][3], t[2][1], t[2][3]), vmax(t[3][1], t[3][3]));
    const float p0 = __builtin_amdgcn_exp2f((sc[0] - M0) * LOG2E), p1 = __builtin_amdgcn_exp2f((sc[1] - M1) * LOG2E);
    const unsigned pp = pk2(p0, p1);
    *(LAS u16*)(L + po) = (u16)pp;
    *(LAS u16*)(L + po + 576) = (u16)(pp >> 16);
    const f32x2 ls = {x16_sum(row_sum(p0)), x16_sum(row_sum(p1))};
    *(LAS f32x2*)(L + rmw + (L_RL - L_RM)) = ls;
    mh = (g & 1) ? M1 : M0;
  }
  const bool chk = is_wo && w == 0;
  const int roff = WS_RDY + kvh * 256 + n * 16;
  v4u rdy;
  ld16_sc1_asm(rdy, chk ? roff : OOB, srd_ws);
#define WOLOAD() do { if (is_wo) { _Pragma("unroll") for (int i = 0; i < 4; ++i) dma_glb<W2_NT>(ldst + L_K + i * 8192, wp0 + (i >> 1) * 16 * DM + (i & 1) * 128); } \
  else { _Pragma("unroll") for (int i = 0; i < 4; ++i) dma_glb<PF_NT>(ldst + L_K + 32768, PF_EARLY ? wp0 + i * 8 * FF : x); } } while (0)
  WOLOAD();
  VMWAIT(7);
  STAMP(14);
  BARRIER();
    v4u pa[8];
#pragma unroll
    for (int i = 0; i < 8; ++i) pa[i] = *(LAS v4u*)(L + pab + i * 64);
    f32x4 t[4];
#pragma unroll
    for (int i = 0; i < 4; ++i) t[i] = *(LAS f32x4*)(L + rmr + (L_RL - L_RM) + i * 16);
    f32x4 acc = {0.f, 0.f, 0.f, 0.f};
#pragma unroll
    for (int i = 0; i < 6; ++i) acc = mfma32(pa[i], tr_frag(L, tv0 + i * 8192, tv1 + i * 8192), acc);
    const f32x4 ts = (t[0] + t[1]) + (t[2] + t[3]);
    unsigned ml[2] = {nosent(__float_as_uint(mh)), nosent(__float_as_uint((g & 1) ? ts[1] + ts[3] : ts[0] + ts[2]))};
    int off[1] = {WS_PA + (kvh + 8 * (8 * (w >> 1) + (lane >> 3))) * 2048 + (2 * ((i32 >> 1) & 3) + (w & 1)) * 256 + (i32 >> 3) * 128 + (lane & 7) * 16};
    int ob[2], wb0[2], wb1[2];
#pragma unroll
    for (int i = 0; i < 2; ++i) {
      ob[i] = (n >> 2) == i ? L_O + (n & 3) * 320 + g * 16 : L_ZERO;
      const int x = ((((4 * w + 2 * i) & 15) ^ (2 * w)) << 4);
      wb0[i] = L_K + (w >> 2) * 8192 + (tr0 ^ x); wb1[i] = L_K + (w >> 2) * 8192 + (tr1 ^ x);
      PIN(ob[i]); PIN(wb0[i]); PIN(wb1[i]);
    }
    int ow = L_O + (w >> 1) * 320 + (32 * (w & 1) + 4 * (n & 7) + (((g & 1) << 1) | (g >> 1))) * 2;
    int so = lane < 32 ? WS_HP + (kvh * 8 + (i32 >> 1)) * 8192 + ((i32 & 1) * 256 + 32 * w + lane) * 16 : OOB;
    PIN(off[0]); PIN(ow); PIN(so);
    int pao = (n & 3) == 0 ? WS_PA + c * 2048 + w * 256 + g * 64 + (n >> 2) * 16 : OOB;
    PIN(ml[0]); PIN(ml[1]); PIN(pao); PIN(acc); PIN(pa[6]); PIN(pa[7]);
    STAMP(15);
    asm volatile("s_waitcnt vmcnt(4)" : "+v"(rdy) :: "memory");
    if (chk) {
#pragma unroll 1
      for (int it = 0; it < SPIN_CAP; ++it) {
        if (__all((rdy[0] == tlo) & (rdy[1] == thi) & (rdy[2] == tlo) & (rdy[3] == thi))) break;
        ld16_sc1_rw(rdy, roff, srd_ws);
        vmwait_pin<0>(rdy);
      }
    }
    STAMP(5);
    BARRIER();
#pragma unroll
    for (int i = 6; i < 8; ++i) acc = mfma32(pa[i], tr_frag(L, tv0 + i * 8192, tv1 + i * 8192), acc);
    const unsigned d0 = nosent(pk2(acc[0], dpp<SHL1>(acc[0])));
    st_sc1(rs, pao, v4u{d0, dppu<SHL2>(d0), ml[0], ml[1]});
  STAMP(6);

  if (__builtin_expect(is_wo, 1)) {
    v4u pv[1];
    STAMP(7);
    poll_fresh<1, DEPTH_A>(srd_ws, off, pv);
    STAMP(8);
    const float m0 = __uint_as_float(pv[0][2]);
    float mm = vmax(m0, dpp<ROR8>(m0));
    mm = x32_max(x16_max(mm));
    const float e0 = __builtin_amdgcn_exp2f((m0 - mm) * LOG2E);
    float lw = e0 * __uint_as_float(pv[0][3]);
    const auto s01 = __builtin_amdgcn_permlane32_swap(__float_as_uint(e0 * bflo(pv[0][0])), __float_as_uint(e0 * bfhi(pv[0][0])), false, false);
    const auto s23 = __builtin_amdgcn_permlane32_swap(__float_as_uint(e0 * bflo(pv[0][1])), __float_as_uint(e0 * bfhi(pv[0][1])), false, false);
    const float u0 = __uint_as_float(s01[0]) + __uint_as_float(s01[1]), u1 = __uint_as_float(s23[0]) + __uint_as_float(s23[1]);
    const auto su = __builtin_amdgcn_permlane16_swap(__float_as_uint(u0), __float_as_uint(u1), false, false);
    float ov = __uint_as_float(su[0]) + __uint_as_float(su[1]);
    ov += dpp<ROR8>(ov);
    lw += dpp<ROR8>(lw);
    lw = x32_sum(x16_sum(lw));
    *(LAS u16*)(L + ow) = (u16)pk2(ov * __builtin_amdgcn_rcpf(lw), 0.f);
    BARRIER();
    v4u oa[4], bf[4];
#pragma unroll
    for (int i = 0; i < 4; ++i) {
      oa[i] = *(LAS v4u*)(L + ob[i & 1] + (i >> 1) * 64);
      bf[i] = tr_frag(L, wb0[i & 1] + (i >> 1) * 16384, wb1[i & 1] + (i >> 1) * 16384);
    }
    SCHED();
    f32x4 hacc = {0.f, 0.f, 0.f, 0.f};
#pragma unroll
    for (int i = 0; i < 4; ++i) hacc = mfma32(oa[i], bf[i], hacc);
    st_sc1(rs, so, nosent4(hacc));
    SCHED();
    STAMP(9);
    poll_drain<1>(pv);
    EXIT();
  }
  if (is_ffn) {
    const u16* gup = ((lane >> 2) & 1 ? wu : wg) + (size_t)(64 * w + (lane >> 3)) * FF + 32 * jf + 8 * ((lane & 3) ^ (g & 1));
    const int alt = (lane & 2) ? -16 : 16;
    const v4u srd_d = make_srd(wd + (size_t)(32 * jf + 4 * w) * DM, 4 * DM * 2);
#define DOWNLOAD() do { _Pragma("unroll") for (int i = 0; i < 4; ++i) dma_buf<W2_NT>(ldst + L_K + i * 8192, vv0 + g * (DM * 2 - 256), srd_d, i * 256); } while (0)
    STAMP(20);
    BARRIER();
    STAMP(22);
#pragma unroll
    for (int i = 0; i < 8; ++i) dma_glb<GU_NT>(lbase + L_V + w * 8192 + i * 1024, gup + i * 8 * FF + ((i & 1) ? alt : 0));
    DOWNLOAD();
    STAMP(10);
    *(LAS v2u*)(L + L_P + w * 128 + (lane & 15) * 8) = v2u{0x3F803F80u, 0x3F803F80u};
    v4u bd[4];
    int ao[4], so2 = WS_OP + jf * 8192 + w * 1024 + lane * 16;
#pragma unroll
    for (int i = 0; i < 4; ++i) { ao[i] = (n >> 2) == i ? L_HH + (n & 3) * 64 + g * 16 : L_ZERO; PIN(ao[i]); }
    int e0 = L_K + (w >> 1) * 8192 + (tr0 ^ (((8 * (w & 1)) ^ (2 * w)) << 4)), e1 = L_K + (w >> 1) * 8192 + (tr1 ^ (((8 * (w & 1)) ^ (2 * w)) << 4));
    PIN(so2); PIN(e0); PIN(e1);
#define DOWN() do { BARRIER(); v4u a4[4]; \
      _Pragma("unroll") for (int i = 0; i < 4; ++i) a4[i] = *(LAS v4u*)(L + ao[i]); \
      SCHED(); f32x4 d = {0.f, 0.f, 0.f, 0.f}; \
      _Pragma("unroll") for (int i = 0; i < 4; ++i) d = mfma32(a4[i], bd[i], d); \
      st_sc1(rs, so2, nosent4(d)); SCHED(); STAMP(9); } while (0)
#define BDLOAD() do { _Pragma("unroll") for (int i = 0; i < 4; ++i) bd[i] = tr_frag(L, e0 ^ (i << 5), e1 ^ (i << 5)); \
      PIN(bd[0]); PIN(bd[1]); PIN(bd[2]); PIN(bd[3]); } while (0)
    if (w == 4) {
      v4u hv[4];
      int hr = L_HN + (lane & 3) * 1088 + (lane >> 2) * 16, off1[4];
#pragma unroll
      for (int i = 0; i < 4; ++i) { off1[i] = WS_H + kvh * 8192 + i * 2048 + (lane >> 2) * 128 + (lane & 3) * 16; PIN(off1[i]); }
      PIN(hr);
      wait_ready(rs, lane, tlo, thi);
      VMWAIT(0);
      STAMP(11);
      BARRIER();
      STAMP(7);
      poll_fresh<4, DEPTH_H>(srd_ws, off1, hv);
      STAMP(8);
#pragma unroll
      for (int i = 0; i < 4; ++i) *(LAS v4u*)(L + hr + i * 256) = hv[i];
      BARRIER();
      float s0 = 0.f, s1 = 0.f, q0 = 0.f, q1 = 0.f;
#pragma unroll
      for (int i = 0; i < 4; ++i)
#pragma unroll
        for (int k = 0; k < 4; k += 2) {
          s0 = dot2(hv[i][k], 0x3F803F80u, s0); s1 = dot2(hv[i][k + 1], 0x3F803F80u, s1);
          q0 = dot2(hv[i][k], hv[i][k], q0); q1 = dot2(hv[i][k + 1], hv[i][k + 1], q1);
        }
      s0 += s1; q0 += q1;
      s0 += dpp<ROR4>(s0); q0 += dpp<ROR4>(q0);
      s0 += dpp<ROR8>(s0); q0 += dpp<ROR8>(q0);
      const float mean = x32_sum(x16_sum(s0)) * (1.0f / DM);
      const float q = fmaxf(fmaf(-mean * DM, mean, x32_sum(x16_sum(q0))), 0.f);
      const float inv = __builtin_amdgcn_rcpf(rb(__builtin_amdgcn_sqrtf(rb(rb(q * (1.0f / DM)) + 1e-5f))));
      *(LAS f32x2*)(L + L_RM + (lane & 3) * 8) = f32x2{rb(mean), inv};
      STAMP(27);
      BARRIER();
      BDLOAD();
      DOWN();
      poll_drain<4>(hv);
      EXIT();
    }
    VMWAIT(0);
    STAMP(11);
    BARRIER();
    if (w > 4) {
      BDLOAD();
      BARRIER();
      BARRIER();
      DOWN();
      EXIT();
    }
    {
      int uo = L_V + (8 * g + tq) * 128 + ((4 * (tp >> 1) + (w ^ (((tq >> 1) & 1) | ((g & 1) << 1)))) * 16) + 8 * (tp & 1);
      PIN(uo);
      v4u bf[16];
#pragma unroll
      for (int i = 0; i < 16; ++i) bf[i] = tr_frag(L, uo + i * 4096, uo + i * 4096 + 512);
      BDLOAD();
      int hb = (n & 3) == 1 ? L_P : L_HN + (n >> 2) * 1088 + g * 16, hw = n < 8 ? L_HH + g * 64 + (8 * w + n) * 2 : L_JUNK;
      PIN(hb); PIN(hw);
#pragma unroll
      for (int i = 0; i < 16; ++i) PIN(bf[i]);
      STAMP(25);
      BARRIER();
      STAMP(26);
      v4u ha[8];
#pragma unroll
      for (int i = 0; i < 8; ++i) ha[i] = *(LAS v4u*)(L + hb + i * 64);
      SCHED();
      f32x4 ac[2] = {{0.f, 0.f, 0.f, 0.f}, {0.f, 0.f, 0.f, 0.f}};
#pragma unroll
      for (int i = 0; i < 8; ++i) { ac[i & 1] = mfma32(ha[i], bf[i], ac[i & 1]); ha[i] = *(LAS v4u*)(L + hb + (8 + i) * 64); SCHED(); }
#pragma unroll
      for (int i = 0; i < 8; ++i) ac[i & 1] = mfma32(ha[i], bf[8 + i], ac[i & 1]);
      float acc0 = ac[0][0] + ac[1][0], cs = ac[0][1] + ac[1][1];
      PIN(acc0); PIN(cs);
      STAMP(28);
      BARRIER();
      const f32x2 st = *(LAS f32x2*)(L + L_RM + g * 8);
      const float a = rb(fmaf(-st[0], cs, acc0) * st[1]), u = dpp<ROR8>(a);
      const float sg = rb(__builtin_amdgcn_rcpf(1.0f + __builtin_amdgcn_exp2f(a * -LOG2E)));
      *(LAS u16*)(L + hw) = (u16)pk2(sg * u, 0.f);
      STAMP(29);
      DOWN();
      EXIT();
    }
  }
  if (is_red) {
    const int r2 = lane >> 3, k = lane & 7, bb = r2 & 3;
    const float xv = bflo(x[bb * DM + 8 * rho + k]);
    const unsigned pre = out[0];
    int off1[1] = {WS_HP + (8 * w + r2) * 8192 + (8 * rho + k) * 16}, off2[1] = {w < 7 ? WS_OP + (8 * w + r2) * 8192 + (8 * rho + k) * 16 : OOB};
    int ho = (w == 0 && lane < 32) ? WS_H + k * 8192 + rho * 128 + bb * 16 : OOB;
    int oo = (w == 0 && k == 0 && lane < 32) ? (bb * DM + 8 * rho) * 2 : OOB;
    int rw_ = L_P + (((((g & 1) << 1) | (g >> 1)) * 8 + k) * 8 + w) * 4, rr_ = L_P + (bb * 8 + k) * 32;
    PIN(ho); PIN(oo); PIN(rw_); PIN(rr_); PIN(off1[0]); PIN(off2[0]);
    asm volatile("" :: "v"(pre));
    if (w == 0) wait_ready(rs, lane, tlo, thi);
    BARRIER();
    STAMP(7);
#define PARTIAL_SUM(PV, AREA, RES) do { \
      const auto s01 = __builtin_amdgcn_permlane32_swap(PV[0], PV[1], false, false); \
      const auto s23 = __builtin_amdgcn_permlane32_swap(PV[2], PV[3], false, false); \
      const float u0 = __uint_as_float(s01[0]) + __uint_as_float(s01[1]), u1 = __uint_as_float(s23[0]) + __uint_as_float(s23[1]); \
      const auto su = __builtin_amdgcn_permlane16_swap(__float_as_uint(u0), __float_as_uint(u1), false, false); \
      float t_ = __uint_as_float(su[0]) + __uint_as_float(su[1]); \
      t_ += dpp<ROR8>(t_); \
      *(LAS float*)(L + rw_ + (AREA)) = t_; \
      BARRIER(); \
      const f32x4 ra = *(LAS f32x4*)(L + rr_ + (AREA)), rb_ = *(LAS f32x4*)(L + rr_ + (AREA) + 16); \
      RES = ((ra[0] + ra[1]) + (ra[2] + ra[3])) + ((rb_[0] + rb_[1]) + (rb_[2] + rb_[3])); } while (0)
    v4u pv[1], pw[1];
    poll_fresh<1, DEPTH_R>(srd_ws, off1, pv);
    STAMP(8);
    float av;
    PARTIAL_SUM(pv[0], 0, av);
    const float hval = rb(xv + rb(av));
    {
      const unsigned e0 = nosent(pk2(dpp<0xA0>(hval), dpp<0xF5>(hval)));
      const unsigned qa = dppu<0x00>(e0), qb2 = dppu<0xAA>(e0);
      const unsigned oa = dppu<0x141>(qa), ob2 = dppu<0x141>(qb2);
      const bool hi = k & 4;
      st_sc1(rs, ho, v4u{hi ? oa : qa, hi ? ob2 : qb2, hi ? qa : oa, hi ? qb2 : ob2});
    }
    SCHED();
    STAMP(9);
    poll_drain<1>(pv);
    __builtin_amdgcn_s_sleep(C_DELAY);
    poll_fresh<1, DEPTH_R>(srd_ws, off2, pw);
    STAMP(10);
    PARTIAL_SUM(pw[0], 1024, av);
    {
      const float ov = hval + rb(av);
      const unsigned dw = pk2(ov, dpp<SHL1>(ov));
      const unsigned dw2 = dppu<SHL2>(dw);
      const unsigned dw4 = dppu<SHL4>(dw), dw6 = dppu<SHL4>(dw2);
      __builtin_amdgcn_raw_buffer_store_b128(v4u{dw, dw2, dw4, dw6}, __builtin_amdgcn_make_buffer_rsrc(out, 0, 4 * DM * 2, 0x00020000), oo, 0, 0);
      st8_sc1_asm(v2u{tlo, thi}, (c == 192 && w == 0 && lane == 0) ? WS_EPOCH : OOB, srd_ws);
    }
    SCHED();
    STAMP(11);
    poll_drain<1>(pw);
  }
  STAMP_OUT();
}

}

extern "C" void kernel_launch(void* const* d_in, const int* in_sizes, int n_in, void* d_out, int out_size, void* d_ws, size_t ws_size,
                              hipStream_t stream) {
  (void)in_sizes; (void)n_in; (void)out_size; (void)ws_size;
  fused_block<<<256, NTHR, 0, stream>>>((const u16*)d_in[0], (const u16*)d_in[1], (const u16*)d_in[2], (const u16*)d_in[3], (const u16*)d_in[4],
                                        (const u16*)d_in[6], (const u16*)d_in[7], (const u16*)d_in[8], (u16*)d_out, (unsigned char*)d_ws);
}
```

```cpp
#define ENC 0
#define ENCW 7
#define ENCC 192
#include <hip/hip_runtime.h>
#include <hip/hip_bf16.h>
#include <stdint.h>

namespace {

typedef unsigned short u16;
typedef unsigned long long u64;
typedef unsigned v4u __attribute__((ext_vector_type(4)));
typedef unsigned v2u __attribute__((ext_vector_type(2)));
typedef float f32x4 __attribute__((ext_vector_type(4)));
typedef float f32x2 __attribute__((ext_vector_type(2)));
typedef short bf16x8 __attribute__((ext_vector_type(8)));
typedef short s16x4 __attribute__((ext_vector_type(4)));
typedef __bf16 bf2 __attribute__((ext_vector_type(2)));
#define LAS __attribute__((address_space(3)))
#define GAS __attribute__((address_space(1)))

constexpr int DM = 512, HKV = 8, SK = 8192, DH = 128, WIN = 2048, FF = 1792, NQ = 4096;
constexpr int NTHR = 640;

constexpr int WS_EPOCH = 0;
constexpr int WS_RDY = 4096;
constexpr int WS_QG = 8192;
constexpr int WS_PA = WS_QG + 512 * 1024;
constexpr int WS_HP = WS_PA + 512 * 1024;
constexpr int REC = 8320;
constexpr int WS_H = WS_HP + 64 * REC;
constexpr int WS_OP = WS_H + 65536;
constexpr int WS_END = WS_OP + 56 * REC;
constexpr int OOB = 0x40000000;

constexpr int L_K = 0;
constexpr int L_V = 65536;
constexpr int L_WQ = 131072;
constexpr int L_HN = L_WQ;
constexpr int L_Q = L_WQ + 16384;
constexpr int L_XN = L_Q + 1280;
constexpr int L_O = L_XN;
constexpr int L_QP = L_XN + 1280;
constexpr int L_P = L_QP + 1024;
constexpr int L_RM = L_P + 2304;
constexpr int L_RL = L_RM + 128;
constexpr int L_HH = L_RL + 128;
constexpr int L_ZERO = L_HH + 256;
constexpr int L_FLAG = L_ZERO + 128;
constexpr int L_JUNK = L_FLAG + 16;
constexpr int L_BYTES = L_JUNK + 2048;

constexpr unsigned SENT = 0xFFFFFFFFu;
constexpr int SPIN_CAP = 1 << 16;
constexpr int AUX_SC1 = 16;
constexpr float LOG2E = 1.4426950408889634f;

#define POLL_SLEEP 1
#define ABL_NOW 0
#define CODE_BYTES $
#define WQ_POL "nt"
#define QPP 1
#define QKP 3
#define VTOP 3
#define PVS 5
#define QG_DELAY 30
#define WINDOW 3
#define POLL_DEPTH 3
#define P1_EARLY 1
#define GU_WAIT 1
#define PF_NT 0
#define PF_WIN1 0
#define PF_DELAY 0
#define WO_EARLY 1
#define WO_POS 0
#define DEPTH_A 1
#define DEPTH_H 2
#define DEPTH_R 3
#define C_DELAY 10
#define PF_EARLY 0
#define QG_DEPTH 1
#define QG_GAP 16
#define K0_DELAY 0
#define K1_DELAY 0
#define KV_NT 1
#define WQ_NT 1
#define W2_NT 1
#define GU_NT 1

__device__ __forceinline__ float bflo(unsigned x) { return __builtin_bit_cast(float, x << 16); }
__device__ __forceinline__ float bfhi(unsigned x) { return __builtin_bit_cast(float, x & 0xffff0000u); }
__device__ __forceinline__ unsigned pk2(float lo, float hi) {
  bf2 v = {(__bf16)lo, (__bf16)hi};
  return __builtin_bit_cast(unsigned, v);
}
__device__ __forceinline__ float rb(float x) { return (float)(__bf16)x; }
__device__ __forceinline__ unsigned nosent(unsigned x) { return x < 0xFFFFFFFEu ? x : 0xFFFFFFFEu; }
__device__ __forceinline__ v4u nosent4(f32x4 v) {
  return v4u{nosent(__float_as_uint(v[0])), nosent(__float_as_uint(v[1])), nosent(__float_as_uint(v[2])), nosent(__float_as_uint(v[3]))};
}
__device__ __forceinline__ bool fresh(v4u v) { return max(max(v[0], v[1]), max(v[2], v[3])) != SENT; }
__device__ __forceinline__ float vmax(float a, float b) { float r; asm("v_max_f32 %0, %1, %2" : "=v"(r) : "v"(a), "v"(b)); return r; }

__device__ __forceinline__ float vmax3(float a, float b, float c) { float r; asm("v_max3_f32 %0, %1, %2, %3" : "=v"(r) : "v"(a), "v"(b), "v"(c)); return r; }
__device__ __forceinline__ void row_max2(float& a, float& b) {
  asm("s_nop 1\n\t"
      "v_max_f32_dpp %0, %0, %0 row_ror:8 row_mask:0xf bank_mask:0xf\n\tv_max_f32_dpp %1, %1, %1 row_ror:8 row_mask:0xf bank_mask:0xf\n\ts_nop 0\n\t"
      "v_max_f32_dpp %0, %0, %0 row_ror:4 row_mask:0xf bank_mask:0xf\n\tv_max_f32_dpp %1, %1, %1 row_ror:4 row_mask:0xf bank_mask:0xf\n\ts_nop 0\n\t"
      "v_max_f32_dpp %0, %0, %0 row_ror:2 row_mask:0xf bank_mask:0xf\n\tv_max_f32_dpp %1, %1, %1 row_ror:2 row_mask:0xf bank_mask:0xf\n\ts_nop 0\n\t"
      "v_max_f32_dpp %0, %0, %0 row_ror:1 row_mask:0xf bank_mask:0xf\n\tv_max_f32_dpp %1, %1, %1 row_ror:1 row_mask:0xf bank_mask:0xf\n\ts_nop 1"
      : "+v"(a), "+v"(b));
}

__device__ __forceinline__ float dot2(unsigned a, unsigned b, float c) {
  return __builtin_amdgcn_fdot2_f32_bf16(__builtin_bit_cast(bf2, a), __builtin_bit_cast(bf2, b), c, false);
}
__device__ __forceinline__ f32x4 mfma32(v4u a, v4u b, f32x4 c) {
  return __builtin_amdgcn_mfma_f32_16x16x32_bf16(__builtin_bit_cast(bf16x8, a), __builtin_bit_cast(bf16x8, b), c, 0, 0, 0);
}
template <int CTRL> __device__ __forceinline__ float dpp(float x) {
  return __builtin_bit_cast(float, __builtin_amdgcn_mov_dpp(__builtin_bit_cast(int, x), CTRL, 0xf, 0xf, true));
}
template <int CTRL> __device__ __forceinline__ unsigned dppu(unsigned x) { return (unsigned)__builtin_amdgcn_mov_dpp((int)x, CTRL, 0xf, 0xf, true); }
constexpr int ROR8 = 0x128, ROR4 = 0x124, ROR2 = 0x122, ROR1 = 0x121, SHL1 = 0x101, SHL2 = 0x102, SHL4 = 0x104;
__device__ __forceinline__ float row_max(float x) { x = fmaxf(x, dpp<ROR8>(x)); x = fmaxf(x, dpp<ROR4>(x)); x = fmaxf(x, dpp<ROR2>(x)); return fmaxf(x, dpp<ROR1>(x)); }
__device__ __forceinline__ float row_sum(float x) { x += dpp<ROR8>(x); x += dpp<ROR4>(x); x += dpp<ROR2>(x); return x + dpp<ROR1>(x); }
__device__ __forceinline__ float x16_max(float x) { auto s = __builtin_amdgcn_permlane16_swap(__float_as_uint(x), __float_as_uint(x), false, false); return vmax(__uint_as_float(s[0]), __uint_as_float(s[1])); }
__device__ __forceinline__ float x32_max(float x) { auto s = __builtin_amdgcn_permlane32_swap(__float_as_uint(x), __float_as_uint(x), false, false); return vmax(__uint_as_float(s[0]), __uint_as_float(s[1])); }
__device__ __forceinline__ float x16_sum(float x) { auto s = __builtin_amdgcn_permlane16_swap(__float_as_uint(x), __float_as_uint(x), false, false); return __uint_as_float(s[0]) + __uint_as_float(s[1]); }
__device__ __forceinline__ float x32_sum(float x) { auto s = __builtin_amdgcn_permlane32_swap(__float_as_uint(x), __float_as_uint(x), false, false); return __uint_as_float(s[0]) + __uint_as_float(s[1]); }
__device__ __forceinline__ float wave_sum(float x) { return x32_sum(x16_sum(row_sum(x))); }
__device__ __forceinline__ f32x4 sel2(bool c, f32x4 a, f32x4 b) {
  return f32x4{c ? b[0] : a[0], c ? b[1] : a[1], c ? b[2] : a[2], c ? b[3] : a[3]};
}
__device__ __forceinline__ f32x4 sel4(int g, f32x4 a, f32x4 b, f32x4 c, f32x4 d) { return sel2(g & 2, sel2(g & 1, a, b), sel2(g & 1, c, d)); }

#define PIN(x) asm volatile("" : "+v"(x))
#define SCHED() __builtin_amdgcn_sched_barrier(0)
#define LDS_FENCE() asm volatile("s_waitcnt lgkmcnt(0)" ::: "memory")
#define BARRIER() do { asm volatile("s_waitcnt lgkmcnt(0)\n\ts_barrier" ::: "memory"); } while (0)
#define VMWAIT(N) asm volatile("s_waitcnt vmcnt(" #N ")" ::: "memory")
#define STAMP(i) do { if ((i) == ENC) { u64 t_; SCHED(); asm volatile("s_memrealtime %0\n\ts_waitcnt lgkmcnt(0)" : "=s"(t_)); mini0 = (unsigned)t_; SCHED(); } } while (0)
#define STAMP_OUT() do { if (w == ENCW && c == ENCC) { while ((unsigned)__builtin_amdgcn_s_memrealtime() - mini0 < 4000u) __builtin_amdgcn_s_sleep(1); } } while (0)

#define EXIT() do { STAMP_OUT(); asm volatile("s_endpgm" ::: "memory"); __builtin_unreachable(); } while (0)

__device__ __forceinline__ v4u make_srd(const void* p, unsigned bytes) {
  const u64 a = (u64)p;
  return v4u{(unsigned)a, (unsigned)(a >> 32) & 0xffffu, bytes, 0x00020000u};
}
template <int NT> __device__ __forceinline__ void dma_buf(unsigned lds, int voff, v4u srd, int soff) {
  if (NT) asm volatile("s_mov_b32 m0, %0\n\ts_nop 0\n\tbuffer_load_dwordx4 %1, %2, %3 offen nt lds" :: "s"(lds), "v"(voff), "s"(srd), "s"(soff) : "m0", "memory");
  else asm volatile("s_mov_b32 m0, %0\n\ts_nop 0\n\tbuffer_load_dwordx4 %1, %2, %3 offen lds" :: "s"(lds), "v"(voff), "s"(srd), "s"(soff) : "m0", "memory");
}
template <int NT> __device__ __forceinline__ void dma_glb(unsigned lds, const void* p) {
  if (NT) asm volatile("s_mov_b32 m0, %0\n\ts_nop 0\n\tglobal_load_lds_dwordx4 %1, off nt" :: "s"(lds), "v"(p) : "m0", "memory");
  else asm volatile("s_mov_b32 m0, %0\n\ts_nop 0\n\tglobal_load_lds_dwordx4 %1, off" :: "s"(lds), "v"(p) : "m0", "memory");
}
__device__ __forceinline__ void ld16_asm(v4u& d, int voff, v4u srd, int soff) {
  asm volatile("buffer_load_dwordx4 %0, %1, %2, %3 offen" : "=v"(d) : "v"(voff), "s"(srd), "s"(soff) : "memory");
}
__device__ __forceinline__ void ld16_sc1_asm(v4u& d, int voff, v4u srd) {
  asm volatile("buffer_load_dwordx4 %0, %1, %2, 0 offen sc1" : "=v"(d) : "v"(voff), "s"(srd) : "memory");
}
__device__ __forceinline__ void ld8_sc1_asm(v2u& d, v4u srd) {
  asm volatile("buffer_load_dwordx2 %0, off, %1, 0 sc1" : "=v"(d) : "s"(srd) : "memory");
}
__device__ __forceinline__ void st16_sc1_asm(v4u v, int voff, v4u srd) {
  asm volatile("buffer_store_dwordx4 %0, %1, %2, 0 offen sc1\n\ts_nop 1" :: "v"(v), "v"(voff), "s"(srd) : "memory");
}

__device__ __forceinline__ void st8_sc1_asm(v2u v, int voff, v4u srd) {
  asm volatile("buffer_store_dwordx2 %0, %1, %2, 0 offen sc1" :: "v"(v), "v"(voff), "s"(srd) : "memory");
}
__device__ __forceinline__ float sel4f(int b, f32x4 a) { const float lo = (b & 1) ? a[1] : a[0], hi = (b & 1) ? a[3] : a[2]; float r = (b & 2) ? hi : lo; return r; }

__device__ __forceinline__ v4u ld_sc1(__amdgpu_buffer_rsrc_t rs, int off) { return __builtin_amdgcn_raw_buffer_load_b128(rs, off, 0, AUX_SC1); }
__device__ __forceinline__ void st_sc1(__amdgpu_buffer_rsrc_t rs, int off, v4u v) { __builtin_amdgcn_raw_buffer_store_b128(v, rs, off, 0, AUX_SC1); }
__device__ __forceinline__ v2u tr_rd(LAS unsigned char* L, int off) {
  return __builtin_bit_cast(v2u, __builtin_amdgcn_ds_read_tr16_b64_v4i16((LAS s16x4*)(L + off)));
}
__device__ __forceinline__ v4u tr_frag(LAS unsigned char* L, int off0, int off1) {
  const v2u a = tr_rd(L, off0), b = tr_rd(L, off1);
  return v4u{a[0], a[1], b[0], b[1]};
}

__device__ __forceinline__ void ld16_sc1_rw(v4u& d, int voff, v4u srd) {
  asm volatile("buffer_load_dwordx4 %0, %1, %2, 0 offen sc1" : "+v"(d) : "v"(voff), "s"(srd) : "memory");
}
template <int CNT> __device__ __forceinline__ void vmwait_pin(v4u& a) { asm volatile("s_waitcnt vmcnt(%1)" : "+v"(a) : "n"(CNT) : "memory"); }
template <int N, int D> __device__ __forceinline__ void poll_start(v4u srd, const int (&off)[N], v4u (&v)[N]) {
#pragma unroll
  for (int i = 0; i < N; ++i) v[i] = v4u{SENT, SENT, SENT, SENT};
#pragma unroll
  for (int k = 0; k < D - 1; ++k) {
#pragma unroll
    for (int i = 0; i < N; ++i) ld16_sc1_rw(v[i], off[i], srd);
    __builtin_amdgcn_s_sleep(POLL_SLEEP);
  }
}
__device__ __forceinline__ void ld16_sc1_masked(v4u& d, int voff, v4u srd, u64 mask) {
  asm volatile("s_mov_b64 exec, %3\n\tbuffer_load_dwordx4 %0, %1, %2, 0 offen sc1\n\ts_mov_b64 exec, -1" : "+v"(d) : "v"(voff), "s"(srd), "s"(mask) : "memory");
}
template <int N, int D> __device__ __forceinline__ void poll_finish(v4u srd, const int (&off)[N], v4u (&v)[N]) {
  u64 miss[N];
#pragma unroll
  for (int i = 0; i < N; ++i) miss[i] = ~0ull;
#pragma unroll 1
  for (int it = 0; it < SPIN_CAP; ++it) {
#pragma unroll
    for (int i = 0; i < N; ++i) ld16_sc1_masked(v[i], off[i], srd, miss[i]);
    vmwait_pin<(D - 1) * N>(v[0]);
#pragma unroll
    for (int i = 1; i < N; ++i) PIN(v[i]);
    u64 any = 0;
#pragma unroll
    for (int i = 0; i < N; ++i) { miss[i] = __builtin_amdgcn_ballot_w64(!fresh(v[i])); any |= miss[i]; }
    if (any == 0) break;
    __builtin_amdgcn_s_sleep(POLL_SLEEP);
  }
}
template <int N, int D> __device__ __forceinline__ void poll_fresh(v4u srd, const int (&off)[N], v4u (&v)[N]) {
  poll_start<N, D>(srd, off, v);
  poll_finish<N, D>(srd, off, v);
}
template <int N> __device__ __forceinline__ void poll_drain(v4u (&v)[N]) {
  vmwait_pin<0>(v[0]);
#pragma unroll
  for (int i = 1; i < N; ++i) PIN(v[i]);
}
__device__ __forceinline__ void wait_ready(__amdgpu_buffer_rsrc_t rs, int lane, unsigned tlo, unsigned thi) {
#pragma unroll 1
  for (int it = 0; it < SPIN_CAP; ++it) {
    asm volatile("" ::: "memory");
    const v4u r0 = ld_sc1(rs, WS_RDY + lane * 16), r1 = ld_sc1(rs, WS_RDY + 1024 + lane * 16);
    const bool ok = (r0[0] == tlo) & (r0[1] == thi) & (r0[2] == tlo) & (r0[3] == thi) & (r1[0] == tlo) & (r1[1] == thi) & (r1[2] == tlo) & (r1[3] == thi);
    if (__all(ok)) break;
    __builtin_amdgcn_s_sleep(4);
  }
}

__global__ void __launch_bounds__(NTHR, 3)
fused_block(const u16* __restrict__ x, const u16* __restrict__ kc, const u16* __restrict__ vc, const u16* __restrict__ wq,
            const u16* __restrict__ wo, const u16* __restrict__ wg, const u16* __restrict__ wu, const u16* __restrict__ wd,
            u16* __restrict__ out, unsigned char* __restrict__ ws) {
  __shared__ __attribute__((aligned(16))) unsigned char smem_[L_BYTES];
  {
    u64 pc0;
    asm volatile("s_getpc_b64 %0" : "=s"(pc0));
    if (__builtin_amdgcn_readfirstlane(threadIdx.x >> 6) == 1) {
      const v4u srd_c = make_srd((const void*)(pc0 & ~255ull), CODE_BYTES);
      const int so = ((blockIdx.x >> 3) & 15) * 2048;
      asm volatile("s_mov_b32 m0, %0\n\ts_nop 0\n\tbuffer_load_dwordx4 %1, %2, %3 offen lds\n\tbuffer_load_dwordx4 %1, %2, %3 offen offset:1024 lds"
                   :: "s"((unsigned)(size_t)(LAS unsigned char*)smem_ + L_JUNK), "v"((threadIdx.x & 63) * 16), "s"(srd_c), "s"(so) : "m0", "memory");
    }
    __builtin_amdgcn_sched_barrier(0);
  }
  LAS unsigned char* L = (LAS unsigned char*)smem_;
  const unsigned lbase = (unsigned)(size_t)L;
  const int c = blockIdx.x, kvh = c & 7, i32 = c >> 3;
  const int lane = threadIdx.x & 63, wv = __builtin_amdgcn_readfirstlane(threadIdx.x >> 6);
  const int w = wv < 2 ? 8 + wv : wv - 2;
  const int n = lane & 15, g = lane >> 4;
  const v4u srd_ws = make_srd(ws, WS_END);
  unsigned mini0 = 0, mini1 = 0;
  STAMP(0);

  const int b = i32 >> 3, s = i32 & 7;
  const size_t kvbase = ((size_t)(b * HKV + kvh) * SK + (SK - WIN) + s * 256) * DH;
  const v4u srd_k = make_srd(kc + kvbase, 65536);
  const int rot = w + 37 * c;
  const int kv0 = g * 256 + ((n ^ g) * 16);
  const unsigned ldst = lbase + w * 1024;
  v2u ep;
  v4u xr[4];
#define KLOAD(i) dma_buf<KV_NT>(ldst + L_K + (i) * 8192, kv0 ^ (((i) & 3) << 6), srd_k, ((8 * (i) + rot) & 63) << 10)
  if (wv == 0) {
    const v4u srd_x = make_srd(x, 4 * DM * 2);
    const v4u srd_wq = make_srd(wq + (size_t)b * 128 * NQ + kvh * 512 + s * 64, 128 * NQ * 2);
    ld8_sc1_asm(ep, srd_ws);
#pragma unroll
    for (int i = 0; i < 4; ++i) ld16_asm(xr[i], g * 1024 + n * 16, srd_x, ((b + i) & 3) * 256);
    const int row = lane >> 3, v0 = row * (NQ * 2) + (((lane & 7) ^ (2 * ((row >> 1) & 1))) * 16);
    int so;
#define WQ2 "buffer_load_dwordx4 %1, %3, %0 offen " WQ_POL " lds\n\ts_addk_i32 m0, 0x400\n\ts_add_u32 %0, %0, %5\n\t" \
            "buffer_load_dwordx4 %2, %3, %0 offen " WQ_POL " lds\n\ts_addk_i32 m0, 0x400\n\ts_add_u32 %0, %0, %5\n\t"
    asm volatile("s_mov_b32 m0, %4\n\ts_mov_b32 %0, 0\n\t" WQ2 WQ2 WQ2 WQ2 WQ2 WQ2 WQ2 WQ2
                 : "=&s"(so) : "v"(v0), "v"(v0 ^ 64), "s"(srd_wq), "s"(lbase + L_WQ), "s"(8 * NQ * 2) : "m0", "scc", "memory");
    *(LAS unsigned*)(L + L_FLAG) = 0u;
    LDS_FENCE();
  }
  asm volatile("s_barrier" ::: "memory");
  if (wv >= 2) { __builtin_amdgcn_s_sleep(K0_DELAY); KLOAD(0); __builtin_amdgcn_s_sleep(K1_DELAY); KLOAD(1); KLOAD(2); }
  int wv2 = wv;
  asm volatile("" : "+s"(wv2));

  if (wv2 == 1) {
    v2u ep1;
    ld8_sc1_asm(ep1, srd_ws);
    const v4u sent = {SENT, SENT, SENT, SENT};
    st16_sc1_asm(sent, WS_PA + kvh * 65536 + g * 8192 + i32 * 256 + n * 16, srd_ws);
    st16_sc1_asm(sent, WS_PA + kvh * 65536 + (g + 4) * 8192 + i32 * 256 + n * 16, srd_ws);
    const int base = i32 < 16 ? WS_HP + (kvh * 8 + (i32 >> 1)) * REC + (i32 & 1) * 4096 : WS_OP + (kvh * 7 + i32 - 16) * REC;
    const int len = i32 < 16 ? 4096 : i32 < 23 ? 8192 : 0;
#pragma unroll 1
    for (int o = 0; o < len; o += 1024) st16_sc1_asm(sent, base + o + lane * 16, srd_ws);
    st16_sc1_asm(sent, (i32 >= 24 && lane < 32) ? WS_H + ((i32 - 24) * 8 + kvh) * 1024 + (lane >> 2) * 128 + (lane & 3) * 16 : OOB, srd_ws);
    *(LAS v4u*)(L + L_ZERO + (lane & 7) * 16) = v4u{0u, 0u, 0u, 0u};
    asm volatile("s_waitcnt vmcnt(0)" : "+v"(ep1) :: "memory");
    const u64 tag = (((u64)ep1[1] << 32) | ep1[0]) + 0x9E3779B97F4A7C15ull;
    st8_sc1_asm(v2u{(unsigned)tag, (unsigned)(tag >> 32)}, lane == 0 ? WS_RDY + (kvh * 32 + i32) * 8 : OOB, srd_ws);
    STAMP(1);
    EXIT();
  }
  if (wv2 == 0) {
    const int rq = b, cq = s;
    STAMP(7);
    asm volatile("s_waitcnt vmcnt(16)" : "+v"(ep), "+v"(xr[0]), "+v"(xr[1]), "+v"(xr[2]), "+v"(xr[3]) :: "memory");
    STAMP(9);
    {
      float s0 = 0.f, s1 = 0.f, q0 = 0.f, q1 = 0.f;
#pragma unroll
      for (int i = 0; i < 4; ++i)
#pragma unroll
        for (int k = 0; k < 4; k += 2) {
          s0 = dot2(xr[i][k], 0x3F803F80u, s0); s1 = dot2(xr[i][k + 1], 0x3F803F80u, s1);
          q0 = dot2(xr[i][k], xr[i][k], q0); q1 = dot2(xr[i][k + 1], xr[i][k + 1], q1);
        }
      const float mean = row_sum(s0 + s1) * (1.0f / DM);
      const float q = fmaxf(fmaf(-mean * DM, mean, row_sum(q0 + q1)), 0.f);
      float v[8];
#pragma unroll
      for (int k = 0; k < 4; ++k) { v[2 * k] = bflo(xr[0][k]); v[2 * k + 1] = bfhi(xr[0][k]); }
      const float mb = rb(mean);
      const float inv = __builtin_amdgcn_rcpf(rb(__builtin_amdgcn_sqrtf(rb(rb(q * (1.0f / DM)) + 1e-5f))));
      v4u o;
#pragma unroll
      for (int k = 0; k < 4; ++k) o[k] = pk2(rb(v[2 * k] - mb) * inv, rb(v[2 * k + 1] - mb) * inv);
      *(LAS v4u*)(L + L_XN + g * 320 + n * 16) = o;
    }
    STAMP(1);
    LDS_FENCE();
    f32x4 acc[4];
    {
      v4u xa[4];
#pragma unroll
      for (int ks = 0; ks < 4; ++ks) xa[ks] = *(LAS v4u*)(L + L_XN + (n & 3) * 320 + ks * 64 + g * 16);
      const int q4 = n >> 2, p = n & 3, y = ((q4 >> 1) & 1) | ((g & 1) << 1);
      const int rbase = L_WQ + (8 * g + q4) * 128 + 8 * (p & 1);
      int o[4];
#pragma unroll
      for (int t = 0; t < 4; ++t) { o[t] = rbase + (((2 * t + (p >> 1)) ^ (2 * y)) * 16); acc[t] = f32x4{0.f, 0.f, 0.f, 0.f}; }
#pragma unroll
      for (int ks = 0; ks < 4; ++ks) {
        if (ks == 0) VMWAIT(12); else if (ks == 1) VMWAIT(8); else if (ks == 2) VMWAIT(4); else VMWAIT(0);
        if (ks == 3) STAMP(4);
#pragma unroll
        for (int t = 0; t < 4; ++t) acc[t] = mfma32(xa[ks], tr_frag(L, o[t] + ks * 4096, o[t] + ks * 4096 + 512), acc[t]);
      }
    }
    *(LAS f32x4*)(L + L_QP + lane * 16) = sel4(g, acc[0], acc[1], acc[2], acc[3]);
    LDS_FENCE();
    const f32x4 q0 = *(LAS f32x4*)(L + L_QP + (lane & 31) * 32), q1 = *(LAS f32x4*)(L + L_QP + (lane & 31) * 32 + 16);
    const u64 tag = (((u64)ep[1] << 32) | ep[0]) + 0x9E3779B97F4A7C15ull;
    const unsigned tlo = (unsigned)tag, thi = (unsigned)(tag >> 32);
    STAMP(5);
    STAMP(2);
    {
      const bool up = lane >= 32;
      const int o = WS_QG + ((kvh * 4 + (up ? 1 : 0)) * 4 + rq) * 4096 + (32 * cq + (lane & 31)) * 16;
      st16_sc1_asm(v4u{__float_as_uint(up ? q0[1] : q0[0]), __float_as_uint(up ? q1[1] : q1[0]), tlo, thi}, o, srd_ws);
      st16_sc1_asm(v4u{__float_as_uint(up ? q0[3] : q0[2]), __float_as_uint(up ? q1[3] : q1[2]), tlo, thi}, o + 2 * 4 * 4096, srd_ws);
    }
    STAMP(3);
    __builtin_amdgcn_s_sleep(QG_DELAY);
    const int qo = WS_QG + (kvh * 4 + b) * 16384 + lane * 16;
    v4u qg[16];
    u64 miss[16];
#pragma unroll
    for (int i = 0; i < 16; ++i) { asm volatile("" : "=v"(qg[i])); miss[i] = ~0ull; }
#define QPASS() do { _Pragma("unroll") for (int i = 0; i < 16; ++i) \
      asm volatile("s_mov_b64 exec, %4\n\tbuffer_load_dwordx4 %0, %1, %2, %3 offen offset:%5 sc1\n\ts_mov_b64 exec, -1" \
                   : "+v"(qg[i]) : "v"(qo), "s"(srd_ws), "s"((i >> 2) * 4096), "s"(miss[i]), "n"((i & 3) * 1024) : "memory"); } while (0)
#pragma unroll 1
    for (int it = 0; it < SPIN_CAP; ++it) {
      QPASS();
      vmwait_pin<16 * (QG_DEPTH - 1)>(qg[0]);
#pragma unroll
      for (int i = 1; i < 16; ++i) PIN(qg[i]);
      u64 any = 0;
#pragma unroll
      for (int i = 0; i < 16; ++i) { miss[i] = __builtin_amdgcn_ballot_w64((((u64)qg[i][3] << 32) | qg[i][2]) != tag); any |= miss[i]; }
      if (any == 0) break;
    }
#pragma unroll
    for (int u = 0; u < 4; ++u) {
      const float a = (__uint_as_float(qg[u][0]) + __uint_as_float(qg[4 + u][0])) + (__uint_as_float(qg[8 + u][0]) + __uint_as_float(qg[12 + u][0]));
      const float bq = (__uint_as_float(qg[u][1]) + __uint_as_float(qg[4 + u][1])) + (__uint_as_float(qg[8 + u][1]) + __uint_as_float(qg[12 + u][1]));
      *(LAS unsigned*)(L + L_Q + u * 320 + lane * 4) = pk2(a, bq);
    }
    *(LAS v2u*)(L + L_FLAG + 8) = v2u{tlo, thi};
    LDS_FENCE();
    *(volatile LAS unsigned*)(L + L_FLAG) = 1u;
    poll_drain<16>(qg);
    STAMP(6);
    EXIT();
  }

  const bool is_wo = i32 < 16, is_ffn = i32 >= 16 && i32 < 23, is_red = i32 >= 24;
  const int jf = kvh * 7 + (i32 - 16);
  const int rho = (i32 - 24) * 8 + kvh;
  const __amdgpu_buffer_rsrc_t rs = __builtin_amdgcn_make_buffer_rsrc(ws, 0, WS_END, 0x00020000);
  const v4u srd_v = make_srd(vc + kvbase, 65536);
  const int vv0 = g * 256 + ((n ^ ((g << 2) | (w & 3))) * 16);
  asm volatile("" :: "s"(wo), "s"(wg), "s"(wu), "s"(wd), "s"(out), "s"(x));
#define VLOAD(i) dma_buf<KV_NT>(ldst + L_V + (i) * 8192, vv0, srd_v, ((8 * (i) + rot) & 63) << 10)
#define WW() VMWAIT(2)
  WW();
  STAMP(12);
  KLOAD(3);
  WW(); KLOAD(4); WW(); KLOAD(5); WW(); KLOAD(6); WW(); KLOAD(7);
  STAMP(1);
  WW(); VLOAD(0); WW(); VLOAD(1); WW(); VLOAD(2);
  constexpr int VNEXT = 3;
  v4u kf[8];
  {
    const int kb = L_K + (n >> 2) * 8192 + w * 1024 + (n & 3) * 256 + ((g ^ n) * 16);
#pragma unroll
    for (int ks = 0; ks < 4; ++ks) { kf[ks] = *(LAS v4u*)(L + (kb ^ (ks << 6))); kf[4 + ks] = *(LAS v4u*)(L + (kb ^ (ks << 6)) + 32768); }
  }
  int qb = L_Q + (2 * (n >> 3) + (n & 1)) * 320 + g * 16;
  int rmw = L_RM + (g >> 1) * 64 + w * 8, rmr = L_RM + (g >> 1) * 64;
  int po = L_P + (g >> 1) * 1152 + ((4 * (g & 1) + (n >> 2)) * 32 + 4 * w + (n & 3)) * 2;
  const u16* wp0 = is_wo ? wo + (size_t)(kvh * 512 + (2 * (i32 >> 3) + (w >> 2)) * 128 + 32 * ((i32 >> 1) & 3) + 4 * (w & 3)) * DM + (i32 & 1) * 256 + g * (DM - 128) + (vv0 >> 1)
                         : (lane >= 32 ? wu : wg) + (size_t)(64 * w + (i32 & 7) + (i32 & 8) * 4) * FF + ((224 * kvh) & ~63) + 8 * (lane & 31);
  PIN(wp0);
  const int tq = n >> 2, tp = n & 3;
  int tr0 = (8 * g + tq) * 256 + (((2 * w + (tp >> 1)) ^ ((tq << 2) | ((2 * g) & 3))) * 16) + 8 * (tp & 1);
  int tr1 = (8 * g + 4 + tq) * 256 + (((2 * w + (tp >> 1)) ^ ((tq << 2) | ((2 * g + 1) & 3))) * 16) + 8 * (tp & 1);
  PIN(tr0); PIN(tr1);
  int tv0 = L_V + tr0, tv1 = L_V + tr1, pab = L_P + (n >> 2) * 576 + g * 16;
  PIN(tv0); PIN(tv1); PIN(pab);
  PIN(qb); PIN(rmw); PIN(rmr); PIN(po);
    int off[1] = {WS_PA + kvh * 65536 + (2 * ((i32 >> 1) & 3) + (w & 1)) * 8192 + (8 * (w >> 1) + (lane >> 3)) * 256 + (i32 >> 3) * 128 + (lane & 7) * 16};
    int ob[2], wb0[2], wb1[2];
#pragma unroll
    for (int i = 0; i < 2; ++i) {
      ob[i] = (n >> 2) == i ? L_O + (n & 3) * 320 + g * 16 : L_ZERO;
      const int x = ((((4 * w + 2 * i) & 15) ^ (2 * w)) << 4);
      wb0[i] = L_K + (w >> 2) * 8192 + (tr0 ^ x); wb1[i] = L_K + (w >> 2) * 8192 + (tr1 ^ x);
      PIN(ob[i]); PIN(wb0[i]); PIN(wb1[i]);
    }
    int ow = L_O + (w >> 1) * 320 + (32 * (w & 1) + 4 * (n & 7) + (((g & 1) << 1) | (g >> 1))) * 2;
    int so = lane < 32 ? WS_HP + (kvh * 8 + (i32 >> 1)) * REC + ((i32 & 1) * 256 + 32 * w + lane) * 16 : OOB;
    PIN(off[0]); PIN(ow); PIN(so);
    int pao = (n & 3) == 0 ? WS_PA + kvh * 65536 + w * 8192 + i32 * 256 + g * 64 + (n >> 2) * 16 : OOB;
    PIN(pao);
  const bool chk = is_wo && w == 0;
  const int roff = WS_RDY + kvh * 256 + n * 16;
  int rdo = chk ? roff : OOB;
  PIN(rdo);
#pragma unroll
  for (int i = 0; i < 8; ++i) PIN(kf[i]);
#define QFLAG() (__builtin_amdgcn_readfirstlane(*(volatile LAS unsigned*)(L + L_FLAG)) != 0u)
  int vi = VNEXT;
#pragma unroll 1
  while (vi < 8 && (vi <= QKP || !QFLAG())) { WW(); VLOAD(vi); ++vi; }
  STAMP(13);
#pragma unroll 1
  for (int it = 0; it < (SPIN_CAP << 4); ++it) {
    if (QFLAG()) break;
  }
  asm volatile("" ::: "memory");
  const v2u tagv = *(LAS v2u*)(L + L_FLAG + 8);
  const unsigned tlo = tagv[0], thi = tagv[1];
  STAMP(3);
  float sc[2];
  {
    v4u qa[4];
#pragma unroll
    for (int ks = 0; ks < 4; ++ks) qa[ks] = *(LAS v4u*)(L + qb + ks * 64);
    f32x4 d0 = {0.f, 0.f, 0.f, 0.f}, d1 = {0.f, 0.f, 0.f, 0.f};
#pragma unroll
    for (int ks = 0; ks < 4; ++ks) { d0 = mfma32(qa[ks], kf[ks], d0); d1 = mfma32(qa[ks], kf[4 + ks], d1); }
#pragma unroll
    for (int j = 0; j < 2; ++j) sc[j] = rb(rb((g & 1) ? d1[j] : d0[j]) * 0.08838834764831845f);
    float m0 = sc[0], m1 = sc[1];
    row_max2(m0, m1);
    const f32x2 mx = {x16_max(m0), x16_max(m1)};
    *(LAS f32x2*)(L + rmw) = mx;
  }
#pragma unroll 1
  for (int k = 0; k < VTOP && vi < 8; ++k) { VLOAD(vi); ++vi; }
  STAMP(2);
  BARRIER();
  float mh;
  {
    f32x4 t[4];
#pragma unroll
    for (int i = 0; i < 4; ++i) t[i] = *(LAS f32x4*)(L + rmr + i * 16);
    const float M0 = vmax3(vmax3(t[0][0], t[0][2], t[1][0]), vmax3(t[1][2], t[2][0], t[2][2]), vmax(t[3][0], t[3][2]));
    const float M1 = vmax3(vmax3(t[0][1], t[0][3], t[1][1]), vmax3(t[1][3], t[2][1], t[2][3]), vmax(t[3][1], t[3][3]));
    const float p0 = __builtin_amdgcn_exp2f((sc[0] - M0) * LOG2E), p1 = __builtin_amdgcn_exp2f((sc[1] - M1) * LOG2E);
    const unsigned pp = pk2(p0, p1);
    *(LAS u16*)(L + po) = (u16)pp;
    *(LAS u16*)(L + po + 576) = (u16)(pp >> 16);
    const f32x2 ls = {x16_sum(row_sum(p0)), x16_sum(row_sum(p1))};
    *(LAS f32x2*)(L + rmw + (L_RL - L_RM)) = ls;
    mh = (g & 1) ? M1 : M0;
  }
#pragma unroll 1
  while (vi < 8) { VLOAD(vi); ++vi; }
  v4u rdy;
  ld16_sc1_asm(rdy, rdo, srd_ws);
#define WOLOAD() do { if (is_wo) { _Pragma("unroll") for (int i = 0; i < 4; ++i) dma_glb<W2_NT>(ldst + L_K + i * 8192, wp0 + (i >> 1) * 16 * DM + (i & 1) * 128); } \
  else { _Pragma("unroll") for (int i = 0; i < 4; ++i) dma_glb<PF_NT>(ldst + L_K + 32768, PF_EARLY ? wp0 + i * 8 * FF : x); } } while (0)
  WOLOAD();
  asm volatile("s_waitcnt vmcnt(%0)" :: "n"(8 - PVS + 5) : "memory");
  STAMP(14);
  BARRIER();
    v4u pa[8];
#pragma unroll
    for (int i = 0; i < 8; ++i) pa[i] = *(LAS v4u*)(L + pab + i * 64);
    f32x4 t[4];
#pragma unroll
    for (int i = 0; i < 4; ++i) t[i] = *(LAS f32x4*)(L + rmr + (L_RL - L_RM) + i * 16);
    f32x4 acc = {0.f, 0.f, 0.f, 0.f};
#pragma unroll
    for (int i = 0; i < PVS; ++i) acc = mfma32(pa[i], tr_frag(L, tv0 + i * 8192, tv1 + i * 8192), acc);
    const f32x4 ts = (t[0] + t[1]) + (t[2] + t[3]);
    unsigned ml[2] = {nosent(__float_as_uint(mh)), nosent(__float_as_uint((g & 1) ? ts[1] + ts[3] : ts[0] + ts[2]))};
    PIN(ml[0]); PIN(ml[1]); PIN(acc);
#pragma unroll
    for (int i = PVS; i < 8; ++i) PIN(pa[i]);
    STAMP(15);
    asm volatile("s_waitcnt vmcnt(4)" : "+v"(rdy) :: "memory");
    if (chk) {
#pragma unroll 1
      for (int it = 0; it < SPIN_CAP; ++it) {
        if (__all((rdy[0] == tlo) & (rdy[1] == thi) & (rdy[2] == tlo) & (rdy[3] == thi))) break;
        ld16_sc1_rw(rdy, roff, srd_ws);
        vmwait_pin<0>(rdy);
      }
    }
    STAMP(5);
    BARRIER();
    v4u vf[8 - PVS];
#pragma unroll
    for (int i = PVS; i < 8; ++i) vf[i - PVS] = tr_frag(L, tv0 + i * 8192, tv1 + i * 8192);
    SCHED();
#pragma unroll
    for (int i = PVS; i < 8; ++i) acc = mfma32(pa[i], vf[i - PVS], acc);
    const unsigned d0 = nosent(pk2(acc[0], dpp<SHL1>(acc[0])));
    st_sc1(rs, pao, v4u{d0, dppu<SHL2>(d0), ml[0], ml[1]});
  STAMP(6);

  if (__builtin_expect(is_wo, 1)) {
    v4u pv[1];
    STAMP(7);
    poll_fresh<1, DEPTH_A>(srd_ws, off, pv);
    STAMP(8);
    const float m0 = __uint_as_float(pv[0][2]);
    float mm = vmax(m0, dpp<ROR8>(m0));
    mm = x32_max(x16_max(mm));
    const float e0 = __builtin_amdgcn_exp2f((m0 - mm) * LOG2E);
    float lw = e0 * __uint_as_float(pv[0][3]);
    const auto s01 = __builtin_amdgcn_permlane32_swap(__float_as_uint(e0 * bflo(pv[0][0])), __float_as_uint(e0 * bfhi(pv[0][0])), false, false);
    const auto s23 = __builtin_amdgcn_permlane32_swap(__float_as_uint(e0 * bflo(pv[0][1])), __float_as_uint(e0 * bfhi(pv[0][1])), false, false);
    const float u0 = __uint_as_float(s01[0]) + __uint_as_float(s01[1]), u1 = __uint_as_float(s23[0]) + __uint_as_float(s23[1]);
    const auto su = __builtin_amdgcn_permlane16_swap(__float_as_uint(u0), __float_as_uint(u1), false, false);
    float ov = __uint_as_float(su[0]) + __uint_as_float(su[1]);
    ov += dpp<ROR8>(ov);
    lw += dpp<ROR8>(lw);
    lw = x32_sum(x16_sum(lw));
    *(LAS u16*)(L + ow) = (u16)pk2(ov * __builtin_amdgcn_rcpf(lw), 0.f);
    BARRIER();
    v4u oa[4], bf[4];
#pragma unroll
    for (int i = 0; i < 4; ++i) {
      oa[i] = *(LAS v4u*)(L + ob[i & 1] + (i >> 1) * 64);
      bf[i] = tr_frag(L, wb0[i & 1] + (i >> 1) * 16384, wb1[i & 1] + (i >> 1) * 16384);
    }
    SCHED();
    f32x4 hacc = {0.f, 0.f, 0.f, 0.f};
#pragma unroll
    for (int i = 0; i < 4; ++i) hacc = mfma32(oa[i], bf[i], hacc);
    st_sc1(rs, so, nosent4(hacc));
    SCHED();
    STAMP(9);
    poll_drain<1>(pv);
    EXIT();
  }
#pragma unroll
  for (int i = 0; i < 4; ++i) dma_glb<PF_NT>(ldst + L_K + 32768, wp0 + i * 8 * FF);
  if (is_ffn) {
    const u16* gup = ((lane >> 2) & 1 ? wu : wg) + (size_t)(64 * w + (lane >> 3)) * FF + 32 * jf + 8 * ((lane & 3) ^ (g & 1));
    const int alt = (lane & 2) ? -16 : 16;
    const v4u srd_d = make_srd(wd + (size_t)(32 * jf + 4 * w) * DM, 4 * DM * 2);
#define DOWNLOAD() do { _Pragma("unroll") for (int i = 0; i < 4; ++i) dma_buf<W2_NT>(ldst + L_K + i * 8192, vv0 + g * (DM * 2 - 256), srd_d, i * 256); } while (0)
    STAMP(20);
    DOWNLOAD();
    VMWAIT(4);
    BARRIER();
    STAMP(22);
#pragma unroll
    for (int i = 0; i < 8; ++i) dma_glb<GU_NT>(lbase + L_V + w * 8192 + i * 1024, gup + i * 8 * FF + ((i & 1) ? alt : 0));
    STAMP(10);
    *(LAS v2u*)(L + L_P + w * 128 + (lane & 15) * 8) = v2u{0x3F803F80u, 0x3F803F80u};
    v4u bd[4];
    int ao[4], so2 = WS_OP + jf * REC + w * 1024 + lane * 16;
#pragma unroll
    for (int i = 0; i < 4; ++i) { ao[i] = (n >> 2) == i ? L_HH + (n & 3) * 64 + g * 16 : L_ZERO; PIN(ao[i]); }
    int e0 = L_K + (w >> 1) * 8192 + (tr0 ^ (((8 * (w & 1)) ^ (2 * w)) << 4)), e1 = L_K + (w >> 1) * 8192 + (tr1 ^ (((8 * (w & 1)) ^ (2 * w)) << 4));
    PIN(so2); PIN(e0); PIN(e1);
#define DOWN() do { BARRIER(); v4u a4[4]; \
      _Pragma("unroll") for (int i = 0; i < 4; ++i) a4[i] = *(LAS v4u*)(L + ao[i]); \
      SCHED(); f32x4 d = {0.f, 0.f, 0.f, 0.f}; \
      _Pragma("unroll") for (int i = 0; i < 4; ++i) d = mfma32(a4[i], bd[i], d); \
      st_sc1(rs, so2, nosent4(d)); SCHED(); STAMP(9); } while (0)
#define BDLOAD() do { _Pragma("unroll") for (int i = 0; i < 4; ++i) bd[i] = tr_frag(L, e0 ^ (i << 5), e1 ^ (i << 5)); \
      PIN(bd[0]); PIN(bd[1]); PIN(bd[2]); PIN(bd[3]); } while (0)
    if (w == 4) {
      v4u hv[4];
      int hr = L_HN + (lane & 3) * 1088 + (lane >> 2) * 16, off1[4];
#pragma unroll
      for (int i = 0; i < 4; ++i) { off1[i] = WS_H + (16 * i + (lane >> 2)) * 1024 + kvh * 128 + (lane & 3) * 16; PIN(off1[i]); }
      PIN(hr);
      wait_ready(rs, lane, tlo, thi);
      VMWAIT(0);
      STAMP(11);
      BARRIER();
      STAMP(7);
      poll_fresh<4, DEPTH_H>(srd_ws, off1, hv);
      STAMP(8);
#pragma unroll
      for (int i = 0; i < 4; ++i) *(LAS v4u*)(L + hr + i * 256) = hv[i];
      BARRIER();
      float s0 = 0.f, s1 = 0.f, q0 = 0.f, q1 = 0.f;
#pragma unroll
      for (int i = 0; i < 4; ++i)
#pragma unroll
        for (int k = 0; k < 4; k += 2) {
          s0 = dot2(hv[i][k], 0x3F803F80u, s0); s1 = dot2(hv[i][k + 1], 0x3F803F80u, s1);
          q0 = dot2(hv[i][k], hv[i][k], q0); q1 = dot2(hv[i][k + 1], hv[i][k + 1], q1);
        }
      s0 += s1; q0 += q1;
      s0 += dpp<ROR4>(s0); q0 += dpp<ROR4>(q0);
      s0 += dpp<ROR8>(s0); q0 += dpp<ROR8>(q0);
      const float mean = x32_sum(x16_sum(s0)) * (1.0f / DM);
      const float q = fmaxf(fmaf(-mean * DM, mean, x32_sum(x16_sum(q0))), 0.f);
      const float inv = __builtin_amdgcn_rcpf(rb(__builtin_amdgcn_sqrtf(rb(rb(q * (1.0f / DM)) + 1e-5f))));
      *(LAS f32x2*)(L + L_RM + (lane & 3) * 8) = f32x2{rb(mean), inv};
      STAMP(27);
      BARRIER();
      BDLOAD();
      DOWN();
      poll_drain<4>(hv);
      EXIT();
    }
    VMWAIT(0);
    STAMP(11);
    BARRIER();
    if (w > 4) {
      BDLOAD();
      BARRIER();
      BARRIER();
      DOWN();
      EXIT();
    }
    {
      int uo = L_V + (8 * g + tq) * 128 + ((4 * (tp >> 1) + (w ^ (((tq >> 1) & 1) | ((g & 1) << 1)))) * 16) + 8 * (tp & 1);
      PIN(uo);
      v4u bf[16];
#pragma unroll
      for (int i = 0; i < 16; ++i) bf[i] = tr_frag(L, uo + i * 4096, uo + i * 4096 + 512);
      BDLOAD();
      int hb = (n & 3) == 1 ? L_P : L_HN + (n >> 2) * 1088 + g * 16, hw = n < 8 ? L_HH + g * 64 + (8 * w + n) * 2 : L_JUNK;
      PIN(hb); PIN(hw);
#pragma unroll
      for (int i = 0; i < 16; ++i) PIN(bf[i]);
      STAMP(25);
      BARRIER();
      STAMP(26);
      v4u ha[8];
#pragma unroll
      for (int i = 0; i < 8; ++i) ha[i] = *(LAS v4u*)(L + hb + i * 64);
      SCHED();
      f32x4 ac[2] = {{0.f, 0.f, 0.f, 0.f}, {0.f, 0.f, 0.f, 0.f}};
#pragma unroll
      for (int i = 0; i < 8; ++i) { ac[i & 1] = mfma32(ha[i], bf[i], ac[i & 1]); ha[i] = *(LAS v4u*)(L + hb + (8 + i) * 64); SCHED(); }
#pragma unroll
      for (int i = 0; i < 8; ++i) ac[i & 1] = mfma32(ha[i], bf[8 + i], ac[i & 1]);
      float acc0 = ac[0][0] + ac[1][0], cs = ac[0][1] + ac[1][1];
      PIN(acc0); PIN(cs);
      STAMP(28);
      BARRIER();
      const f32x2 st = *(LAS f32x2*)(L + L_RM + g * 8);
      const float a = rb(fmaf(-st[0], cs, acc0) * st[1]), u = dpp<ROR8>(a);
      const float sg = rb(__builtin_amdgcn_rcpf(1.0f + __builtin_amdgcn_exp2f(a * -LOG2E)));
      *(LAS u16*)(L + hw) = (u16)pk2(sg * u, 0.f);
      STAMP(29);
      DOWN();
      EXIT();
    }
  }
  if (is_red) {
    const int r2 = lane >> 3, k = lane & 7, bb = r2 & 3;
    const float xv = bflo(x[bb * DM + 8 * rho + k]);
    const unsigned pre = out[0];
    int off1[1] = {WS_HP + (8 * w + r2) * REC + (8 * rho + k) * 16}, off2[1] = {w < 7 ? WS_OP + (8 * w + r2) * REC + (8 * rho + k) * 16 : OOB};
    int ho = (w == 0 && lane < 32) ? WS_H + rho * 1024 + k * 128 + bb * 16 : OOB;
    int oo = (w == 0 && k == 0 && lane < 32) ? (bb * DM + 8 * rho) * 2 : OOB;
    int rw_ = L_P + (((((g & 1) << 1) | (g >> 1)) * 8 + k) * 8 + w) * 4, rr_ = L_P + (bb * 8 + k) * 32;
    PIN(ho); PIN(oo); PIN(rw_); PIN(rr_); PIN(off1[0]); PIN(off2[0]);
    asm volatile("" :: "v"(pre));
    if (w == 0) wait_ready(rs, lane, tlo, thi);
    BARRIER();
    STAMP(7);
#define PARTIAL_SUM(PV, AREA, RES) do { \
      const auto s01 = __builtin_amdgcn_permlane32_swap(PV[0], PV[1], false, false); \
      const auto s23 = __builtin_amdgcn_permlane32_swap(PV[2], PV[3], false, false); \
      const float u0 = __uint_as_float(s01[0]) + __uint_as_float(s01[1]), u1 = __uint_as_float(s23[0]) + __uint_as_float(s23[1]); \
      const auto su = __builtin_amdgcn_permlane16_swap(__float_as_uint(u0), __float_as_uint(u1), false, false); \
      float t_ = __uint_as_float(su[0]) + __uint_as_float(su[1]); \
      t_ += dpp<ROR8>(t_); \
      *(LAS float*)(L + rw_ + (AREA)) = t_; \
      BARRIER(); \
      const f32x4 ra = *(LAS f32x4*)(L + rr_ + (AREA)), rb_ = *(LAS f32x4*)(L + rr_ + (AREA) + 16); \
      RES = ((ra[0] + ra[1]) + (ra[2] + ra[3])) + ((rb_[0] + rb_[1]) + (rb_[2] + rb_[3])); } while (0)
    v4u pv[1], pw[1];
    poll_fresh<1, DEPTH_R>(srd_ws, off1, pv);
    STAMP(8);
    float av;
    PARTIAL_SUM(pv[0], 0, av);
    const float hval = rb(xv + rb(av));
    {
      const unsigned e0 = nosent(pk2(dpp<0xA0>(hval), dpp<0xF5>(hval)));
      const unsigned qa = dppu<0x00>(e0), qb2 = dppu<0xAA>(e0);
      const unsigned oa = dppu<0x141>(qa), ob2 = dppu<0x141>(qb2);
      const bool hi = k & 4;
      st_sc1(rs, ho, v4u{hi ? oa : qa, hi ? ob2 : qb2, hi ? qa : oa, hi ? qb2 : ob2});
    }
    SCHED();
    STAMP(9);
    poll_drain<1>(pv);
    __builtin_amdgcn_s_sleep(C_DELAY);
    poll_fresh<1, DEPTH_R>(srd_ws, off2, pw);
    STAMP(10);
    PARTIAL_SUM(pw[0], 1024, av);
    {
      const float ov = hval + rb(av);
      const unsigned dw = pk2(ov, dpp<SHL1>(ov));
      const unsigned dw2 = dppu<SHL2>(dw);
      const unsigned dw4 = dppu<SHL4>(dw), dw6 = dppu<SHL4>(dw2);
      __builtin_amdgcn_raw_buffer_store_b128(v4u{dw, dw2, dw4, dw6}, __builtin_amdgcn_make_buffer_rsrc(out, 0, 4 * DM * 2, 0x00020000), oo, 0, 0);
      st8_sc1_asm(v2u{tlo, thi}, (c == 192 && w == 0 && lane == 0) ? WS_EPOCH : OOB, srd_ws);
    }
    SCHED();
    STAMP(11);
    poll_drain<1>(pw);
  }
  STAMP_OUT();
}

}

extern "C" void kernel_launch(void* const* d_in, const int* in_sizes, int n_in, void* d_out, int out_size, void* d_ws, size_t ws_size,
                              hipStream_t stream) {
  (void)in_sizes; (void)n_in; (void)out_size; (void)ws_size;
  fused_block<<<256, NTHR, 0, stream>>>((const u16*)d_in[0], (const u16*)d_in[1], (const u16*)d_in[2], (const u16*)d_in[3], (const u16*)d_in[4],
                                        (const u16*)d_in[6], (const u16*)d_in[7], (const u16*)d_in[8], (u16*)d_out, (unsigned char*)d_ws);
}
```

```cpp
#define SKEW 0
#define SKC 192
#include <hip/hip_runtime.h>
#include <hip/hip_bf16.h>
#include <stdint.h>

namespace {

typedef unsigned short u16;
typedef unsigned long long u64;
typedef unsigned v4u __attribute__((ext_vector_type(4)));
typedef unsigned v2u __attribute__((ext_vector_type(2)));
typedef float f32x4 __attribute__((ext_vector_type(4)));
typedef float f32x2 __attribute__((ext_vector_type(2)));
typedef short bf16x8 __attribute__((ext_vector_type(8)));
typedef short s16x4 __attribute__((ext_vector_type(4)));
typedef __bf16 bf2 __attribute__((ext_vector_type(2)));
#define LAS __attribute__((address_space(3)))
#define GAS __attribute__((address_space(1)))

constexpr int DM = 512, HKV = 8, SK = 8192, DH = 128, WIN = 2048, FF = 1792, NQ = 4096;
constexpr int NTHR = 640;

constexpr int WS_EPOCH = 0;
constexpr int WS_RDY = 4096;
constexpr int WS_QG = 8192;
constexpr int WS_PA = WS_QG + 512 * 1024;
constexpr int WS_HP = WS_PA + 512 * 1024;
constexpr int REC = 8320;
constexpr int WS_H = WS_HP + 64 * REC;
constexpr int WS_OP = WS_H + 65536;
constexpr int WS_END = WS_OP + 56 * REC;
constexpr int OOB = 0x40000000;

constexpr int L_K = 0;
constexpr int L_V = 65536;
constexpr int L_WQ = 131072;
constexpr int L_HN = L_WQ;
constexpr int L_Q = L_WQ + 16384;
constexpr int L_XN = L_Q + 1280;
constexpr int L_O = L_XN;
constexpr int L_QP = L_XN + 1280;
constexpr int L_P = L_QP + 1024;
constexpr int L_RM = L_P + 2304;
constexpr int L_RL = L_RM + 128;
constexpr int L_HH = L_RL + 128;
constexpr int L_ZERO = L_HH + 256;
constexpr int L_FLAG = L_ZERO + 128;
constexpr int L_JUNK = L_FLAG + 16;
constexpr int L_BYTES = L_JUNK + 2048;

constexpr unsigned SENT = 0xFFFFFFFFu;
constexpr u64 TAG_STEP = 0x9E3779B97F4A7C15ull;
constexpr int SPIN_CAP = 1 << 16;
constexpr int AUX_SC1 = 16;
constexpr float LOG2E = 1.4426950408889634f;
constexpr unsigned ONES2 = 0x3F803F80u;
#define CODE_BYTES 12288
#define QG_DELAY 28

__device__ __forceinline__ float bflo(unsigned x) { return __builtin_bit_cast(float, x << 16); }
__device__ __forceinline__ float bfhi(unsigned x) { return __builtin_bit_cast(float, x & 0xffff0000u); }
__device__ __forceinline__ unsigned pk2(float lo, float hi) {
  bf2 v = {(__bf16)lo, (__bf16)hi};
  return __builtin_bit_cast(unsigned, v);
}
__device__ __forceinline__ float rb(float x) { return (float)(__bf16)x; }
__device__ __forceinline__ unsigned nosent(unsigned x) { return x < 0xFFFFFFFEu ? x : 0xFFFFFFFEu; }
__device__ __forceinline__ v4u nosent4(f32x4 v) {
  return v4u{nosent(__float_as_uint(v[0])), nosent(__float_as_uint(v[1])), nosent(__float_as_uint(v[2])), nosent(__float_as_uint(v[3]))};
}
__device__ __forceinline__ float vmax(float a, float b) { float r; asm("v_max_f32 %0, %1, %2" : "=v"(r) : "v"(a), "v"(b)); return r; }
__device__ __forceinline__ float vmax3(float a, float b, float c) { float r; asm("v_max3_f32 %0, %1, %2, %3" : "=v"(r) : "v"(a), "v"(b), "v"(c)); return r; }
__device__ __forceinline__ void row_max2(float& a, float& b) {
  asm("s_nop 1\n\t"
      "v_max_f32_dpp %0, %0, %0 row_ror:8 row_mask:0xf bank_mask:0xf\n\tv_max_f32_dpp %1, %1, %1 row_ror:8 row_mask:0xf bank_mask:0xf\n\ts_nop 0\n\t"
      "v_max_f32_dpp %0, %0, %0 row_ror:4 row_mask:0xf bank_mask:0xf\n\tv_max_f32_dpp %1, %1, %1 row_ror:4 row_mask:0xf bank_mask:0xf\n\ts_nop 0\n\t"
      "v_max_f32_dpp %0, %0, %0 row_ror:2 row_mask:0xf bank_mask:0xf\n\tv_max_f32_dpp %1, %1, %1 row_ror:2 row_mask:0xf bank_mask:0xf\n\ts_nop 0\n\t"
      "v_max_f32_dpp %0, %0, %0 row_ror:1 row_mask:0xf bank_mask:0xf\n\tv_max_f32_dpp %1, %1, %1 row_ror:1 row_mask:0xf bank_mask:0xf\n\ts_nop 1"
      : "+v"(a), "+v"(b));
}
__device__ __forceinline__ float dot2(unsigned a, unsigned b, float c) {
  return __builtin_amdgcn_fdot2_f32_bf16(__builtin_bit_cast(bf2, a), __builtin_bit_cast(bf2, b), c, false);
}
__device__ __forceinline__ f32x4 mfma32(v4u a, v4u b, f32x4 c) {
  return __builtin_amdgcn_mfma_f32_16x16x32_bf16(__builtin_bit_cast(bf16x8, a), __builtin_bit_cast(bf16x8, b), c, 0, 0, 0);
}
template <int CTRL> __device__ __forceinline__ float dpp(float x) {
  return __builtin_bit_cast(float, __builtin_amdgcn_mov_dpp(__builtin_bit_cast(int, x), CTRL, 0xf, 0xf, true));
}
template <int CTRL> __device__ __forceinline__ unsigned dppu(unsigned x) { return (unsigned)__builtin_amdgcn_mov_dpp((int)x, CTRL, 0xf, 0xf, true); }
constexpr int ROR8 = 0x128, ROR4 = 0x124, ROR2 = 0x122, ROR1 = 0x121, SHL1 = 0x101, SHL2 = 0x102, SHL4 = 0x104;
__device__ __forceinline__ float row_sum(float x) { x += dpp<ROR8>(x); x += dpp<ROR4>(x); x += dpp<ROR2>(x); return x + dpp<ROR1>(x); }
__device__ __forceinline__ float swap16_add(float a, float b) {
  const auto s = __builtin_amdgcn_permlane16_swap(__float_as_uint(a), __float_as_uint(b), false, false);
  return __uint_as_float(s[0]) + __uint_as_float(s[1]);
}
__device__ __forceinline__ float swap32_add(float a, float b) {
  const auto s = __builtin_amdgcn_permlane32_swap(__float_as_uint(a), __float_as_uint(b), false, false);
  return __uint_as_float(s[0]) + __uint_as_float(s[1]);
}
__device__ __forceinline__ float x16_max(float x) {
  const auto s = __builtin_amdgcn_permlane16_swap(__float_as_uint(x), __float_as_uint(x), false, false);
  return vmax(__uint_as_float(s[0]), __uint_as_float(s[1]));
}
__device__ __forceinline__ float x32_max(float x) {
  const auto s = __builtin_amdgcn_permlane32_swap(__float_as_uint(x), __float_as_uint(x), false, false);
  return vmax(__uint_as_float(s[0]), __uint_as_float(s[1]));
}
__device__ __forceinline__ float wave_sum(float x) { x = row_sum(x); x = swap16_add(x, x); return swap32_add(x, x); }
__device__ __forceinline__ float sum8_scatter4(float a, float b, float c, float d) {
  const float t = swap16_add(swap32_add(a, b), swap32_add(c, d));
  return t + dpp<ROR8>(t);
}
__device__ __forceinline__ f32x4 sel2(bool c, f32x4 a, f32x4 b) {
  return f32x4{c ? b[0] : a[0], c ? b[1] : a[1], c ? b[2] : a[2], c ? b[3] : a[3]};
}
__device__ __forceinline__ float ln_inv(float q) {
  return __builtin_amdgcn_rcpf(rb(__builtin_amdgcn_sqrtf(rb(rb(q * (1.0f / DM)) + 1e-5f))));
}

#define PIN(x) asm volatile("" : "+v"(x))
#define SCHED() __builtin_amdgcn_sched_barrier(0)
#define LDS_FENCE() asm volatile("s_waitcnt lgkmcnt(0)" ::: "memory")
#define BARRIER() do { asm volatile("s_waitcnt lgkmcnt(0)\n\ts_barrier" ::: "memory"); SCHED(); } while (0)
#define VMWAIT(N) asm volatile("s_waitcnt vmcnt(" #N ")" ::: "memory")
#define STAMP(i) do { if ((i) == 0) { u64 t_; asm volatile("s_memrealtime %0\n\ts_waitcnt lgkmcnt(0)" : "=s"(t_)); mini0 = (unsigned)t_; } } while (0)
#define STAMP_OUT() do { if (w == 9) __hip_atomic_store((GAS unsigned*)(ws + (4 << 20)) + c * 32, mini0, __ATOMIC_RELAXED, __HIP_MEMORY_SCOPE_AGENT); \
  if (w == 0 && c == SKC) { unsigned mn = 0x7fffffffu, mx = 0; for (int z = 0; z < 4; ++z) { unsigned v = __hip_atomic_load((GAS unsigned*)(ws + (4 << 20)) + (lane + 64 * z) * 32, __ATOMIC_RELAXED, __HIP_MEMORY_SCOPE_AGENT) - mini0 + 1000u; mn = min(mn, v); mx = max(mx, v); } \
    for (int o = 32; o >= 1; o >>= 1) { mn = min(mn, (unsigned)__shfl_xor((int)mn, o)); mx = max(mx, (unsigned)__shfl_xor((int)mx, o)); } \
    mn = __builtin_amdgcn_readfirstlane(mn); mx = __builtin_amdgcn_readfirstlane(mx); \
      \
    const unsigned dl = (SKEW == 0 ? mn : mx) + 3000u; \
    while ((unsigned)__builtin_amdgcn_s_memrealtime() - mini0 < dl) __builtin_amdgcn_s_sleep(1); } } while (0)
#define EXIT() do { STAMP_OUT(); asm volatile("s_endpgm" ::: "memory"); __builtin_unreachable(); } while (0)

__device__ __forceinline__ v4u make_srd(const void* p, unsigned bytes) {
  const u64 a = (u64)p;
  return v4u{(unsigned)a, (unsigned)(a >> 32) & 0xffffu, bytes, 0x00020000u};
}
__device__ __forceinline__ void dma_buf(unsigned lds, int voff, v4u srd, int soff) {
  asm volatile("s_mov_b32 m0, %0\n\ts_nop 0\n\tbuffer_load_dwordx4 %1, %2, %3 offen nt lds" :: "s"(lds), "v"(voff), "s"(srd), "s"(soff) : "m0", "memory");
}
template <int NT> __device__ __forceinline__ void dma_glb(unsigned lds, const void* p) {
  if (NT) asm volatile("s_mov_b32 m0, %0\n\ts_nop 0\n\tglobal_load_lds_dwordx4 %1, off nt" :: "s"(lds), "v"(p) : "m0", "memory");
  else asm volatile("s_mov_b32 m0, %0\n\ts_nop 0\n\tglobal_load_lds_dwordx4 %1, off" :: "s"(lds), "v"(p) : "m0", "memory");
}
__device__ __forceinline__ void ld16_asm(v4u& d, int voff, v4u srd, int soff) {
  asm volatile("buffer_load_dwordx4 %0, %1, %2, %3 offen" : "=v"(d) : "v"(voff), "s"(srd), "s"(soff) : "memory");
}
__device__ __forceinline__ void ld16_sc1_asm(v4u& d, int voff, v4u srd) {
  asm volatile("buffer_load_dwordx4 %0, %1, %2, 0 offen sc1" : "=v"(d) : "v"(voff), "s"(srd) : "memory");
}
__device__ __forceinline__ void ld8_sc1_asm(v2u& d, v4u srd) {
  asm volatile("buffer_load_dwordx2 %0, off, %1, 0 sc1" : "=v"(d) : "s"(srd) : "memory");
}
__device__ __forceinline__ void st16_sc1_asm(v4u v, int voff, v4u srd) {
  asm volatile("buffer_store_dwordx4 %0, %1, %2, 0 offen sc1\n\ts_nop 1" :: "v"(v), "v"(voff), "s"(srd) : "memory");
}
__device__ __forceinline__ void st8_sc1_asm(v2u v, int voff, v4u srd) {
  asm volatile("buffer_store_dwordx2 %0, %1, %2, 0 offen sc1" :: "v"(v), "v"(voff), "s"(srd) : "memory");
}
__device__ __forceinline__ v4u ld_sc1(__amdgpu_buffer_rsrc_t rs, int off) { return __builtin_amdgcn_raw_buffer_load_b128(rs, off, 0, AUX_SC1); }
__device__ __forceinline__ void st_sc1(__amdgpu_buffer_rsrc_t rs, int off, v4u v) { __builtin_amdgcn_raw_buffer_store_b128(v, rs, off, 0, AUX_SC1); }
__device__ __forceinline__ v2u tr_rd(LAS unsigned char* L, int off) {
  return __builtin_bit_cast(v2u, __builtin_amdgcn_ds_read_tr16_b64_v4i16((LAS s16x4*)(L + off)));
}
__device__ __forceinline__ v4u tr_frag(LAS unsigned char* L, int off0, int off1) {
  const v2u a = tr_rd(L, off0), b = tr_rd(L, off1);
  return v4u{a[0], a[1], b[0], b[1]};
}

__device__ __forceinline__ v4u poll_slot(v4u srd, int off) {
  v4u v;
  unsigned t, cnt;
  u64 miss;
  asm volatile(
      "s_mov_b32 %2, %6\n\ts_mov_b64 %3, -1\n\ts_branch 2f\n"
      "1:\n\ts_sleep 1\n"
      "2:\n\ts_mov_b64 exec, %3\n\tbuffer_load_dwordx4 v[96:99], %4, %5, 0 offen sc1\n\ts_mov_b64 exec, -1\n\t"
      "s_sub_u32 %2, %2, 1\n\ts_waitcnt vmcnt(0)\n\ts_cbranch_scc1 3f\n\t"
      "v_max_u32 %1, v96, v97\n\tv_max3_u32 %1, %1, v98, v99\n\tv_cmp_eq_u32 %3, -1, %1\n\ts_cmp_lg_u64 %3, 0\n\ts_cbranch_scc1 1b\n"
      "3:"
      : "=&{v[96:99]}"(v), "=&v"(t), "=&s"(cnt), "=&s"(miss) : "v"(off), "s"(srd), "n"(SPIN_CAP) : "scc", "memory");
  return v;
}
__device__ __forceinline__ void wait_ready(__amdgpu_buffer_rsrc_t rs, int lane, unsigned tlo, unsigned thi) {
#pragma unroll 1
  for (int it = 0; it < SPIN_CAP; ++it) {
    asm volatile("" ::: "memory");
    const v4u r0 = ld_sc1(rs, WS_RDY + lane * 16), r1 = ld_sc1(rs, WS_RDY + 1024 + lane * 16);
    const unsigned bad = ((r0[0] ^ tlo) | (r0[1] ^ thi) | (r0[2] ^ tlo)) | ((r0[3] ^ thi) | (r1[0] ^ tlo) | (r1[1] ^ thi)) | ((r1[2] ^ tlo) | (r1[3] ^ thi));
    if (__builtin_amdgcn_ballot_w64(bad != 0u) == 0) break;
    __builtin_amdgcn_s_sleep(4);
  }
}

__global__ void __launch_bounds__(NTHR, 3)
fused_block(const u16* __restrict__ x, const u16* __restrict__ kc, const u16* __restrict__ vc, const u16* __restrict__ wq,
            const u16* __restrict__ wo, const u16* __restrict__ wg, const u16* __restrict__ wu, const u16* __restrict__ wd,
            u16* __restrict__ out, unsigned char* __restrict__ ws) {
  __shared__ __attribute__((aligned(16))) unsigned char smem_[L_BYTES];
  {
    u64 pc0;
    asm volatile("s_getpc_b64 %0" : "=s"(pc0));
    if (__builtin_amdgcn_readfirstlane(threadIdx.x >> 6) == 1) {
      const v4u srd_c = make_srd((const void*)(pc0 & ~255ull), CODE_BYTES);
      const int so = ((blockIdx.x >> 3) & 15) * 2048;
      asm volatile("s_mov_b32 m0, %0\n\ts_nop 0\n\tbuffer_load_dwordx4 %1, %2, %3 offen lds\n\tbuffer_load_dwordx4 %1, %2, %3 offen offset:1024 lds"
                   :: "s"((unsigned)(size_t)(LAS unsigned char*)smem_ + L_JUNK), "v"((threadIdx.x & 63) * 16), "s"(srd_c), "s"(so) : "m0", "memory");
    }
    SCHED();
  }
  LAS unsigned char* L = (LAS unsigned char*)smem_;
  const unsigned lbase = (unsigned)(size_t)L;
  const int c = blockIdx.x, kvh = c & 7, i32 = c >> 3;
  const int lane = threadIdx.x & 63, wv = __builtin_amdgcn_readfirstlane(threadIdx.x >> 6);
  const int w = wv < 2 ? 8 + wv : wv - 2;
  const int n = lane & 15, g = lane >> 4;
  const v4u srd_ws = make_srd(ws, WS_END);
  unsigned mini0 = 0, mini1 = 0;
  STAMP(0);

  const int b = i32 >> 3, s = i32 & 7;
  const size_t kvbase = ((size_t)(b * HKV + kvh) * SK + (SK - WIN) + s * 256) * DH;
  const v4u srd_k = make_srd(kc + kvbase, 65536);
  const int rot = w + 37 * c;
  const int kv0 = g * 256 + ((n ^ g) * 16);
  const unsigned ldst = lbase + w * 1024;
  v2u ep;
  v4u xr[4];
#define KLOAD(i) dma_buf(ldst + L_K + (i) * 8192, kv0 ^ (((i) & 3) << 6), srd_k, ((8 * (i) + rot) & 63) << 10)
  if (wv == 0) {
    const v4u srd_x = make_srd(x, 4 * DM * 2);
    const v4u srd_wq = make_srd(wq + (size_t)b * 128 * NQ + kvh * 512 + s * 64, 128 * NQ * 2);
    ld8_sc1_asm(ep, srd_ws);
#pragma unroll
    for (int i = 0; i < 4; ++i) ld16_asm(xr[i], g * 1024 + n * 16, srd_x, ((b + i) & 3) * 256);
    const int row = lane >> 3, v0 = row * (NQ * 2) + (((lane & 7) ^ (2 * ((row >> 1) & 1))) * 16);
    int so;
#define WQ2 "buffer_load_dwordx4 %1, %3, %0 offen nt lds\n\ts_addk_i32 m0, 0x400\n\ts_add_u32 %0, %0, %5\n\t" \
            "buffer_load_dwordx4 %2, %3, %0 offen nt lds\n\ts_addk_i32 m0, 0x400\n\ts_add_u32 %0, %0, %5\n\t"
    asm volatile("s_mov_b32 m0, %4\n\ts_mov_b32 %0, 0\n\t" WQ2 WQ2 WQ2 WQ2 WQ2 WQ2 WQ2 WQ2
                 : "=&s"(so) : "v"(v0), "v"(v0 ^ 64), "s"(srd_wq), "s"(lbase + L_WQ), "s"(8 * NQ * 2) : "m0", "scc", "memory");
    *(LAS unsigned*)(L + L_FLAG) = 0u;
    LDS_FENCE();
  }
  asm volatile("s_barrier" ::: "memory");
  if (wv >= 2) { KLOAD(0); KLOAD(1); }
  int wv2 = wv;
  asm volatile("" : "+s"(wv2));

  if (wv2 == 1) {
    v2u ep1;
    ld8_sc1_asm(ep1, srd_ws);
    const v4u sent = {SENT, SENT, SENT, SENT};
    st16_sc1_asm(sent, WS_PA + kvh * 65536 + g * 8192 + i32 * 256 + n * 16, srd_ws);
    st16_sc1_asm(sent, WS_PA + kvh * 65536 + (g + 4) * 8192 + i32 * 256 + n * 16, srd_ws);
    const int base = i32 < 16 ? WS_HP + (kvh * 8 + (i32 >> 1)) * REC + (i32 & 1) * 4096 : WS_OP + (kvh * 7 + i32 - 16) * REC;
    const int len = i32 < 16 ? 4096 : i32 < 23 ? 8192 : 0;
#pragma unroll 1
    for (int o = 0; o < len; o += 1024) st16_sc1_asm(sent, base + o + lane * 16, srd_ws);
    st16_sc1_asm(sent, (i32 >= 24 && lane < 32) ? WS_H + ((i32 - 24) * 8 + kvh) * 1024 + (lane >> 2) * 128 + (lane & 3) * 16 : OOB, srd_ws);
    *(LAS v4u*)(L + L_ZERO + (lane & 7) * 16) = v4u{0u, 0u, 0u, 0u};
    asm volatile("s_waitcnt vmcnt(0)" : "+v"(ep1) :: "memory");
    const u64 tag = (((u64)ep1[1] << 32) | ep1[0]) + TAG_STEP;
    st8_sc1_asm(v2u{(unsigned)tag, (unsigned)(tag >> 32)}, lane == 0 ? WS_RDY + (kvh * 32 + i32) * 8 : OOB, srd_ws);
    STAMP(1);
    EXIT();
  }
  if (wv2 == 0) {
    STAMP(7);
    asm volatile("s_waitcnt vmcnt(16)" : "+v"(ep), "+v"(xr[0]), "+v"(xr[1]), "+v"(xr[2]), "+v"(xr[3]) :: "memory");
    STAMP(9);
    {
      float s0 = 0.f, s1 = 0.f, q0 = 0.f, q1 = 0.f;
#pragma unroll
      for (int i = 0; i < 4; ++i)
#pragma unroll
        for (int k = 0; k < 4; k += 2) {
          s0 = dot2(xr[i][k], ONES2, s0); s1 = dot2(xr[i][k + 1], ONES2, s1);
          q0 = dot2(xr[i][k], xr[i][k], q0); q1 = dot2(xr[i][k + 1], xr[i][k + 1], q1);
        }
      const float ml = (s0 + s1) * (1.0f / 32), pv = dpp<0x150>(ml), dl = ml - pv;
      const float dm = row_sum(dl) * (1.0f / 16), mean = pv + dm;
      const float inv = ln_inv(fmaxf(fmaf(-dm * DM, dm, row_sum(fmaf(32.f * dl, dl, fmaf(-32.f * ml, ml, q0 + q1)))), 0.f));
      const float mb = rb(mean);
      v4u o;
#pragma unroll
      for (int k = 0; k < 4; ++k) {
        const unsigned d = pk2(bflo(xr[0][k]) - mb, bfhi(xr[0][k]) - mb);
        o[k] = pk2(bflo(d) * inv, bfhi(d) * inv);
      }
      *(LAS v4u*)(L + L_XN + g * 320 + n * 16) = o;
    }
    STAMP(1);
    LDS_FENCE();
    f32x4 acc[4];
    {
      v4u xa[4];
#pragma unroll
      for (int ks = 0; ks < 4; ++ks) xa[ks] = *(LAS v4u*)(L + L_XN + (n & 3) * 320 + ks * 64 + g * 16);
      const int q4 = n >> 2, p = n & 3, y = ((q4 >> 1) & 1) | ((g & 1) << 1);
      const int rbase = L_WQ + (8 * g + q4) * 128 + 8 * (p & 1);
      int o[4];
#pragma unroll
      for (int t = 0; t < 4; ++t) { o[t] = rbase + (((2 * t + (p >> 1)) ^ (2 * y)) * 16); acc[t] = f32x4{0.f, 0.f, 0.f, 0.f}; }
#pragma unroll
      for (int ks = 0; ks < 4; ++ks) {
        if (ks == 0) VMWAIT(12); else if (ks == 1) VMWAIT(8); else if (ks == 2) VMWAIT(4); else VMWAIT(0);
        if (ks == 3) STAMP(4);
#pragma unroll
        for (int t = 0; t < 4; ++t) acc[t] = mfma32(xa[ks], tr_frag(L, o[t] + ks * 4096, o[t] + ks * 4096 + 512), acc[t]);
      }
    }
    *(LAS f32x4*)(L + L_QP + lane * 16) = sel2(g & 2, sel2(g & 1, acc[0], acc[1]), sel2(g & 1, acc[2], acc[3]));
    LDS_FENCE();
    const f32x4 q0 = *(LAS f32x4*)(L + L_QP + (lane & 31) * 32), q1 = *(LAS f32x4*)(L + L_QP + (lane & 31) * 32 + 16);
    const u64 tag = (((u64)ep[1] << 32) | ep[0]) + TAG_STEP;
    const unsigned tlo = (unsigned)tag, thi = (unsigned)(tag >> 32);
    STAMP(2);
    {
      const bool up = lane >= 32;
      const int o = WS_QG + ((kvh * 4 + (up ? 1 : 0)) * 4 + b) * 4096 + (32 * s + (lane & 31)) * 16;
      st16_sc1_asm(v4u{__float_as_uint(up ? q0[1] : q0[0]), tlo, __float_as_uint(up ? q1[1] : q1[0]), thi}, o, srd_ws);
      st16_sc1_asm(v4u{__float_as_uint(up ? q0[3] : q0[2]), tlo, __float_as_uint(up ? q1[3] : q1[2]), thi}, o + 2 * 4 * 4096, srd_ws);
    }
    STAMP(3);
    __builtin_amdgcn_s_sleep(QG_DELAY);
    const int qo = WS_QG + (kvh * 4 + b) * 16384 + lane * 16;
    v4u qg[16];
    u64 miss[16];
#pragma unroll
    for (int i = 0; i < 16; ++i) { asm volatile("" : "=v"(qg[i])); miss[i] = ~0ull; }
#pragma unroll 1
    for (int it = 0; it < SPIN_CAP; ++it) {
#pragma unroll
      for (int i = 0; i < 16; ++i)
        asm volatile("s_mov_b64 exec, %4\n\tbuffer_load_dwordx4 %0, %1, %2, %3 offen offset:%5 sc1\n\ts_mov_b64 exec, -1"
                     : "+v"(qg[i]) : "v"(qo), "s"(srd_ws), "s"((i >> 2) * 4096), "s"(miss[i]), "n"((i & 3) * 1024) : "memory");
      VMWAIT(0);
      u64 any = 0;
#pragma unroll
      for (int i = 0; i < 16; ++i) {
        PIN(qg[i]);
        miss[i] = __builtin_amdgcn_ballot_w64(qg[i][1] != tlo) | __builtin_amdgcn_ballot_w64(qg[i][3] != thi);
        any |= miss[i];
      }
      if (any == 0) break;
    }
#pragma unroll
    for (int u = 0; u < 4; ++u) {
      const float a = (__uint_as_float(qg[u][0]) + __uint_as_float(qg[4 + u][0])) + (__uint_as_float(qg[8 + u][0]) + __uint_as_float(qg[12 + u][0]));
      const float bq = (__uint_as_float(qg[u][2]) + __uint_as_float(qg[4 + u][2])) + (__uint_as_float(qg[8 + u][2]) + __uint_as_float(qg[12 + u][2]));
      *(LAS unsigned*)(L + L_Q + u * 320 + lane * 4) = pk2(a, bq);
    }
    *(LAS v2u*)(L + L_FLAG + 8) = v2u{tlo, thi};
    LDS_FENCE();
    *(volatile LAS unsigned*)(L + L_FLAG) = 1u;
    STAMP(6);
    EXIT();
  }

  const bool is_wo = i32 < 16, is_ffn = i32 >= 16 && i32 < 23, is_red = i32 >= 24;
  const int jf = kvh * 7 + (i32 - 16);
  const int rho = (i32 - 24) * 8 + kvh;
  const __amdgpu_buffer_rsrc_t rs = __builtin_amdgcn_make_buffer_rsrc(ws, 0, WS_END, 0x00020000);
  const v4u srd_v = make_srd(vc + kvbase, 65536);
  const int vv0 = g * 256 + ((n ^ ((g << 2) | (w & 3))) * 16);
  asm volatile("" :: "s"(wo), "s"(wg), "s"(wu), "s"(wd), "s"(out), "s"(x));
#define VLOAD(i) dma_buf(ldst + L_V + (i) * 8192, vv0, srd_v, ((8 * (i) + rot) & 63) << 10)
#define WW() VMWAIT(1)
  WW();
  STAMP(12);
  KLOAD(2); WW(); KLOAD(3); WW(); KLOAD(4); WW(); KLOAD(5); WW(); KLOAD(6); WW(); KLOAD(7);
  STAMP(1);
  WW(); VLOAD(0); WW(); VLOAD(1); VLOAD(2);
  VMWAIT(3);
  v4u kf[8];
  {
    const int kb = L_K + (n >> 2) * 8192 + w * 1024 + (n & 3) * 256 + ((g ^ n) * 16);
#pragma unroll
    for (int ks = 0; ks < 4; ++ks) { kf[ks] = *(LAS v4u*)(L + (kb ^ (ks << 6))); kf[4 + ks] = *(LAS v4u*)(L + (kb ^ (ks << 6)) + 32768); }
  }
  int qb = L_Q + (2 * (n >> 3) + (n & 1)) * 320 + g * 16;
  int rmw = L_RM + (g >> 1) * 64 + w * 8, rmr = L_RM + (g >> 1) * 64;
  int po = L_P + (g >> 1) * 1152 + ((4 * (g & 1) + (n >> 2)) * 32 + 4 * w + (n & 3)) * 2;
  const u16* wp0 = is_wo ? wo + (size_t)(kvh * 512 + (2 * (i32 >> 3) + (w >> 2)) * 128 + 32 * ((i32 >> 1) & 3) + 4 * (w & 3)) * DM + (i32 & 1) * 256 + g * (DM - 128) + (vv0 >> 1)
                 : is_ffn ? wd + (size_t)(32 * jf + 4 * w) * DM + g * (DM - 128) + (vv0 >> 1)
                 : is_red ? (lane >= 32 ? wu : wg) + (size_t)(64 * w + (i32 & 7)) * FF + ((224 * kvh) & ~63) + 8 * (lane & 31) : x;
  const int ws1 = i32 < 23 ? 128 : 0, ws2 = is_wo ? 16 * DM : is_ffn ? 256 : 0;
  const int tq = n >> 2, tp = n & 3;
  int tr0 = (8 * g + tq) * 256 + (((2 * w + (tp >> 1)) ^ ((tq << 2) | ((2 * g) & 3))) * 16) + 8 * (tp & 1);
  int tr1 = (8 * g + 4 + tq) * 256 + (((2 * w + (tp >> 1)) ^ ((tq << 2) | ((2 * g + 1) & 3))) * 16) + 8 * (tp & 1);
  PIN(wp0); PIN(tr0); PIN(tr1);
  int tv0 = L_V + tr0, tv1 = L_V + tr1, pab = L_P + (n >> 2) * 576 + g * 16;
  int pao = (n & 3) == 0 ? WS_PA + kvh * 65536 + w * 8192 + i32 * 256 + g * 64 + (n >> 2) * 16 : OOB;
  int pai = WS_PA + kvh * 65536 + (2 * ((i32 >> 1) & 3) + (w & 1)) * 8192 + (8 * (w >> 1) + (lane >> 3)) * 256 + (i32 >> 3) * 128 + (lane & 7) * 16;
  int ob[2], wb0[2], wb1[2];
#pragma unroll
  for (int i = 0; i < 2; ++i) {
    ob[i] = (n >> 2) == i ? L_O + (n & 3) * 320 + g * 16 : L_ZERO;
    const int xo = ((((4 * w + 2 * i) & 15) ^ (2 * w)) << 4);
    wb0[i] = L_K + (w >> 2) * 8192 + (tr0 ^ xo); wb1[i] = L_K + (w >> 2) * 8192 + (tr1 ^ xo);
    PIN(ob[i]); PIN(wb0[i]); PIN(wb1[i]);
  }
  int ow = L_O + (w >> 1) * 320 + (32 * (w & 1) + 4 * (n & 7) + (((g & 1) << 1) | (g >> 1))) * 2;
  int hpo = lane < 32 ? WS_HP + (kvh * 8 + (i32 >> 1)) * REC + ((i32 & 1) * 256 + 32 * w + lane) * 16 : OOB;
  const bool chk = is_wo && w == 0;
  int rdo = chk ? WS_RDY + kvh * 256 + n * 16 : OOB;
  PIN(tv0); PIN(tv1); PIN(pab); PIN(qb); PIN(rmw); PIN(rmr); PIN(po); PIN(pao); PIN(pai); PIN(ow); PIN(hpo); PIN(rdo);
#pragma unroll
  for (int i = 0; i < 8; ++i) PIN(kf[i]);
#define QFLAG() (__builtin_amdgcn_readfirstlane(*(volatile LAS unsigned*)(L + L_FLAG)) != 0u)
  int vi = 3;
#pragma unroll 1
  while (vi < 8 && (vi <= 3 || !QFLAG())) { VMWAIT(3); VLOAD(vi); ++vi; }
  STAMP(13);
#pragma unroll 1
  for (int it = 0; it < (SPIN_CAP << 6); ++it) {
    if (QFLAG()) break;
  }
  asm volatile("" ::: "memory");
  const v2u tagv = *(LAS v2u*)(L + L_FLAG + 8);
  const unsigned tlo = tagv[0], thi = tagv[1];
  STAMP(3);
  float sc[2];
  {
    v4u qa[4];
#pragma unroll
    for (int ks = 0; ks < 4; ++ks) qa[ks] = *(LAS v4u*)(L + qb + ks * 64);
    f32x4 d0 = {0.f, 0.f, 0.f, 0.f}, d1 = {0.f, 0.f, 0.f, 0.f};
#pragma unroll
    for (int ks = 0; ks < 4; ++ks) { d0 = mfma32(qa[ks], kf[ks], d0); d1 = mfma32(qa[ks], kf[4 + ks], d1); }
#pragma unroll
    for (int j = 0; j < 2; ++j) sc[j] = rb(rb((g & 1) ? d1[j] : d0[j]) * 0.08838834764831845f);
    float m0 = sc[0], m1 = sc[1];
    row_max2(m0, m1);
    const f32x2 mx = {x16_max(m0), x16_max(m1)};
    *(LAS f32x2*)(L + rmw) = mx;
  }
#pragma unroll 1
  while (vi < 8) { VLOAD(vi); ++vi; }
  v4u rdy;
  ld16_sc1_asm(rdy, rdo, srd_ws);
  STAMP(2);
  BARRIER();
  STAMP(16);
  float mh;
  {
    f32x4 t[4];
#pragma unroll
    for (int i = 0; i < 4; ++i) t[i] = *(LAS f32x4*)(L + rmr + i * 16);
    const float M0 = vmax3(vmax3(t[0][0], t[0][2], t[1][0]), vmax3(t[1][2], t[2][0], t[2][2]), vmax(t[3][0], t[3][2]));
    const float M1 = vmax3(vmax3(t[0][1], t[0][3], t[1][1]), vmax3(t[1][3], t[2][1], t[2][3]), vmax(t[3][1], t[3][3]));
    const float p0 = __builtin_amdgcn_exp2f((sc[0] - M0) * LOG2E), p1 = __builtin_amdgcn_exp2f((sc[1] - M1) * LOG2E);
    const unsigned pp = pk2(p0, p1);
    *(LAS u16*)(L + po) = (u16)pp;
    *(LAS u16*)(L + po + 576) = (u16)(pp >> 16);
    const float l0 = row_sum(p0), l1 = row_sum(p1);
    *(LAS f32x2*)(L + rmw + (L_RL - L_RM)) = f32x2{swap16_add(l0, l0), swap16_add(l1, l1)};
    mh = (g & 1) ? M1 : M0;
  }
  STAMP(17);
  if (is_red) {
#pragma unroll
    for (int i = 0; i < 4; ++i) dma_glb<0>(ldst + L_K, wp0 + i * 8 * FF);
  } else {
#pragma unroll
    for (int i = 0; i < 4; ++i) dma_glb<1>(ldst + L_K + i * 8192, wp0 + (i >> 1) * ws2 + (i & 1) * ws1);
  }
  VMWAIT(9);
  STAMP(14);
  BARRIER();
  {
    v4u pa[8], vf[4];
#pragma unroll
    for (int i = 0; i < 4; ++i) { pa[i] = *(LAS v4u*)(L + pab + i * 64); vf[i] = tr_frag(L, tv0 + i * 8192, tv1 + i * 8192); }
    f32x4 t[4];
#pragma unroll
    for (int i = 0; i < 4; ++i) t[i] = *(LAS f32x4*)(L + rmr + (L_RL - L_RM) + i * 16);
#pragma unroll
    for (int i = 4; i < 8; ++i) pa[i] = *(LAS v4u*)(L + pab + i * 64);
    SCHED();
    f32x4 acc = {0.f, 0.f, 0.f, 0.f};
#pragma unroll
    for (int i = 0; i < 4; ++i) acc = mfma32(pa[i], vf[i], acc);
    const f32x4 ts = (t[0] + t[1]) + (t[2] + t[3]);
    unsigned ml[2] = {nosent(__float_as_uint(mh)), nosent(__float_as_uint((g & 1) ? ts[1] + ts[3] : ts[0] + ts[2]))};
    PIN(ml[0]); PIN(ml[1]); PIN(acc); PIN(pa[4]); PIN(pa[5]); PIN(pa[6]); PIN(pa[7]);
    STAMP(15);
    asm volatile("s_waitcnt vmcnt(4)" : "+v"(rdy) :: "memory");
    if (chk) {
#pragma unroll 1
      for (int it = 0; it < SPIN_CAP; ++it) {
        if (__builtin_amdgcn_ballot_w64(((rdy[0] ^ tlo) | (rdy[1] ^ thi) | (rdy[2] ^ tlo) | (rdy[3] ^ thi)) != 0u) == 0) break;
        asm volatile("buffer_load_dwordx4 %0, %1, %2, 0 offen sc1\n\ts_waitcnt vmcnt(0)" : "+v"(rdy) : "v"(rdo), "s"(srd_ws) : "memory");
      }
    }
    STAMP(5);
    BARRIER();
#pragma unroll
    for (int i = 0; i < 4; ++i) vf[i] = tr_frag(L, tv0 + (4 + i) * 8192, tv1 + (4 + i) * 8192);
    SCHED();
#pragma unroll
    for (int i = 0; i < 4; ++i) acc = mfma32(pa[4 + i], vf[i], acc);
    const unsigned d0 = nosent(pk2(acc[0], dpp<SHL1>(acc[0])));
    st_sc1(rs, pao, v4u{d0, dppu<SHL2>(d0), ml[0], ml[1]});
  }
  STAMP(6);

  if (__builtin_expect(is_wo, 1)) {
    VMWAIT(1);
    BARRIER();
    v4u bf[4];
#pragma unroll
    for (int i = 0; i < 4; ++i) bf[i] = tr_frag(L, wb0[i & 1] + (i >> 1) * 16384, wb1[i & 1] + (i >> 1) * 16384);
    PIN(bf[0]); PIN(bf[1]); PIN(bf[2]); PIN(bf[3]);
    STAMP(7);
    const v4u pv = poll_slot(srd_ws, pai);
    STAMP(8);
    const float m0 = __uint_as_float(pv[2]);
    const float mm = x32_max(x16_max(vmax(m0, dpp<ROR8>(m0))));
    const float e0 = __builtin_amdgcn_exp2f((m0 - mm) * LOG2E);
    float lw = e0 * __uint_as_float(pv[3]);
    const float ov = sum8_scatter4(e0 * bflo(pv[0]), e0 * bfhi(pv[0]), e0 * bflo(pv[1]), e0 * bfhi(pv[1]));
    lw += dpp<ROR8>(lw);
    lw = swap16_add(lw, lw);
    lw = swap32_add(lw, lw);
    *(LAS u16*)(L + ow) = (u16)pk2(ov * __builtin_amdgcn_rcpf(lw), 0.f);
    BARRIER();
    v4u oa[4];
#pragma unroll
    for (int i = 0; i < 4; ++i) oa[i] = *(LAS v4u*)(L + ob[i & 1] + (i >> 1) * 64);
    SCHED();
    f32x4 hacc = {0.f, 0.f, 0.f, 0.f};
#pragma unroll
    for (int i = 0; i < 4; ++i) hacc = mfma32(oa[i], bf[i], hacc);
    st_sc1(rs, hpo, nosent4(hacc));
    STAMP(9);
    EXIT();
  }
  if (is_ffn) {
    const u16* gup = ((lane >> 2) & 1 ? wu : wg) + (size_t)(64 * w + (lane >> 3)) * FF + 32 * jf + 8 * ((lane & 3) ^ (g & 1));
    const int alt = (lane & 2) ? -16 : 16;
    STAMP(20);
    BARRIER();
#pragma unroll
    for (int i = 0; i < 8; ++i) dma_glb<1>(lbase + L_V + w * 8192 + i * 1024, gup + i * 8 * FF + ((i & 1) ? alt : 0));
    STAMP(10);
    *(LAS v2u*)(L + L_P + w * 128 + n * 8) = v2u{ONES2, ONES2};
    v4u bd[4];
    int ao[4], opo = WS_OP + jf * REC + w * 1024 + lane * 16;
#pragma unroll
    for (int i = 0; i < 4; ++i) { ao[i] = (n >> 2) == i ? L_HH + (n & 3) * 64 + g * 16 : L_ZERO; PIN(ao[i]); }
    int e0 = L_K + (w >> 1) * 8192 + (tr0 ^ (((8 * (w & 1)) ^ (2 * w)) << 4)), e1 = L_K + (w >> 1) * 8192 + (tr1 ^ (((8 * (w & 1)) ^ (2 * w)) << 4));
    PIN(opo); PIN(e0); PIN(e1);
#define BDLOAD() do { _Pragma("unroll") for (int i = 0; i < 4; ++i) bd[i] = tr_frag(L, e0 ^ (i << 5), e1 ^ (i << 5)); \
      PIN(bd[0]); PIN(bd[1]); PIN(bd[2]); PIN(bd[3]); } while (0)
#define DOWN() do { BARRIER(); v4u a4[4]; \
      _Pragma("unroll") for (int i = 0; i < 4; ++i) a4[i] = *(LAS v4u*)(L + ao[i]); \
      SCHED(); f32x4 d = {0.f, 0.f, 0.f, 0.f}; \
      _Pragma("unroll") for (int i = 0; i < 4; ++i) d = mfma32(a4[i], bd[i], d); \
      st_sc1(rs, opo, nosent4(d)); STAMP(9); } while (0)
    if (w >= 4) {
      int hr = L_HN + (lane & 3) * 1088 + (16 * (w & 3) + (lane >> 2)) * 16, hs = L_HN + (w & 3) * 1088 + lane * 16;
      int hi = WS_H + (16 * (w & 3) + (lane >> 2)) * 1024 + kvh * 128 + (lane & 3) * 16;
      PIN(hr); PIN(hs); PIN(hi);
      wait_ready(rs, lane, tlo, thi);
      VMWAIT(0);
      STAMP(11);
      BARRIER();
      BDLOAD();
      STAMP(7);
      const v4u hv = poll_slot(srd_ws, hi);
      STAMP(8);
      *(LAS v4u*)(L + hr) = hv;
      BARRIER();
      const v4u h4 = *(LAS v4u*)(L + hs);
      const float s0 = dot2(h4[0], ONES2, dot2(h4[1], ONES2, 0.f)) + dot2(h4[2], ONES2, dot2(h4[3], ONES2, 0.f));
      const float q0 = dot2(h4[0], h4[0], dot2(h4[1], h4[1], 0.f)) + dot2(h4[2], h4[2], dot2(h4[3], h4[3], 0.f));
      const float ml = s0 * 0.125f, pv = __builtin_bit_cast(float, __builtin_amdgcn_readfirstlane(__builtin_bit_cast(int, ml))), dl = ml - pv;
      const float dm = wave_sum(dl) * (1.0f / 64);
      const float m2 = wave_sum(fmaf(8.f * dl, dl, fmaf(-8.f * ml, ml, q0)));
      *(LAS f32x2*)(L + L_RM + (w & 3) * 8) = f32x2{rb(pv + dm), ln_inv(fmaxf(fmaf(-dm * DM, dm, m2), 0.f))};
      STAMP(27);
      BARRIER();
      DOWN();
      EXIT();
    }
    VMWAIT(0);
    STAMP(11);
    BARRIER();
    int uo = L_V + (8 * g + tq) * 128 + ((4 * (tp >> 1) + (w ^ (((tq >> 1) & 1) | ((g & 1) << 1)))) * 16) + 8 * (tp & 1);
    PIN(uo);
    v4u bf[16];
#pragma unroll
    for (int i = 0; i < 16; ++i) bf[i] = tr_frag(L, uo + i * 4096, uo + i * 4096 + 512);
    BDLOAD();
    int hb = (n & 3) == 1 ? L_P : L_HN + (n >> 2) * 1088 + g * 16, hw = n < 8 ? L_HH + g * 64 + (8 * w + n) * 2 : L_JUNK;
    int sto = L_RM + g * 8;
    PIN(hb); PIN(hw); PIN(sto);
#pragma unroll
    for (int i = 0; i < 16; ++i) PIN(bf[i]);
    STAMP(25);
    BARRIER();
    STAMP(26);
    v4u ha[8];
#pragma unroll
    for (int i = 0; i < 8; ++i) ha[i] = *(LAS v4u*)(L + hb + i * 64);
    SCHED();
    f32x4 ac[2] = {{0.f, 0.f, 0.f, 0.f}, {0.f, 0.f, 0.f, 0.f}};
#pragma unroll
    for (int i = 0; i < 8; ++i) { ac[i & 1] = mfma32(ha[i], bf[i], ac[i & 1]); ha[i] = *(LAS v4u*)(L + hb + (8 + i) * 64); SCHED(); }
#pragma unroll
    for (int i = 0; i < 8; ++i) ac[i & 1] = mfma32(ha[i], bf[8 + i], ac[i & 1]);
    float hw_ = ac[0][0] + ac[1][0], cs = ac[0][1] + ac[1][1];
    PIN(hw_); PIN(cs);
    STAMP(28);
    BARRIER();
    const f32x2 st = *(LAS f32x2*)(L + sto);
    const float a = rb(fmaf(-st[0], cs, hw_) * st[1]), u = dpp<ROR8>(a);
    const float sg = rb(__builtin_amdgcn_rcpf(1.0f + __builtin_amdgcn_exp2f(a * -LOG2E)));
    *(LAS u16*)(L + hw) = (u16)pk2(sg * u, 0.f);
    STAMP(29);
    DOWN();
    EXIT();
  }
  if (is_red) {
#pragma unroll
    for (int i = 4; i < 8; ++i) dma_glb<0>(ldst + L_K, wp0 + i * 8 * FF);
    const __amdgpu_buffer_rsrc_t ro = __builtin_amdgcn_make_buffer_rsrc(out, 0, 4 * DM * 2, 0x00020000);
    const int r2 = lane >> 3, k = lane & 7, bb = r2 & 3;
    float xv = bflo(x[bb * DM + 8 * rho + k]);
    int hpi = WS_HP + (8 * w + r2) * REC + (8 * rho + k) * 16, opi = w < 7 ? WS_OP + (8 * w + r2) * REC + (8 * rho + k) * 16 : OOB;
    int ho = w == 0 ? WS_H + rho * 1024 + ((k & 3) + 4 * (lane >> 5)) * 128 + bb * 16 + (k >> 2) * 8 : OOB;
    int oo = (w == 0 && k == 0 && lane < 32) ? (bb * DM + 8 * rho) * 2 : OOB;
    int sw = L_P + (((((g & 1) << 1) | (g >> 1)) * 8 + k) * 8 + w) * 4, sr = L_P + (bb * 8 + k) * 32;
    __builtin_amdgcn_raw_buffer_store_b128(v4u{0u, 0u, 0u, 0u}, ro, oo, 0, 0);
    PIN(xv); PIN(ho); PIN(oo); PIN(sw); PIN(sr); PIN(hpi); PIN(opi);
    if (w == 0) wait_ready(rs, lane, tlo, thi);
    BARRIER();
    STAMP(7);
#define PARTIAL_SUM(PV, AREA, RES) do { \
      *(LAS float*)(L + sw + (AREA)) = sum8_scatter4(__uint_as_float(PV[0]), __uint_as_float(PV[1]), __uint_as_float(PV[2]), __uint_as_float(PV[3])); \
      BARRIER(); \
      const f32x4 rc = *(LAS f32x4*)(L + sr + (AREA)) + *(LAS f32x4*)(L + sr + (AREA) + 16); \
      RES = (rc[0] + rc[2]) + (rc[1] + rc[3]); } while (0)
    float av;
    const v4u pv = poll_slot(srd_ws, hpi);
    STAMP(8);
    PARTIAL_SUM(pv, 0, av);
    const float hval = rb(xv + rb(av));
    {
      const unsigned e0 = nosent(pk2(dpp<0xA0>(hval), dpp<0xF5>(hval)));
      st8_sc1_asm(v2u{dppu<0x00>(e0), dppu<0xAA>(e0)}, ho, srd_ws);
    }
    SCHED();
    STAMP(9);
    st8_sc1_asm(v2u{tlo, thi}, (c == 192 && w == 1 && lane == 0) ? WS_EPOCH : OOB, srd_ws);
    const v4u pw = poll_slot(srd_ws, opi);
    STAMP(10);
    PARTIAL_SUM(pw, 1024, av);
    const float ov = hval + rb(av);
    const unsigned dw = pk2(ov, dpp<SHL1>(ov)), dw2 = dppu<SHL2>(dw);
    __builtin_amdgcn_raw_buffer_store_b128(v4u{dw, dw2, dppu<SHL4>(dw), dppu<SHL4>(dw2)}, ro, oo, 0, 0);
    STAMP(11);
  }
  STAMP_OUT();
}

}

extern "C" void kernel_launch(void* const* d_in, const int* in_sizes, int n_in, void* d_out, int out_size, void* d_ws, size_t ws_size,
                              hipStream_t stream) {
  (void)in_sizes; (void)n_in; (void)out_size; (void)ws_size;
  fused_block<<<256, NTHR, 0, stream>>>((const u16*)d_in[0], (const u16*)d_in[1], (const u16*)d_in[2], (const u16*)d_in[3], (const u16*)d_in[4],
                                        (const u16*)d_in[6], (const u16*)d_in[7], (const u16*)d_in[8], (u16*)d_out, (unsigned char*)d_ws);
}
```

```cpp
#include <hip/hip_runtime.h>
#include <hip/hip_bf16.h>
#include <stdint.h>

namespace {

typedef unsigned short u16;
typedef unsigned long long u64;
typedef unsigned v4u __attribute__((ext_vector_type(4)));
typedef unsigned v2u __attribute__((ext_vector_type(2)));
typedef float f32x4 __attribute__((ext_vector_type(4)));
typedef float f32x2 __attribute__((ext_vector_type(2)));
typedef short bf16x8 __attribute__((ext_vector_type(8)));
typedef short s16x4 __attribute__((ext_vector_type(4)));
typedef __bf16 bf2 __attribute__((ext_vector_type(2)));
#define LAS __attribute__((address_space(3)))

constexpr int DM = 512, HKV = 8, SK = 8192, DH = 128, WIN = 2048, FF = 1792, NQ = 4096;
constexpr int NTHR = 640;

constexpr int WS_EPOCH = 0;
constexpr int WS_RDY = 4096;
constexpr int WS_QG = 8192;
constexpr int WS_PA = WS_QG + 512 * 1024;
constexpr int WS_HP = WS_PA + 512 * 1024;
constexpr int REC = 8320;
constexpr int WS_H = WS_HP + 64 * REC;
constexpr int WS_OP = WS_H + 65536;
constexpr int WS_END = WS_OP + 56 * REC;
constexpr int OOB = 0x40000000;

constexpr int L_K = 0;
constexpr int L_V = 65536;
constexpr int L_WQ = 131072;
constexpr int L_HN = L_WQ;
constexpr int L_Q = L_WQ + 16384;
constexpr int L_XN = L_Q + 1280;
constexpr int L_O = L_XN;
constexpr int L_QP = L_XN + 1280;
constexpr int L_P = L_QP + 1024;
constexpr int L_RM = L_P + 2304;
constexpr int L_RL = L_RM + 128;
constexpr int L_HH = L_RL + 128;
constexpr int L_ZERO = L_HH + 256;
constexpr int L_FLAG = L_ZERO + 128;
constexpr int L_JUNK = L_FLAG + 16;
constexpr int L_BYTES = L_JUNK + 2048;

constexpr unsigned SENT = 0xFFFFFFFFu;
constexpr u64 TAG_STEP = 0x9E3779B97F4A7C15ull;
constexpr int SPIN_CAP = 1 << 16;
constexpr int AUX_SC1 = 16;
constexpr float LOG2E = 1.4426950408889634f;
constexpr unsigned ONES2 = 0x3F803F80u;
constexpr int CODE_BYTES = 12352;
constexpr int QG_DELAY = 28;

__device__ __forceinline__ float bflo(unsigned x) { return __builtin_bit_cast(float, x << 16); }
__device__ __forceinline__ float bfhi(unsigned x) { return __builtin_bit_cast(float, x & 0xffff0000u); }
__device__ __forceinline__ unsigned pk2(float lo, float hi) {
  bf2 v = {(__bf16)lo, (__bf16)hi};
  return __builtin_bit_cast(unsigned, v);
}
__device__ __forceinline__ float rb(float x) { return (float)(__bf16)x; }
__device__ __forceinline__ unsigned nosent(unsigned x) { return x < 0xFFFFFFFEu ? x : 0xFFFFFFFEu; }
__device__ __forceinline__ v4u nosent4(f32x4 v) {
  return v4u{nosent(__float_as_uint(v[0])), nosent(__float_as_uint(v[1])), nosent(__float_as_uint(v[2])), nosent(__float_as_uint(v[3]))};
}
__device__ __forceinline__ float vmax(float a, float b) { float r; asm("v_max_f32 %0, %1, %2" : "=v"(r) : "v"(a), "v"(b)); return r; }
__device__ __forceinline__ float vmax3(float a, float b, float c) { float r; asm("v_max3_f32 %0, %1, %2, %3" : "=v"(r) : "v"(a), "v"(b), "v"(c)); return r; }
__device__ __forceinline__ void row_max2(float& a, float& b) {
  asm("s_nop 1\n\t"
      "v_max_f32_dpp %0, %0, %0 row_ror:8 row_mask:0xf bank_mask:0xf\n\tv_max_f32_dpp %1, %1, %1 row_ror:8 row_mask:0xf bank_mask:0xf\n\ts_nop 0\n\t"
      "v_max_f32_dpp %0, %0, %0 row_ror:4 row_mask:0xf bank_mask:0xf\n\tv_max_f32_dpp %1, %1, %1 row_ror:4 row_mask:0xf bank_mask:0xf\n\ts_nop 0\n\t"
      "v_max_f32_dpp %0, %0, %0 row_ror:2 row_mask:0xf bank_mask:0xf\n\tv_max_f32_dpp %1, %1, %1 row_ror:2 row_mask:0xf bank_mask:0xf\n\ts_nop 0\n\t"
      "v_max_f32_dpp %0, %0, %0 row_ror:1 row_mask:0xf bank_mask:0xf\n\tv_max_f32_dpp %1, %1, %1 row_ror:1 row_mask:0xf bank_mask:0xf\n\ts_nop 1"
      : "+v"(a), "+v"(b));
}
__device__ __forceinline__ float dot2(unsigned a, unsigned b, float c) {
  return __builtin_amdgcn_fdot2_f32_bf16(__builtin_bit_cast(bf2, a), __builtin_bit_cast(bf2, b), c, false);
}
__device__ __forceinline__ f32x4 mfma32(v4u a, v4u b, f32x4 c) {
  return __builtin_amdgcn_mfma_f32_16x16x32_bf16(__builtin_bit_cast(bf16x8, a), __builtin_bit_cast(bf16x8, b), c, 0, 0, 0);
}
template <int CTRL> __device__ __forceinline__ float dpp(float x) {
  return __builtin_bit_cast(float, __builtin_amdgcn_mov_dpp(__builtin_bit_cast(int, x), CTRL, 0xf, 0xf, true));
}
template <int CTRL> __device__ __forceinline__ unsigned dppu(unsigned x) { return (unsigned)__builtin_amdgcn_mov_dpp((int)x, CTRL, 0xf, 0xf, true); }
constexpr int ROR8 = 0x128, ROR4 = 0x124, ROR2 = 0x122, ROR1 = 0x121, SHL1 = 0x101, SHL2 = 0x102, SHL4 = 0x104;
__device__ __forceinline__ float row_sum(float x) { x += dpp<ROR8>(x); x += dpp<ROR4>(x); x += dpp<ROR2>(x); return x + dpp<ROR1>(x); }
__device__ __forceinline__ float swap16_add(float a, float b) {
  const auto s = __builtin_amdgcn_permlane16_swap(__float_as_uint(a), __float_as_uint(b), false, false);
  return __uint_as_float(s[0]) + __uint_as_float(s[1]);
}
__device__ __forceinline__ float swap32_add(float a, float b) {
  const auto s = __builtin_amdgcn_permlane32_swap(__float_as_uint(a), __float_as_uint(b), false, false);
  return __uint_as_float(s[0]) + __uint_as_float(s[1]);
}
__device__ __forceinline__ float x16_max(float x) {
  const auto s = __builtin_amdgcn_permlane16_swap(__float_as_uint(x), __float_as_uint(x), false, false);
  return vmax(__uint_as_float(s[0]), __uint_as_float(s[1]));
}
__device__ __forceinline__ float x32_max(float x) {
  const auto s = __builtin_amdgcn_permlane32_swap(__float_as_uint(x), __float_as_uint(x), false, false);
  return vmax(__uint_as_float(s[0]), __uint_as_float(s[1]));
}
__device__ __forceinline__ float wave_sum(float x) { x = row_sum(x); x = swap16_add(x, x); return swap32_add(x, x); }
__device__ __forceinline__ float sum8_scatter4(float a, float b, float c, float d) {
  const float t = swap16_add(swap32_add(a, b), swap32_add(c, d));
  return t + dpp<ROR8>(t);
}
__device__ __forceinline__ f32x4 sel2(bool c, f32x4 a, f32x4 b) {
  return f32x4{c ? b[0] : a[0], c ? b[1] : a[1], c ? b[2] : a[2], c ? b[3] : a[3]};
}
__device__ __forceinline__ float ln_inv(float q) {
  return __builtin_amdgcn_rcpf(rb(__builtin_amdgcn_sqrtf(rb(rb(q * (1.0f / DM)) + 1e-5f))));
}

#define PIN(x) asm volatile("" : "+v"(x))
#define SCHED() __builtin_amdgcn_sched_barrier(0)
#define LDS_FENCE() asm volatile("s_waitcnt lgkmcnt(0)" ::: "memory")
#define BARRIER() do { asm volatile("s_waitcnt lgkmcnt(0)\n\ts_barrier" ::: "memory"); SCHED(); } while (0)
#define VMWAIT(N) asm volatile("s_waitcnt vmcnt(" #N ")" ::: "memory")
#define EXIT() do { asm volatile("s_endpgm" ::: "memory"); __builtin_unreachable(); } while (0)

__device__ __forceinline__ v4u make_srd(const void* p, unsigned bytes) {
  const u64 a = (u64)p;
  return v4u{(unsigned)a, (unsigned)(a >> 32) & 0xffffu, bytes, 0x00020000u};
}
__device__ __forceinline__ void dma_buf(unsigned lds, int voff, v4u srd, int soff) {
  asm volatile("s_mov_b32 m0, %0\n\ts_nop 0\n\tbuffer_load_dwordx4 %1, %2, %3 offen nt lds" :: "s"(lds), "v"(voff), "s"(srd), "s"(soff) : "m0", "memory");
}
template <int NT> __device__ __forceinline__ void dma_glb(unsigned lds, const void* p) {
  if (NT) asm volatile("s_mov_b32 m0, %0\n\ts_nop 0\n\tglobal_load_lds_dwordx4 %1, off nt" :: "s"(lds), "v"(p) : "m0", "memory");
  else asm volatile("s_mov_b32 m0, %0\n\ts_nop 0\n\tglobal_load_lds_dwordx4 %1, off" :: "s"(lds), "v"(p) : "m0", "memory");
}
__device__ __forceinline__ void ld16_asm(v4u& d, int voff, v4u srd, int soff) {
  asm volatile("buffer_load_dwordx4 %0, %1, %2, %3 offen" : "=v"(d) : "v"(voff), "s"(srd), "s"(soff) : "memory");
}
__device__ __forceinline__ void ld16_sc1_asm(v4u& d, int voff, v4u srd) {
  asm volatile("buffer_load_dwordx4 %0, %1, %2, 0 offen sc1" : "=v"(d) : "v"(voff), "s"(srd) : "memory");
}
__device__ __forceinline__ void ld8_sc1_asm(v2u& d, v4u srd) {
  asm volatile("buffer_load_dwordx2 %0, off, %1, 0 sc1" : "=v"(d) : "s"(srd) : "memory");
}
__device__ __forceinline__ void st16_sc1_asm(v4u v, int voff, v4u srd) {
  asm volatile("buffer_store_dwordx4 %0, %1, %2, 0 offen sc1\n\ts_nop 1" :: "v"(v), "v"(voff), "s"(srd) : "memory");
}
__device__ __forceinline__ void st8_sc1_asm(v2u v, int voff, v4u srd) {
  asm volatile("buffer_store_dwordx2 %0, %1, %2, 0 offen sc1" :: "v"(v), "v"(voff), "s"(srd) : "memory");
}
__device__ __forceinline__ v4u ld_sc1(__amdgpu_buffer_rsrc_t rs, int off) { return __builtin_amdgcn_raw_buffer_load_b128(rs, off, 0, AUX_SC1); }
__device__ __forceinline__ void st_sc1(__amdgpu_buffer_rsrc_t rs, int off, v4u v) { __builtin_amdgcn_raw_buffer_store_b128(v, rs, off, 0, AUX_SC1); }
__device__ __forceinline__ v2u tr_rd(LAS unsigned char* L, int off) {
  return __builtin_bit_cast(v2u, __builtin_amdgcn_ds_read_tr16_b64_v4i16((LAS s16x4*)(L + off)));
}
__device__ __forceinline__ v4u tr_frag(LAS unsigned char* L, int off0, int off1) {
  const v2u a = tr_rd(L, off0), b = tr_rd(L, off1);
  return v4u{a[0], a[1], b[0], b[1]};
}

__device__ __forceinline__ v4u poll_slot(v4u srd, int off) {
  v4u v;
  unsigned t, cnt;
  u64 miss;
  asm volatile(
      "s_mov_b32 %2, %6\n\ts_mov_b64 %3, -1\n\ts_branch 2f\n"
      "1:\n\ts_sleep 1\n"
      "2:\n\ts_mov_b64 exec, %3\n\tbuffer_load_dwordx4 v[96:99], %4, %5, 0 offen sc1\n\ts_mov_b64 exec, -1\n\t"
      "s_sub_u32 %2, %2, 1\n\ts_waitcnt vmcnt(0)\n\ts_cbranch_scc1 3f\n\t"
      "v_max_u32 %1, v96, v97\n\tv_max3_u32 %1, %1, v98, v99\n\tv_cmp_eq_u32 %3, -1, %1\n\ts_cmp_lg_u64 %3, 0\n\ts_cbranch_scc1 1b\n"
      "3:"
      : "=&{v[96:99]}"(v), "=&v"(t), "=&s"(cnt), "=&s"(miss) : "v"(off), "s"(srd), "n"(SPIN_CAP) : "scc", "memory");
  return v;
}
__device__ __forceinline__ void wait_ready(__amdgpu_buffer_rsrc_t rs, int lane, unsigned tlo, unsigned thi) {
#pragma unroll 1
  for (int it = 0; it < SPIN_CAP; ++it) {
    asm volatile("" ::: "memory");
    const v4u r0 = ld_sc1(rs, WS_RDY + lane * 16), r1 = ld_sc1(rs, WS_RDY + 1024 + lane * 16);
    const unsigned bad = ((r0[0] ^ tlo) | (r0[1] ^ thi) | (r0[2] ^ tlo)) | ((r0[3] ^ thi) | (r1[0] ^ tlo) | (r1[1] ^ thi)) | ((r1[2] ^ tlo) | (r1[3] ^ thi));
    if (__builtin_amdgcn_ballot_w64(bad != 0u) == 0) break;
    __builtin_amdgcn_s_sleep(4);
  }
}

__global__ void __launch_bounds__(NTHR, 3)
fused_block(const u16* __restrict__ x, const u16* __restrict__ kc, const u16* __restrict__ vc, const u16* __restrict__ wq,
            const u16* __restrict__ wo, const u16* __restrict__ wg, const u16* __restrict__ wu, const u16* __restrict__ wd,
            u16* __restrict__ out, unsigned char* __restrict__ ws) {
  __shared__ __attribute__((aligned(16))) unsigned char smem_[L_BYTES];
  {
    u64 pc0;
    asm volatile("s_getpc_b64 %0" : "=s"(pc0));
    if (__builtin_amdgcn_readfirstlane(threadIdx.x >> 6) == 1) {
      const v4u srd_c = make_srd((const void*)(pc0 & ~255ull), CODE_BYTES);
      const int so = ((blockIdx.x >> 3) & 15) * 2048;
      asm volatile("s_mov_b32 m0, %0\n\ts_nop 0\n\tbuffer_load_dwordx4 %1, %2, %3 offen lds\n\tbuffer_load_dwordx4 %1, %2, %3 offen offset:1024 lds"
                   :: "s"((unsigned)(size_t)(LAS unsigned char*)smem_ + L_JUNK), "v"((threadIdx.x & 63) * 16), "s"(srd_c), "s"(so) : "m0", "memory");
    }
    SCHED();
  }
  LAS unsigned char* L = (LAS unsigned char*)smem_;
  const unsigned lbase = (unsigned)(size_t)L;
  const int c = blockIdx.x, kvh = c & 7, i32 = c >> 3;
  const int lane = threadIdx.x & 63, wv = __builtin_amdgcn_readfirstlane(threadIdx.x >> 6);
  const int w = wv < 2 ? 8 + wv : wv - 2;
  const int n = lane & 15, g = lane >> 4;
  const v4u srd_ws = make_srd(ws, WS_END);

  const int b = i32 >> 3, s = i32 & 7;
  const size_t kvbase = ((size_t)(b * HKV + kvh) * SK + (SK - WIN) + s * 256) * DH;
  const v4u srd_k = make_srd(kc + kvbase, 65536);
  const int rot = w + 37 * c;
  const int kv0 = g * 256 + ((n ^ g) * 16);
  const unsigned ldst = lbase + w * 1024;
  v2u ep;
  v4u xr[4];
#define KLOAD(i) dma_buf(ldst + L_K + (i) * 8192, kv0 ^ (((i) & 3) << 6), srd_k, ((8 * (i) + rot) & 63) << 10)
  if (wv == 0) {
    const v4u srd_x = make_srd(x, 4 * DM * 2);
    const v4u srd_wq = make_srd(wq + (size_t)b * 128 * NQ + kvh * 512 + s * 64, 128 * NQ * 2);
    ld8_sc1_asm(ep, srd_ws);
#pragma unroll
    for (int i = 0; i < 4; ++i) ld16_asm(xr[i], g * 1024 + n * 16, srd_x, ((b + i) & 3) * 256);
    const int row = lane >> 3, v0 = row * (NQ * 2) + (((lane & 7) ^ (2 * ((row >> 1) & 1))) * 16);
    int so;
#define WQ2 "buffer_load_dwordx4 %1, %3, %0 offen nt lds\n\ts_addk_i32 m0, 0x400\n\ts_add_u32 %0, %0, %5\n\t" \
            "buffer_load_dwordx4 %2, %3, %0 offen nt lds\n\ts_addk_i32 m0, 0x400\n\ts_add_u32 %0, %0, %5\n\t"
    asm volatile("s_mov_b32 m0, %4\n\ts_mov_b32 %0, 0\n\t" WQ2 WQ2 WQ2 WQ2 WQ2 WQ2 WQ2 WQ2
                 : "=&s"(so) : "v"(v0), "v"(v0 ^ 64), "s"(srd_wq), "s"(lbase + L_WQ), "s"(8 * NQ * 2) : "m0", "scc", "memory");
    *(LAS unsigned*)(L + L_FLAG) = 0u;
    LDS_FENCE();
  }
  asm volatile("s_barrier" ::: "memory");
  if (wv >= 2) { KLOAD(0); KLOAD(1); }
  int wv2 = wv;
  asm volatile("" : "+s"(wv2));

  if (wv2 == 1) {
    v2u ep1;
    ld8_sc1_asm(ep1, srd_ws);
    const v4u sent = {SENT, SENT, SENT, SENT};
    st16_sc1_asm(sent, WS_PA + kvh * 65536 + g * 8192 + i32 * 256 + n * 16, srd_ws);
    st16_sc1_asm(sent, WS_PA + kvh * 65536 + (g + 4) * 8192 + i32 * 256 + n * 16, srd_ws);
    const int base = i32 < 16 ? WS_HP + (kvh * 8 + (i32 >> 1)) * REC + (i32 & 1) * 4096 : WS_OP + (kvh * 7 + i32 - 16) * REC;
    const int len = i32 < 16 ? 4096 : i32 < 23 ? 8192 : 0;
#pragma unroll 1
    for (int o = 0; o < len; o += 1024) st16_sc1_asm(sent, base + o + lane * 16, srd_ws);
    st16_sc1_asm(sent, (i32 >= 24 && lane < 32) ? WS_H + ((i32 - 24) * 8 + kvh) * 1024 + (lane >> 2) * 128 + (lane & 3) * 16 : OOB, srd_ws);
    *(LAS v4u*)(L + L_ZERO + (lane & 7) * 16) = v4u{0u, 0u, 0u, 0u};
    asm volatile("s_waitcnt vmcnt(0)" : "+v"(ep1) :: "memory");
    const u64 tag = (((u64)ep1[1] << 32) | ep1[0]) + TAG_STEP;
    st8_sc1_asm(v2u{(unsigned)tag, (unsigned)(tag >> 32)}, lane == 0 ? WS_RDY + (kvh * 32 + i32) * 8 : OOB, srd_ws);
    EXIT();
  }
  if (wv2 == 0) {
    asm volatile("s_waitcnt vmcnt(16)" : "+v"(ep), "+v"(xr[0]), "+v"(xr[1]), "+v"(xr[2]), "+v"(xr[3]) :: "memory");
    {
      float s0 = 0.f, s1 = 0.f, q0 = 0.f, q1 = 0.f;
#pragma unroll
      for (int i = 0; i < 4; ++i)
#pragma unroll
        for (int k = 0; k < 4; k += 2) {
          s0 = dot2(xr[i][k], ONES2, s0); s1 = dot2(xr[i][k + 1], ONES2, s1);
          q0 = dot2(xr[i][k], xr[i][k], q0); q1 = dot2(xr[i][k + 1], xr[i][k + 1], q1);
        }
      const float ml = (s0 + s1) * (1.0f / 32), pv = dpp<0x150>(ml), dl = ml - pv;
      const float dm = row_sum(dl) * (1.0f / 16), mean = pv + dm;
      const float inv = ln_inv(fmaxf(fmaf(-dm * DM, dm, row_sum(fmaf(32.f * dl, dl, fmaf(-32.f * ml, ml, q0 + q1)))), 0.f));
      const float mb = rb(mean);
      v4u o;
#pragma unroll
      for (int k = 0; k < 4; ++k) {
        const unsigned d = pk2(bflo(xr[0][k]) - mb, bfhi(xr[0][k]) - mb);
        o[k] = pk2(bflo(d) * inv, bfhi(d) * inv);
      }
      *(LAS v4u*)(L + L_XN + g * 320 + n * 16) = o;
    }
    LDS_FENCE();
    f32x4 acc[4];
    {
      v4u xa[4];
#pragma unroll
      for (int ks = 0; ks < 4; ++ks) xa[ks] = *(LAS v4u*)(L + L_XN + (n & 3) * 320 + ks * 64 + g * 16);
      const int q4 = n >> 2, p = n & 3, y = ((q4 >> 1) & 1) | ((g & 1) << 1);
      const int rbase = L_WQ + (8 * g + q4) * 128 + 8 * (p & 1);
      int o[4];
#pragma unroll
      for (int t = 0; t < 4; ++t) { o[t] = rbase + (((2 * t + (p >> 1)) ^ (2 * y)) * 16); acc[t] = f32x4{0.f, 0.f, 0.f, 0.f}; }
#pragma unroll
      for (int ks = 0; ks < 4; ++ks) {
        if (ks == 0) VMWAIT(12); else if (ks == 1) VMWAIT(8); else if (ks == 2) VMWAIT(4); else VMWAIT(0);
#pragma unroll
        for (int t = 0; t < 4; ++t) acc[t] = mfma32(xa[ks], tr_frag(L, o[t] + ks * 4096, o[t] + ks * 4096 + 512), acc[t]);
      }
    }
    *(LAS f32x4*)(L + L_QP + lane * 16) = sel2(g & 2, sel2(g & 1, acc[0], acc[1]), sel2(g & 1, acc[2], acc[3]));
    LDS_FENCE();
    const f32x4 q0 = *(LAS f32x4*)(L + L_QP + (lane & 31) * 32), q1 = *(LAS f32x4*)(L + L_QP + (lane & 31) * 32 + 16);
    const u64 tag = (((u64)ep[1] << 32) | ep[0]) + TAG_STEP;
    const unsigned tlo = (unsigned)tag, thi = (unsigned)(tag >> 32);
    {
      const bool up = lane >= 32;
      const int o = WS_QG + ((kvh * 4 + (up ? 1 : 0)) * 4 + b) * 4096 + (32 * s + (lane & 31)) * 16;
      st16_sc1_asm(v4u{__float_as_uint(up ? q0[1] : q0[0]), tlo, __float_as_uint(up ? q1[1] : q1[0]), thi}, o, srd_ws);
      st16_sc1_asm(v4u{__float_as_uint(up ? q0[3] : q0[2]), tlo, __float_as_uint(up ? q1[3] : q1[2]), thi}, o + 2 * 4 * 4096, srd_ws);
    }
    __builtin_amdgcn_s_sleep(QG_DELAY);
    const int qo = WS_QG + (kvh * 4 + b) * 16384 + lane * 16;
    v4u qg[16];
    u64 miss[16];
#pragma unroll
    for (int i = 0; i < 16; ++i) { asm volatile("" : "=v"(qg[i])); miss[i] = ~0ull; }
#pragma unroll 1
    for (int it = 0; it < SPIN_CAP; ++it) {
#pragma unroll
      for (int i = 0; i < 16; ++i)
        asm volatile("s_mov_b64 exec, %4\n\tbuffer_load_dwordx4 %0, %1, %2, %3 offen offset:%5 sc1\n\ts_mov_b64 exec, -1"
                     : "+v"(qg[i]) : "v"(qo), "s"(srd_ws), "s"((i >> 2) * 4096), "s"(miss[i]), "n"((i & 3) * 1024) : "memory");
      VMWAIT(0);
      u64 any = 0;
#pragma unroll
      for (int i = 0; i < 16; ++i) {
        PIN(qg[i]);
        miss[i] = __builtin_amdgcn_ballot_w64(qg[i][1] != tlo) | __builtin_amdgcn_ballot_w64(qg[i][3] != thi);
        any |= miss[i];
      }
      if (any == 0) break;
    }
#pragma unroll
    for (int u = 0; u < 4; ++u) {
      const float a = (__uint_as_float(qg[u][0]) + __uint_as_float(qg[4 + u][0])) + (__uint_as_float(qg[8 + u][0]) + __uint_as_float(qg[12 + u][0]));
      const float bq = (__uint_as_float(qg[u][2]) + __uint_as_float(qg[4 + u][2])) + (__uint_as_float(qg[8 + u][2]) + __uint_as_float(qg[12 + u][2]));
      *(LAS unsigned*)(L + L_Q + u * 320 + lane * 4) = pk2(a, bq);
    }
    *(LAS v2u*)(L + L_FLAG + 8) = v2u{tlo, thi};
    LDS_FENCE();
    *(volatile LAS unsigned*)(L + L_FLAG) = 1u;
    EXIT();
  }

  const bool is_wo = i32 < 16, is_ffn = i32 >= 16 && i32 < 23, is_red = i32 >= 24;
  const int jf = kvh * 7 + (i32 - 16);
  const int rho = (i32 - 24) * 8 + kvh;
  const __amdgpu_buffer_rsrc_t rs = __builtin_amdgcn_make_buffer_rsrc(ws, 0, WS_END, 0x00020000);
  const v4u srd_v = make_srd(vc + kvbase, 65536);
  const int vv0 = g * 256 + ((n ^ ((g << 2) | (w & 3))) * 16);
  asm volatile("" :: "s"(wo), "s"(wg), "s"(wu), "s"(wd), "s"(out), "s"(x));
#define VLOAD(i) dma_buf(ldst + L_V + (i) * 8192, vv0, srd_v, ((8 * (i) + rot) & 63) << 10)
#define WW() VMWAIT(1)
  WW();
  KLOAD(2); WW(); KLOAD(3); WW(); KLOAD(4); WW(); KLOAD(5); WW(); KLOAD(6); WW(); KLOAD(7);
  WW(); VLOAD(0); WW(); VLOAD(1); VLOAD(2);
  VMWAIT(3);
  v4u kf[8];
  {
    const int kb = L_K + (n >> 2) * 8192 + w * 1024 + (n & 3) * 256 + ((g ^ n) * 16);
#pragma unroll
    for (int ks = 0; ks < 4; ++ks) { kf[ks] = *(LAS v4u*)(L + (kb ^ (ks << 6))); kf[4 + ks] = *(LAS v4u*)(L + (kb ^ (ks << 6)) + 32768); }
  }
  int qb = L_Q + (2 * (n >> 3) + (n & 1)) * 320 + g * 16;
  int rmw = L_RM + (g >> 1) * 64 + w * 8, rmr = L_RM + (g >> 1) * 64;
  int po = L_P + (g >> 1) * 1152 + ((4 * (g & 1) + (n >> 2)) * 32 + 4 * w + (n & 3)) * 2;
  const u16* wp0 = is_wo ? wo + (size_t)(kvh * 512 + (2 * (i32 >> 3) + (w >> 2)) * 128 + 32 * ((i32 >> 1) & 3) + 4 * (w & 3)) * DM + (i32 & 1) * 256 + g * (DM - 128) + (vv0 >> 1)
                 : is_ffn ? wd + (size_t)(32 * jf + 4 * w) * DM + g * (DM - 128) + (vv0 >> 1)
                 : is_red ? (lane >= 32 ? wu : wg) + (size_t)(64 * w + (i32 & 7)) * FF + ((224 * kvh) & ~63) + 8 * (lane & 31) : x;
  const int ws1 = i32 < 23 ? 128 : 0, ws2 = is_wo ? 16 * DM : is_ffn ? 256 : 0;
  const int tq = n >> 2, tp = n & 3;
  int tr0 = (8 * g + tq) * 256 + (((2 * w + (tp >> 1)) ^ ((tq << 2) | ((2 * g) & 3))) * 16) + 8 * (tp & 1);
  int tr1 = (8 * g + 4 + tq) * 256 + (((2 * w + (tp >> 1)) ^ ((tq << 2) | ((2 * g + 1) & 3))) * 16) + 8 * (tp & 1);
  PIN(wp0); PIN(tr0); PIN(tr1);
  int tv0 = L_V + tr0, tv1 = L_V + tr1, pab = L_P + (n >> 2) * 576 + g * 16;
  int pao = (n & 3) == 0 ? WS_PA + kvh * 65536 + w * 8192 + i32 * 256 + g * 64 + (n >> 2) * 16 : OOB;
  int pai = WS_PA + kvh * 65536 + (2 * ((i32 >> 1) & 3) + (w & 1)) * 8192 + (8 * (w >> 1) + (lane >> 3)) * 256 + (i32 >> 3) * 128 + (lane & 7) * 16;
  int ob[2], wb0[2], wb1[2];
#pragma unroll
  for (int i = 0; i < 2; ++i) {
    ob[i] = (n >> 2) == i ? L_O + (n & 3) * 320 + g * 16 : L_ZERO;
    const int xo = ((((4 * w + 2 * i) & 15) ^ (2 * w)) << 4);
    wb0[i] = L_K + (w >> 2) * 8192 + (tr0 ^ xo); wb1[i] = L_K + (w >> 2) * 8192 + (tr1 ^ xo);
    PIN(ob[i]); PIN(wb0[i]); PIN(wb1[i]);
  }
  int ow = L_O + (w >> 1) * 320 + (32 * (w & 1) + 4 * (n & 7) + (((g & 1) << 1) | (g >> 1))) * 2;
  int hpo = lane < 32 ? WS_HP + (kvh * 8 + (i32 >> 1)) * REC + ((i32 & 1) * 256 + 32 * w + lane) * 16 : OOB;
  const bool chk = is_wo && w == 0;
  int rdo = chk ? WS_RDY + kvh * 256 + n * 16 : OOB;
  PIN(tv0); PIN(tv1); PIN(pab); PIN(qb); PIN(rmw); PIN(rmr); PIN(po); PIN(pao); PIN(pai); PIN(ow); PIN(hpo); PIN(rdo);
#pragma unroll
  for (int i = 0; i < 8; ++i) PIN(kf[i]);
#define QFLAG() (__builtin_amdgcn_readfirstlane(*(volatile LAS unsigned*)(L + L_FLAG)) != 0u)
  int vi = 3;
#pragma unroll 1
  while (vi < 8 && (vi <= 3 || !QFLAG())) { VMWAIT(3); VLOAD(vi); ++vi; }
#pragma unroll 1
  for (int it = 0; it < (SPIN_CAP << 6); ++it) {
    if (QFLAG()) break;
  }
  asm volatile("" ::: "memory");
  const v2u tagv = *(LAS v2u*)(L + L_FLAG + 8);
  const unsigned tlo = tagv[0], thi = tagv[1];
  float sc[2];
  {
    v4u qa[4];
#pragma unroll
    for (int ks = 0; ks < 4; ++ks) qa[ks] = *(LAS v4u*)(L + qb + ks * 64);
    f32x4 d0 = {0.f, 0.f, 0.f, 0.f}, d1 = {0.f, 0.f, 0.f, 0.f};
#pragma unroll
    for (int ks = 0; ks < 4; ++ks) { d0 = mfma32(qa[ks], kf[ks], d0); d1 = mfma32(qa[ks], kf[4 + ks], d1); }
#pragma unroll
    for (int j = 0; j < 2; ++j) sc[j] = rb(rb((g & 1) ? d1[j] : d0[j]) * 0.08838834764831845f);
    float m0 = sc[0], m1 = sc[1];
    row_max2(m0, m1);
    const f32x2 mx = {x16_max(m0), x16_max(m1)};
    *(LAS f32x2*)(L + rmw) = mx;
  }
#pragma unroll 1
  while (vi < 8) { VLOAD(vi); ++vi; }
  v4u rdy;
  ld16_sc1_asm(rdy, rdo, srd_ws);
  BARRIER();
  float mh;
  {
    f32x4 t[4];
#pragma unroll
    for (int i = 0; i < 4; ++i) t[i] = *(LAS f32x4*)(L + rmr + i * 16);
    const float M0 = vmax3(vmax3(t[0][0], t[0][2], t[1][0]), vmax3(t[1][2], t[2][0], t[2][2]), vmax(t[3][0], t[3][2]));
    const float M1 = vmax3(vmax3(t[0][1], t[0][3], t[1][1]), vmax3(t[1][3], t[2][1], t[2][3]), vmax(t[3][1], t[3][3]));
    const float p0 = __builtin_amdgcn_exp2f((sc[0] - M0) * LOG2E), p1 = __builtin_amdgcn_exp2f((sc[1] - M1) * LOG2E);
    const unsigned pp = pk2(p0, p1);
    *(LAS u16*)(L + po) = (u16)pp;
    *(LAS u16*)(L + po + 576) = (u16)(pp >> 16);
    const float l0 = row_sum(p0), l1 = row_sum(p1);
    *(LAS f32x2*)(L + rmw + (L_RL - L_RM)) = f32x2{swap16_add(l0, l0), swap16_add(l1, l1)};
    mh = (g & 1) ? M1 : M0;
  }
  if (is_red) {
#pragma unroll
    for (int i = 0; i < 4; ++i) dma_glb<0>(ldst + L_K, wp0 + i * 8 * FF);
  } else {
#pragma unroll
    for (int i = 0; i < 4; ++i) dma_glb<1>(ldst + L_K + i * 8192, wp0 + (i >> 1) * ws2 + (i & 1) * ws1);
  }
  VMWAIT(9);
  BARRIER();
  {
    v4u pa[8], vf[4];
#pragma unroll
    for (int i = 0; i < 4; ++i) { pa[i] = *(LAS v4u*)(L + pab + i * 64); vf[i] = tr_frag(L, tv0 + i * 8192, tv1 + i * 8192); }
    f32x4 t[4];
#pragma unroll
    for (int i = 0; i < 4; ++i) t[i] = *(LAS f32x4*)(L + rmr + (L_RL - L_RM) + i * 16);
#pragma unroll
    for (int i = 4; i < 8; ++i) pa[i] = *(LAS v4u*)(L + pab + i * 64);
    SCHED();
    f32x4 acc = {0.f, 0.f, 0.f, 0.f};
#pragma unroll
    for (int i = 0; i < 4; ++i) acc = mfma32(pa[i], vf[i], acc);
    const f32x4 ts = (t[0] + t[1]) + (t[2] + t[3]);
    unsigned ml[2] = {nosent(__float_as_uint(mh)), nosent(__float_as_uint((g & 1) ? ts[1] + ts[3] : ts[0] + ts[2]))};
    PIN(ml[0]); PIN(ml[1]); PIN(acc); PIN(pa[4]); PIN(pa[5]); PIN(pa[6]); PIN(pa[7]);
    asm volatile("s_waitcnt vmcnt(4)" : "+v"(rdy) :: "memory");
    if (chk) {
#pragma unroll 1
      for (int it = 0; it < SPIN_CAP; ++it) {
        if (__builtin_amdgcn_ballot_w64(((rdy[0] ^ tlo) | (rdy[1] ^ thi) | (rdy[2] ^ tlo) | (rdy[3] ^ thi)) != 0u) == 0) break;
        asm volatile("buffer_load_dwordx4 %0, %1, %2, 0 offen sc1\n\ts_waitcnt vmcnt(0)" : "+v"(rdy) : "v"(rdo), "s"(srd_ws) : "memory");
      }
    }
    BARRIER();
#pragma unroll
    for (int i = 0; i < 4; ++i) vf[i] = tr_frag(L, tv0 + (4 + i) * 8192, tv1 + (4 + i) * 8192);
    SCHED();
#pragma unroll
    for (int i = 0; i < 4; ++i) acc = mfma32(pa[4 + i], vf[i], acc);
    const unsigned d0 = nosent(pk2(acc[0], dpp<SHL1>(acc[0])));
    st_sc1(rs, pao, v4u{d0, dppu<SHL2>(d0), ml[0], ml[1]});
  }

  if (__builtin_expect(is_wo, 1)) {
    VMWAIT(1);
    BARRIER();
    v4u bf[4];
#pragma unroll
    for (int i = 0; i < 4; ++i) bf[i] = tr_frag(L, wb0[i & 1] + (i >> 1) * 16384, wb1[i & 1] + (i >> 1) * 16384);
    PIN(bf[0]); PIN(bf[1]); PIN(bf[2]); PIN(bf[3]);
    const v4u pv = poll_slot(srd_ws, pai);
    const float m0 = __uint_as_float(pv[2]);
    const float mm = x32_max(x16_max(vmax(m0, dpp<ROR8>(m0))));
    const float e0 = __builtin_amdgcn_exp2f((m0 - mm) * LOG2E);
    float lw = e0 * __uint_as_float(pv[3]);
    const float ov = sum8_scatter4(e0 * bflo(pv[0]), e0 * bfhi(pv[0]), e0 * bflo(pv[1]), e0 * bfhi(pv[1]));
    lw += dpp<ROR8>(lw);
    lw = swap16_add(lw, lw);
    lw = swap32_add(lw, lw);
    *(LAS u16*)(L + ow) = (u16)pk2(ov * __builtin_amdgcn_rcpf(lw), 0.f);
    BARRIER();
    v4u oa[4];
#pragma unroll
    for (int i = 0; i < 4; ++i) oa[i] = *(LAS v4u*)(L + ob[i & 1] + (i >> 1) * 64);
    SCHED();
    f32x4 hacc = {0.f, 0.f, 0.f, 0.f};
#pragma unroll
    for (int i = 0; i < 4; ++i) hacc = mfma32(oa[i], bf[i], hacc);
    st_sc1(rs, hpo, nosent4(hacc));
    EXIT();
  }
  if (is_ffn) {
    const u16* gup = ((lane >> 2) & 1 ? wu : wg) + (size_t)(64 * w + (lane >> 3)) * FF + 32 * jf + 8 * ((lane & 3) ^ (g & 1));
    const int alt = (lane & 2) ? -16 : 16;
    BARRIER();
#pragma unroll
    for (int i = 0; i < 8; ++i) dma_glb<1>(lbase + L_V + w * 8192 + i * 1024, gup + i * 8 * FF + ((i & 1) ? alt : 0));
    *(LAS v2u*)(L + L_P + w * 128 + n * 8) = v2u{ONES2, ONES2};
    v4u bd[4];
    int ao[4], opo = WS_OP + jf * REC + w * 1024 + lane * 16;
#pragma unroll
    for (int i = 0; i < 4; ++i) { ao[i] = (n >> 2) == i ? L_HH + (n & 3) * 64 + g * 16 : L_ZERO; PIN(ao[i]); }
    int e0 = L_K + (w >> 1) * 8192 + (tr0 ^ (((8 * (w & 1)) ^ (2 * w)) << 4)), e1 = L_K + (w >> 1) * 8192 + (tr1 ^ (((8 * (w & 1)) ^ (2 * w)) << 4));
    PIN(opo); PIN(e0); PIN(e1);
#define BDLOAD() do { _Pragma("unroll") for (int i = 0; i < 4; ++i) bd[i] = tr_frag(L, e0 ^ (i << 5), e1 ^ (i << 5)); \
      PIN(bd[0]); PIN(bd[1]); PIN(bd[2]); PIN(bd[3]); } while (0)
#define DOWN() do { BARRIER(); v4u a4[4]; \
      _Pragma("unroll") for (int i = 0; i < 4; ++i) a4[i] = *(LAS v4u*)(L + ao[i]); \
      SCHED(); f32x4 d = {0.f, 0.f, 0.f, 0.f}; \
      _Pragma("unroll") for (int i = 0; i < 4; ++i) d = mfma32(a4[i], bd[i], d); \
      st_sc1(rs, opo, nosent4(d)); } while (0)
    if (w >= 4) {
      int hr = L_HN + (lane & 3) * 1088 + (16 * (w & 3) + (lane >> 2)) * 16, hs = L_HN + (w & 3) * 1088 + lane * 16;
      int hi = WS_H + (16 * (w & 3) + (lane >> 2)) * 1024 + kvh * 128 + (lane & 3) * 16;
      PIN(hr); PIN(hs); PIN(hi);
      wait_ready(rs, lane, tlo, thi);
      VMWAIT(0);
      BARRIER();
      BDLOAD();
      const v4u hv = poll_slot(srd_ws, hi);
      *(LAS v4u*)(L + hr) = hv;
      BARRIER();
      const v4u h4 = *(LAS v4u*)(L + hs);
      const float s0 = dot2(h4[0], ONES2, dot2(h4[1], ONES2, 0.f)) + dot2(h4[2], ONES2, dot2(h4[3], ONES2, 0.f));
      const float q0 = dot2(h4[0], h4[0], dot2(h4[1], h4[1], 0.f)) + dot2(h4[2], h4[2], dot2(h4[3], h4[3], 0.f));
      const float ml = s0 * 0.125f, pv = __builtin_bit_cast(float, __builtin_amdgcn_readfirstlane(__builtin_bit_cast(int, ml))), dl = ml - pv;
      const float dm = wave_sum(dl) * (1.0f / 64);
      const float m2 = wave_sum(fmaf(8.f * dl, dl, fmaf(-8.f * ml, ml, q0)));
      *(LAS f32x2*)(L + L_RM + (w & 3) * 8) = f32x2{rb(pv + dm), ln_inv(fmaxf(fmaf(-dm * DM, dm, m2), 0.f))};
      BARRIER();
      DOWN();
      EXIT();
    }
    VMWAIT(0);
    BARRIER();
    int uo = L_V + (8 * g + tq) * 128 + ((4 * (tp >> 1) + (w ^ (((tq >> 1) & 1) | ((g & 1) << 1)))) * 16) + 8 * (tp & 1);
    PIN(uo);
    v4u bf[16];
#pragma unroll
    for (int i = 0; i < 16; ++i) bf[i] = tr_frag(L, uo + i * 4096, uo + i * 4096 + 512);
    BDLOAD();
    int hb = (n & 3) == 1 ? L_P : L_HN + (n >> 2) * 1088 + g * 16, hw = n < 8 ? L_HH + g * 64 + (8 * w + n) * 2 : L_JUNK;
    int sto = L_RM + g * 8;
    PIN(hb); PIN(hw); PIN(sto);
#pragma unroll
    for (int i = 0; i < 16; ++i) PIN(bf[i]);
    BARRIER();
    v4u ha[8];
#pragma unroll
    for (int i = 0; i < 8; ++i) ha[i] = *(LAS v4u*)(L + hb + i * 64);
    SCHED();
    f32x4 ac[2] = {{0.f, 0.f, 0.f, 0.f}, {0.f, 0.f, 0.f, 0.f}};
#pragma unroll
    for (int i = 0; i < 8; ++i) { ac[i & 1] = mfma32(ha[i], bf[i], ac[i & 1]); ha[i] = *(LAS v4u*)(L + hb + (8 + i) * 64); SCHED(); }
#pragma unroll
    for (int i = 0; i < 8; ++i) ac[i & 1] = mfma32(ha[i], bf[8 + i], ac[i & 1]);
    float hw_ = ac[0][0] + ac[1][0], cs = ac[0][1] + ac[1][1];
    PIN(hw_); PIN(cs);
    BARRIER();
    const f32x2 st = *(LAS f32x2*)(L + sto);
    const float a = rb(fmaf(-st[0], cs, hw_) * st[1]), u = dpp<ROR8>(a);
    const float sg = rb(__builtin_amdgcn_rcpf(1.0f + __builtin_amdgcn_exp2f(a * -LOG2E)));
    *(LAS u16*)(L + hw) = (u16)pk2(sg * u, 0.f);
    DOWN();
    EXIT();
  }
  if (is_red) {
#pragma unroll
    for (int i = 4; i < 8; ++i) dma_glb<0>(ldst + L_K, wp0 + i * 8 * FF);
    const __amdgpu_buffer_rsrc_t ro = __builtin_amdgcn_make_buffer_rsrc(out, 0, 4 * DM * 2, 0x00020000);
    const int r2 = lane >> 3, k = lane & 7, bb = r2 & 3;
    float xv = bflo(x[bb * DM + 8 * rho + k]);
    int hpi = WS_HP + (8 * w + r2) * REC + (8 * rho + k) * 16, opi = w < 7 ? WS_OP + (8 * w + r2) * REC + (8 * rho + k) * 16 : OOB;
    int ho = w == 0 ? WS_H + rho * 1024 + ((k & 3) + 4 * (lane >> 5)) * 128 + bb * 16 + (k >> 2) * 8 : OOB;
    int oo = (w == 0 && k == 0 && lane < 32) ? (bb * DM + 8 * rho) * 2 : OOB;
    int sw = L_P + (((((g & 1) << 1) | (g >> 1)) * 8 + k) * 8 + w) * 4, sr = L_P + (bb * 8 + k) * 32;
    __builtin_amdgcn_raw_buffer_store_b128(v4u{0u, 0u, 0u, 0u}, ro, oo, 0, 0);
    PIN(xv); PIN(ho); PIN(oo); PIN(sw); PIN(sr); PIN(hpi); PIN(opi);
    if (w == 0) wait_ready(rs, lane, tlo, thi);
    BARRIER();
#define PARTIAL_SUM(PV, AREA, RES) do { \
      *(LAS float*)(L + sw + (AREA)) = sum8_scatter4(__uint_as_float(PV[0]), __uint_as_float(PV[1]), __uint_as_float(PV[2]), __uint_as_float(PV[3])); \
      BARRIER(); \
      const f32x4 rc = *(LAS f32x4*)(L + sr + (AREA)) + *(LAS f32x4*)(L + sr + (AREA) + 16); \
      RES = (rc[0] + rc[2]) + (rc[1] + rc[3]); } while (0)
    float av;
    const v4u pv = poll_slot(srd_ws, hpi);
    PARTIAL_SUM(pv, 0, av);
    const float hval = rb(xv + rb(av));
    {
      const unsigned e0 = nosent(pk2(dpp<0xA0>(hval), dpp<0xF5>(hval)));
      st8_sc1_asm(v2u{dppu<0x00>(e0), dppu<0xAA>(e0)}, ho, srd_ws);
    }
    SCHED();
    st8_sc1_asm(v2u{tlo, thi}, (c == 192 && w == 1 && lane == 0) ? WS_EPOCH : OOB, srd_ws);
    const v4u pw = poll_slot(srd_ws, opi);
    PARTIAL_SUM(pw, 1024, av);
    const float ov = hval + rb(av);
    const unsigned dw = pk2(ov, dpp<SHL1>(ov)), dw2 = dppu<SHL2>(dw);
    __builtin_amdgcn_raw_buffer_store_b128(v4u{dw, dw2, dppu<SHL4>(dw), dppu<SHL4>(dw2)}, ro, oo, 0, 0);
  }
}

}

extern "C" void kernel_launch(void* const* d_in, const int* in_sizes, int n_in, void* d_out, int out_size, void* d_ws, size_t ws_size,
                              hipStream_t stream) {
  (void)in_sizes; (void)n_in; (void)out_size;
  if (ws_size < (size_t)WS_END) return;
  fused_block<<<256, NTHR, 0, stream>>>((const u16*)d_in[0], (const u16*)d_in[1], (const u16*)d_in[2], (const u16*)d_in[3], (const u16*)d_in[4],
                                        (const u16*)d_in[6], (const u16*)d_in[7], (const u16*)d_in[8], (u16*)d_out, (unsigned char*)d_ws);
}
```
